```python
import torch

def _softmax(x, axis=-1):
    return torch.softmax(x, dim=axis)
def _gelu(x):
    return torch.nn.functional.gelu(x, approximate="tanh")
def _silu(x):
    return torch.nn.functional.silu(x)
def _sigmoid(x):
    return torch.sigmoid(x)
def _layernorm(x, axis=-1, eps=1e-5):
    m = x.mean(dim=axis, keepdim=True)
    v = x.var(dim=axis, keepdim=True, unbiased=False)
    return (x - m) / torch.sqrt(v + eps)
def _scatter_add(tgt, idx, src, axis=0):
    return tgt.clone().index_add_(axis, idx, src)
def _segment_sum(src, seg, n):
    out = torch.zeros((n,) + src.shape[1:], dtype=src.dtype)
    return out.index_add_(0, seg, src)
def _pad(x, pw):
    flat = tuple(v for pair in reversed(pw) for v in pair)
    return torch.nn.functional.pad(x, flat)

kernel_name = 'sequential_preLN__ssm_conv_h3__mixer_s_mlp__prompt'

def setup_inputs():

    _w = (lambda a: (lambda t: t.to(torch.bfloat16) if t.is_floating_point() else t)(torch.as_tensor(a)))
    def _A_setup():
        B, S, D = 1, 2048, 576
        ks = 4
        return {
            "q": _w((lambda *s: torch.randn(*s, dtype=torch.bfloat16) * 0.05)(B, S, D)),
            "k": _w((lambda *s: torch.randn(*s, dtype=torch.bfloat16) * 0.4)(B, S, D)),
            "v": _w((lambda *s: torch.randn(*s, dtype=torch.bfloat16) * 0.4)(B, S, D)),
            "shift_k": _w((lambda *s: torch.randn(*s, dtype=torch.bfloat16) * 0.4)(D, ks) * 0.5),
            "ssm_k": _w((lambda *s: torch.randn(*s, dtype=torch.bfloat16) * 0.4)(D, S) * (1.0 / S ** 0.5)),
            "ssm_D": _w((lambda *s: torch.randn(*s, dtype=torch.bfloat16) * 0.4)(D)),
        }
    _A_in = _A_setup()

    def _B_setup():
        B, S, D, F = 1, 2048, 576, 4608
        return {
            "x":  _w((lambda *s: torch.randn(*s, dtype=torch.bfloat16) * 0.05)(B, S, D)),
            "w1": _w((lambda *s: torch.randn(*s, dtype=torch.bfloat16) * 3.2)(D, D) * (D ** -0.5)),
            "w2": _w((lambda *s: torch.randn(*s, dtype=torch.bfloat16) * 3.2)(D, D) * (D ** -0.5)),
            "w3": _w((lambda *s: torch.randn(*s, dtype=torch.bfloat16) * 3.2)(D, F) * (D ** -0.5)),
            "w4": _w((lambda *s: torch.randn(*s, dtype=torch.bfloat16) * 1.6)(F, D) * (F ** -0.5)),
        }
    _B_in = _B_setup()

    return {
        "A_q": _A_in["q"],
        "A_k": _A_in["k"],
        "A_v": _A_in["v"],
        "A_shift_k": _A_in["shift_k"],
        "A_ssm_k": _A_in["ssm_k"],
        "A_ssm_D": _A_in["ssm_D"],
        "B_x": _B_in["x"],
        "B_w1": _B_in["w1"],
        "B_w2": _B_in["w2"],
        "B_w3": _B_in["w3"],
        "B_w4": _B_in["w4"],
    }

def reference(A_q, A_k, A_v, A_shift_k, A_ssm_k, A_ssm_D, B_x, B_w1, B_w2, B_w3, B_w4):
    def _A_ref(q, k, v, shift_k, ssm_k, ssm_D):
        B, S, D = q.shape
        ks = shift_k.shape[-1]
        k_pad = _pad(k, ((0, 0), (ks - 1, 0), (0, 0)))
        k_shift = torch.zeros_like(k)
        for t in range(ks):
            k_shift = k_shift + k_pad[:, t:t + S, :] * shift_k[:, ks - 1 - t][None, None, :]
        kv = k_shift * v
        L = 2 * S
        kv_pad = _pad(kv, ((0, 0), (0, S), (0, 0)))
        kf_pad = _pad(ssm_k, ((0, 0), (0, S)))
        kv_f = (lambda x, axes=(-2,-1): torch.fft.rfft2(x.float(), dim=axes))(kv_pad[:, None, :, :], axes=(1, 2))
        kf_f = (lambda x, axes=(-2,-1): torch.fft.rfft2(x.float(), dim=axes))(kf_pad[:, None, :], axes=(1, 2))
        kf_f = (lambda x, *d: x.permute(*d))(kf_f, 1, 2, 0)
        y_f = kv_f * kf_f[None, :, :, :]
        y = (lambda x, s, axes=(-2,-1): torch.fft.irfft2(x, s=s, dim=axes).to(torch.bfloat16))(y_f, (1, L), axes=(1, 2))[:, 0, :S, :]
        y = y + kv * ssm_D[None, None, :]
        return q * y

    def _B_ref(x, w1, w2, w3, w4):
        B, S, D = x.shape
        h = _gelu(_layernorm(x, axis=-1) @ w1)
        c = D // 4
        g0 = _pad(h[:, :, 0*c:1*c], ((0, 0), (1, 0), (0, 0)))[:, :S, :]
        g1 = _pad(h[:, :, 1*c:2*c], ((0, 0), (0, 1), (0, 0)))[:, 1:, :]
        g2 = _pad(h[:, :, 2*c:3*c], ((0, 0), (2, 0), (0, 0)))[:, :S, :]
        g3 = _pad(h[:, :, 3*c:   ], ((0, 0), (0, 2), (0, 0)))[:, 2:, :]
        hs = torch.concatenate([g0, g1, g2, g3], axis=-1) @ w2
        x1 = x + hs
        h2 = _gelu(_layernorm(x1, axis=-1) @ w3) @ w4
        return x1 + h2

    x = A_q
    h = x + _A_ref(_layernorm(x, axis=-1), A_k, A_v, A_shift_k, A_ssm_k, A_ssm_D)
    h2 = h + _B_ref(_layernorm(h, axis=-1), B_w1, B_w2, B_w3, B_w4)
    return h2
```

```cpp
#include <hip/hip_runtime.h>
#include <hip/hip_bf16.h>
#include <stdint.h>

constexpr int S = 2048, D = 576, F = 4608;
typedef unsigned short u16;

__device__ __forceinline__ float bf2f(u16 x) { return __uint_as_float(((unsigned)x) << 16); }
__device__ __forceinline__ u16 f2bf(float f) { unsigned u = __float_as_uint(f); u += 0x7fffu + ((u >> 16) & 1u); return (u16)(u >> 16); }
__device__ __forceinline__ float rb(float f) { return bf2f(f2bf(f)); }
__device__ __forceinline__ float gelu_f(float x) {
    float u = 0.7978845608028654f * (x + 0.044715f * x * x * x);
    return 0.5f * x * (1.0f + tanhf(u));
}

__global__ void k_kv(const u16* k, const u16* v, const u16* sk, u16* kv) {
    int i = blockIdx.x * 256 + threadIdx.x;
    if (i >= S * D) return;
    int s = i / D, d = i % D;
    float acc = 0.f;
    for (int t = 0; t < 4; ++t) {
        int ss = s + t - 3;
        float kk = ss >= 0 ? bf2f(k[ss * D + d]) : 0.f;
        acc = rb(acc + rb(kk * bf2f(sk[d * 4 + 3 - t])));
    }
    kv[i] = f2bf(acc * bf2f(v[i]));
}

__global__ void k_conv(const u16* kv, const u16* ssmk, u16* y) {
    int i = blockIdx.x * 256 + threadIdx.x;
    if (i >= S * D) return;
    int s = i / D, d = i % D;
    float acc = 0.f;
    for (int j = 0; j <= s; ++j) acc += bf2f(kv[(s - j) * D + d]) * bf2f(ssmk[d * S + j]);
    y[i] = f2bf(acc);
}

__device__ float block_sum(float v, float* sm) {
    for (int o = 32; o > 0; o >>= 1) v += __shfl_xor(v, o);
    return v;
}
__device__ void ln_row(float* x) {
    float s = 0.f;
    for (int j = 0; j < 9; ++j) s += x[j];
    s = block_sum(s, nullptr);
    float mean = s * (1.0f / D);
    float q = 0.f;
    for (int j = 0; j < 9; ++j) q += (x[j] - mean) * (x[j] - mean);
    q = block_sum(q, nullptr);
    float m = rb(mean), var = rb(q * (1.0f / D));
    float den = rb(sqrtf(rb(var + 1e-5f)));
    for (int j = 0; j < 9; ++j) x[j] = rb(rb(x[j] - m) / den);
}

__global__ void k_row(const u16* x, const u16* kv, const u16* y, const u16* ssmD, u16* h, u16* xb, u16* xn) {
    int s = blockIdx.x, l = threadIdx.x;
    float xv[9], q[9];
    for (int j = 0; j < 9; ++j) { xv[j] = bf2f(x[s * D + l + 64 * j]); q[j] = xv[j]; }
    ln_row(q);
    float hv[9];
    for (int j = 0; j < 9; ++j) {
        int d = l + 64 * j;
        float yy = rb(bf2f(y[s * D + d]) + rb(bf2f(kv[s * D + d]) * bf2f(ssmD[d])));
        hv[j] = rb(xv[j] + rb(q[j] * yy));
        h[s * D + d] = f2bf(hv[j]);
    }
    ln_row(hv);
    for (int j = 0; j < 9; ++j) xb[s * D + l + 64 * j] = f2bf(hv[j]);
    ln_row(hv);
    for (int j = 0; j < 9; ++j) xn[s * D + l + 64 * j] = f2bf(hv[j]);
}

__global__ void k_ln(const u16* x, u16* o) {
    int s = blockIdx.x, l = threadIdx.x;
    float xv[9];
    for (int j = 0; j < 9; ++j) xv[j] = bf2f(x[s * D + l + 64 * j]);
    ln_row(xv);
    for (int j = 0; j < 9; ++j) o[s * D + l + 64 * j] = f2bf(xv[j]);
}

template <int MODE>
__global__ void k_gemm(const u16* A, const u16* B, u16* C, const u16* r1, const u16* r2, int M, int N, int K) {
    int n = blockIdx.x * 64 + (threadIdx.x & 63);
    int m = blockIdx.y * 4 + (threadIdx.x >> 6);
    float acc = 0.f;
    for (int k = 0; k < K; ++k) {
        int mm = m;
        if (MODE == 1) { int g = k / 144; mm = m - (g == 0 ? 1 : g == 1 ? -1 : g == 2 ? 2 : -2); }
        float a = (mm >= 0 && mm < M) ? bf2f(A[(size_t)mm * K + k]) : 0.f;
        acc += a * bf2f(B[(size_t)k * N + n]);
    }
    float c = rb(acc);
    if (MODE == 0) C[(size_t)m * N + n] = f2bf(gelu_f(c));
    if (MODE == 1) C[(size_t)m * N + n] = f2bf(bf2f(r1[(size_t)m * N + n]) + c);
    if (MODE == 3) C[(size_t)m * N + n] = f2bf(bf2f(r2[(size_t)m * N + n]) + rb(bf2f(r1[(size_t)m * N + n]) + c));
}

extern "C" void kernel_launch(void* const* d_in, const int* in_sizes, int n_in,
                              void* d_out, int out_size, void* d_ws, size_t ws_size,
                              hipStream_t stream) {
    const u16* q = (const u16*)d_in[0];
    const u16* k = (const u16*)d_in[1];
    const u16* v = (const u16*)d_in[2];
    const u16* sk = (const u16*)d_in[3];
    const u16* ssmk = (const u16*)d_in[4];
    const u16* ssmD = (const u16*)d_in[5];
    const u16* w1 = (const u16*)d_in[7];
    const u16* w2 = (const u16*)d_in[8];
    const u16* w3 = (const u16*)d_in[9];
    const u16* w4 = (const u16*)d_in[10];
    u16* ws = (u16*)d_ws;
    size_t SD = (size_t)S * D;
    u16 *kv = ws, *y = ws + SD, *h = ws + 2 * SD, *xb = ws + 3 * SD, *xn = ws + 4 * SD, *g = ws + 5 * SD, *x1 = ws + 6 * SD, *x1n = ws + 7 * SD, *H = ws + 8 * SD;
    k_kv<<<(S * D + 255) / 256, 256, 0, stream>>>(k, v, sk, kv);
    k_conv<<<(S * D + 255) / 256, 256, 0, stream>>>(kv, ssmk, y);
    k_row<<<S, 64, 0, stream>>>(q, kv, y, ssmD, h, xb, xn);
    k_gemm<0><<<dim3(D / 64, S / 4), 256, 0, stream>>>(xn, w1, g, nullptr, nullptr, S, D, D);
    k_gemm<1><<<dim3(D / 64, S / 4), 256, 0, stream>>>(g, w2, x1, xb, nullptr, S, D, D);
    k_ln<<<S, 64, 0, stream>>>(x1, x1n);
    k_gemm<0><<<dim3(F / 64, S / 4), 256, 0, stream>>>(x1n, w3, H, nullptr, nullptr, S, F, D);
    k_gemm<3><<<dim3(D / 64, S / 4), 256, 0, stream>>>(H, w4, (u16*)d_out, x1, h, S, D, F);
}
```

```cpp
#include <hip/hip_runtime.h>
#include <stdint.h>
#include <utility>
#define LAS __attribute__((address_space(3)))
typedef short s16x4 __attribute__((ext_vector_type(4)));
typedef __bf16 bf16x8 __attribute__((ext_vector_type(8)));
typedef __bf16 bf16x2 __attribute__((ext_vector_type(2)));
typedef float f32x16 __attribute__((ext_vector_type(16)));
typedef float f32x4 __attribute__((ext_vector_type(4)));
typedef float f32x2 __attribute__((ext_vector_type(2)));
typedef unsigned u32x4 __attribute__((ext_vector_type(4)));
typedef unsigned u32x2 __attribute__((ext_vector_type(2)));
typedef unsigned long long u64;
typedef unsigned short u16;
typedef __amdgpu_buffer_rsrc_t rsrc_t;
#define DEVI __device__ __forceinline__
#define AI __attribute__((always_inline))
#define SB() __builtin_amdgcn_sched_barrier(0)
#define OPAQ(x) asm volatile("" : "+v"(x))

DEVI unsigned pk2(float a, float b) { f32x2 p = {a, b}; return __builtin_bit_cast(unsigned, __builtin_convertvector(p, bf16x2)); }
DEVI float bflo(unsigned x) { return __uint_as_float(x << 16); }
DEVI float bfhi(unsigned x) { return __uint_as_float(x & 0xffff0000u); }
DEVI bf16x8 asb(u32x4 x) { return __builtin_bit_cast(bf16x8, x); }
constexpr float GC0 = -2.0f * 0.7978845608028654f * 1.4426950408889634f, GC1 = GC0 * 0.044715f;
DEVI float gelu(float x) {
    float e = __builtin_amdgcn_exp2f(x * __builtin_fmaf(x * x, GC1, GC0));
    return x * __builtin_amdgcn_rcpf(1.0f + e);
}
DEVI void dma16(unsigned ldsaddr, const void* sbase, unsigned voff) {
    asm volatile("s_mov_b32 m0, %0\n\ts_nop 0\n\tglobal_load_lds_dwordx4 %1, %2" :: "s"(ldsaddr), "v"(voff), "s"(sbase) : "memory");
}
template <int N> DEVI void waitvm() { asm volatile("s_waitcnt vmcnt(%0)" :: "i"(N) : "memory"); }
DEVI void bar() { asm volatile("s_waitcnt lgkmcnt(0)\n\ts_barrier" ::: "memory"); }
DEVI bf16x8 trfrag(unsigned a0, unsigned a1) {
    s16x4 a = __builtin_amdgcn_ds_read_tr16_b64_v4i16((LAS s16x4*)(uintptr_t)a0);
    s16x4 b = __builtin_amdgcn_ds_read_tr16_b64_v4i16((LAS s16x4*)(uintptr_t)a1);
    return __builtin_bit_cast(bf16x8, __builtin_shufflevector(a, b, 0, 1, 2, 3, 4, 5, 6, 7));
}
template <int B, class F, int... I> DEVI void sfor_impl(F&& f, std::integer_sequence<int, I...>) { (f(std::integral_constant<int, B + I>{}), ...); }
template <int B, int E, class F> DEVI void sfor(F&& f) { sfor_impl<B>(f, std::make_integer_sequence<int, E - B>{}); }
template <class T> DEVI T ldsr(unsigned a) { return *(LAS T*)(uintptr_t)a; }
template <class T> DEVI void ldsw(unsigned a, T v) { *(LAS T*)(uintptr_t)a = v; }
DEVI rsrc_t mkrs(const void* p, unsigned bytes) { return __builtin_amdgcn_make_buffer_rsrc((void*)p, 0, bytes, 0x00020000); }
template <int AUX> DEVI u32x4 bl16(rsrc_t r, unsigned vo, unsigned so) { return __builtin_amdgcn_raw_buffer_load_b128(r, vo, so, AUX); }
template <int AUX> DEVI u32x2 bl8(rsrc_t r, unsigned vo, unsigned so) { return __builtin_amdgcn_raw_buffer_load_b64(r, vo, so, AUX); }
template <int AUX> DEVI unsigned bl4(rsrc_t r, unsigned vo, unsigned so) { return __builtin_amdgcn_raw_buffer_load_b32(r, vo, so, AUX); }
template <int AUX> DEVI void bs16(u32x4 v, rsrc_t r, unsigned vo, unsigned so) { __builtin_amdgcn_raw_buffer_store_b128(v, r, vo + so, 0, AUX); }
template <int AUX> DEVI void bs8(u32x2 v, rsrc_t r, unsigned vo, unsigned so) { __builtin_amdgcn_raw_buffer_store_b64(v, r, vo, so, AUX); }
template <int AUX> DEVI void bs4(unsigned v, rsrc_t r, unsigned vo, unsigned so) { __builtin_amdgcn_raw_buffer_store_b32(v, r, vo, so, AUX); }
DEVI void swap16(unsigned& a, unsigned& b) { auto r = __builtin_amdgcn_permlane16_swap(a, b, false, false); a = r[0]; b = r[1]; }
DEVI void swap32(unsigned& a, unsigned& b) { auto r = __builtin_amdgcn_permlane32_swap(a, b, false, false); a = r[0]; b = r[1]; }
DEVI void swap16f(float& a, float& b) { unsigned x = __float_as_uint(a), y = __float_as_uint(b); swap16(x, y); a = __uint_as_float(x); b = __uint_as_float(y); }
DEVI void swap32f(float& a, float& b) { unsigned x = __float_as_uint(a), y = __float_as_uint(b); swap32(x, y); a = __uint_as_float(x); b = __uint_as_float(y); }
template <int CTRL> DEVI float dppf(float x) { return __uint_as_float(__builtin_amdgcn_update_dpp(0u, __float_as_uint(x), CTRL, 0xf, 0xf, false)); }
DEVI float rowsum16(float x) { x += dppf<0x128>(x); x += dppf<0x124>(x); x += dppf<0x122>(x); x += dppf<0x121>(x); return x; }
DEVI f32x4 mfma16(bf16x8 a, bf16x8 b, f32x4 c) { return __builtin_amdgcn_mfma_f32_16x16x32_bf16(a, b, c, 0, 0, 0); }

constexpr unsigned CLSZ = 64 * 36 * 1024;
constexpr unsigned GTS = 2056 * 32;
constexpr unsigned O_FLAG = 0, O_YB = 65536, O_XT = O_YB + CLSZ, O_XS = O_XT + CLSZ, O_W1T = O_XS + 16384, O_W2T = O_W1T + 663552,
                   O_CSP = O_W2T + 663552, O_W2M = O_CSP + 41472, O_GB = O_W2M + 2304, O_HT = O_GB + 36 * GTS, O_HS = O_HT + CLSZ,
                   O_XB = O_HS + 65536, O_SQ = O_XB + CLSZ, O_R = O_SQ + 36 * 2048 * 4, O_HB = O_R + 2 * CLSZ, O_PB = O_HB + 128 * 144 * 1024,
                   O_PX = O_PB + 64 * 32 * 4 * 1024, O_END = O_PX + 64 * 4 * 16 * 1024, O_DBG = O_END;
constexpr float EPS = 1e-5f, RD576 = 1.0f / 576.0f;
#define XA 0
#define STAMP(kid, i) do {} while (0)
#define CSTAMP(kid, i) do {} while (0)

constexpr int K1_KC = 0, K1_UR = 33792, K1_P = K1_UR + 18432, K1_LDS = K1_P + 32768;
__global__ __launch_bounds__(512, 2) void k1(const char* __restrict__ xq, const char* __restrict__ kk, const char* __restrict__ vv, const char* __restrict__ shk,
                                             const char* __restrict__ ssk, const u16* __restrict__ ssd, const char* __restrict__ w1, const char* __restrict__ w2, char* __restrict__ ws) {
    extern __shared__ __attribute__((aligned(16))) char smem[];
    const unsigned lb = (unsigned)(uintptr_t)(LAS char*)smem;
    const int wg = blockIdx.x, xcd = wg & 7, loc = wg >> 3, tid = threadIdx.x, lane = tid & 63;
    const int w = __builtin_amdgcn_readfirstlane(tid >> 6);
    const rsrc_t wr = mkrs(ws, O_END);
    STAMP(0, 0);
    if (loc < 18) {
        const int cw = 18 * xcd + loc;
        const int kch = tid >> 7, tau = tid & 127;
        const rsrc_t sr = mkrs(ssk, 576 * 4096);
        const unsigned so = (4 * cw + kch) * 4096 + tau * 32;
        u32x4 K0 = bl16<0>(sr, so, 0), K1 = bl16<0>(sr, so + 16, 0);
        unsigned K8 = bl4<0>(sr, so + 32, 0);
        const float dv = bflo(ssd[4 * cw + kch]);
        const rsrc_t kr = mkrs(kk, 2048 * 1152), vr = mkrs(vv, 2048 * 1152);
        u32x2 ks[4], vs[4];
        auto strip = [&](int g) AI { SB(); ks[g] = bl8<0>(kr, tid * 1152 + cw * 8, g * 512 * 1152); SB(); vs[g] = bl8<0>(vr, tid * 1152 + cw * 8, g * 512 * 1152); SB(); };
        strip(0);
        asm volatile("s_barrier" ::: "memory");
        strip(1);
        u64 halo[4][3];
#pragma unroll
        for (int g = 0; g < 4; ++g)
#pragma unroll
            for (int i = 0; i < 3; ++i) { int r = 512 * g + 64 * w - 1 - i; u64 t = *(const u64*)(kk + (size_t)(r < 0 ? 0 : r) * 1152 + cw * 8); halo[g][i] = r < 0 ? 0ull : t; }
        const u32x4 sa = *(const u32x4*)(shk + cw * 32), sb = *(const u32x4*)(shk + cw * 32 + 16);
        if (tid < 120) ldsw<u32x4>(lb + K1_UR + (tid / 30) * 4608 + 4096 + (tid % 30) * 16, u32x4{0, 0, 0, 0});
        if (tau == 0) K0[0] = (K0[0] & 0xffff0000u) | (pk2(bflo(K0[0]) + dv, 0.f) & 0xffffu);
        {
            const unsigned c0 = lb + K1_KC + kch * 8448 + 32 + tau * 32, c1 = c0 + 4224;
            ldsw<u32x4>(c0, K0); ldsw<u32x4>(c0 + 16, K1);
            ldsw<u32x4>(c1, u32x4{__builtin_amdgcn_alignbit(K0[1], K0[0], 16), __builtin_amdgcn_alignbit(K0[2], K0[1], 16), __builtin_amdgcn_alignbit(K0[3], K0[2], 16), __builtin_amdgcn_alignbit(K1[0], K0[3], 16)});
            ldsw<u32x4>(c1 + 16, u32x4{__builtin_amdgcn_alignbit(K1[1], K1[0], 16), __builtin_amdgcn_alignbit(K1[2], K1[1], 16), __builtin_amdgcn_alignbit(K1[3], K1[2], 16), __builtin_amdgcn_alignbit(K8, K1[3], 16)});
            if (tau == 0) {
                ldsw<u32x4>(c0 - 32, u32x4{0, 0, 0, 0}); ldsw<u32x4>(c0 - 16, u32x4{0, 0, 0, 0});
                ldsw<u32x4>(c1 - 32, u32x4{0, 0, 0, 0}); ldsw<u32x4>(c1 - 16, u32x4{0, 0, 0, K0[0] << 16});
            }
        }
        SB();
        bar();
        strip(2);
        STAMP(0, 1);
        const int ch = w & 3, hf = w >> 2, li = lane & 15, q = lane >> 4;
        u32x4 A[4][8];
        {
            const int r = (li + 1) & 1;
            unsigned aa = lb + K1_KC + ch * 8448 + r * 4224 + (128 * hf + li + 25 - 8 * q - r) * 2;
            OPAQ(aa);
#pragma unroll
            for (int c0 = 0; c0 < 4; ++c0)
#pragma unroll
                for (int c1 = 0; c1 < 8; ++c1) {
                    const unsigned a = aa + c1 * 512 + c0 * 64;
                    A[c0][c1] = u32x4{ldsr<unsigned>(a), ldsr<unsigned>(a + 4), ldsr<unsigned>(a + 8), ldsr<unsigned>(a + 12)};
                }
        }
        strip(3);
        f32x4 acc[8];
#pragma unroll
        for (int t = 0; t < 8; ++t) acc[t] = f32x4{0, 0, 0, 0};
        unsigned ba = lb + K1_UR + ch * 4608 + (2056 - 1792 - 16 * li + 128 * hf - 8 * q) * 2;
        OPAQ(ba);
        const unsigned ua = lb + K1_UR + (511 - tid) * 2;
        const unsigned pw = lb + K1_P + (hf * 4 + ch) * 2048 + li * 64 + q * 16;
        const unsigned pr = lb + K1_P + ((tid >> 4) * 32 + (tid & 15)) * 4;
        auto output = [&](int g) AI {
            if (tid < 256) {
                float y[2][4];
#pragma unroll
                for (int c = 0; c < 4; ++c)
#pragma unroll
                    for (int m = 0; m < 2; ++m) y[m][c] = ldsr<float>(pr + (g & 1) * 16384 + c * 2048 + m * 64) + ldsr<float>(pr + (g & 1) * 16384 + 8192 + c * 2048 + m * 64);
                bs16<0>(u32x4{pk2(y[0][0], y[0][1]), pk2(y[0][2], y[0][3]), pk2(y[1][0], y[1][1]), pk2(y[1][2], y[1][3])}, wr, (tid >> 4) * 36864 + (tid & 15) * 16, O_YB + g * 16 * 36864 + cw * 256);
            }
        };
        sfor<0, 4>([&](auto G) AI {
            constexpr int g = G;
            unsigned kx[4][2];
            kx[0][0] = ks[g][0]; kx[0][1] = ks[g][1];
#pragma unroll
            for (int i = 1; i < 4; ++i) {
                kx[i][0] = __builtin_amdgcn_update_dpp((unsigned)halo[g][i - 1], kx[i - 1][0], 0x138, 0xf, 0xf, false);
                kx[i][1] = __builtin_amdgcn_update_dpp((unsigned)(halo[g][i - 1] >> 32), kx[i - 1][1], 0x138, 0xf, 0xf, false);
            }
            float s0 = bflo(kx[0][0]) * bflo(sa[0]) + bflo(kx[1][0]) * bfhi(sa[0]) + bflo(kx[2][0]) * bflo(sa[1]) + bflo(kx[3][0]) * bfhi(sa[1]);
            float s1 = bfhi(kx[0][0]) * bflo(sa[2]) + bfhi(kx[1][0]) * bfhi(sa[2]) + bfhi(kx[2][0]) * bflo(sa[3]) + bfhi(kx[3][0]) * bfhi(sa[3]);
            float s2 = bflo(kx[0][1]) * bflo(sb[0]) + bflo(kx[1][1]) * bfhi(sb[0]) + bflo(kx[2][1]) * bflo(sb[1]) + bflo(kx[3][1]) * bfhi(sb[1]);
            float s3 = bfhi(kx[0][1]) * bflo(sb[2]) + bfhi(kx[1][1]) * bfhi(sb[2]) + bfhi(kx[2][1]) * bflo(sb[3]) + bfhi(kx[3][1]) * bfhi(sb[3]);
            const unsigned p01 = pk2(s0 * bflo(vs[g][0]), s1 * bfhi(vs[g][0])), p23 = pk2(s2 * bflo(vs[g][1]), s3 * bfhi(vs[g][1]));
            const unsigned u = ua + (3 - g) * 1024;
            ldsw<u16>(u, (u16)p01); ldsw<u16>(u + 4608, (u16)(p01 >> 16)); ldsw<u16>(u + 9216, (u16)p23); ldsw<u16>(u + 13824, (u16)(p23 >> 16));
            SB();
            bar();
            u32x4 B[8];
#pragma unroll
            for (int i = 0; i < 8; ++i) B[i] = ldsr<u32x4>(ba + (7 - 2 * g - (i >> 2)) * 512 + (i & 3) * 64);
            SB();
            if constexpr (g >= 1) output(g - 1);
            SB();
#pragma unroll
            for (int i = 0; i < 8; ++i) {
                const int b = 2 * g + (i >> 2);
#pragma unroll
                for (int c1 = 0; c1 + b < 8; ++c1) acc[b + c1] = mfma16(asb(A[i & 3][c1]), asb(B[i]), acc[b + c1]);
            }
            SB();
            ldsw<f32x4>(pw + (g & 1) * 16384, acc[2 * g]); ldsw<f32x4>(pw + (g & 1) * 16384 + 1024, acc[2 * g + 1]);
            STAMP(0, 2 + g);
        });
        bar();
        output(3);
    } else {
        const int u = (loc - 18) * 8 + w;
        if (u < 32 || (u >= 73 && u < 82)) {
            const bool isx = u < 32;
            const int rl = lane >> 4, c = lane & 15;
            const int rb = 32 * (xcd >> 2) + 4 * (u >> 2) + (xcd & 3), rho = 4 * (u & 3) + rl;
            const int r0 = isx ? 32 * rb + rho : 8 * (9 * xcd + u - 73) + rl, r1 = r0 + (isx ? 16 : 4);
            const rsrc_t xr = mkrs(isx ? xq : w2, isx ? 2048 * 1152 : 576 * 1152);
            u32x2 a[9], b[9];
#pragma unroll
            for (int m = 0; m < 9; ++m) { a[m] = bl8<0>(xr, r0 * 1152 + c * 8, m * 128); b[m] = bl8<0>(xr, r1 * 1152 + c * 8, m * 128); }
            float sa = 0, sb = 0;
#pragma unroll
            for (int m = 0; m < 9; ++m) {
                sa += (bflo(a[m][0]) + bfhi(a[m][0])) + (bflo(a[m][1]) + bfhi(a[m][1]));
                sb += (bflo(b[m][0]) + bfhi(b[m][0])) + (bflo(b[m][1]) + bfhi(b[m][1]));
                if (isx) bs16<0>(u32x4{a[m][0], a[m][1], b[m][0], b[m][1]}, wr, c * 256 + rho * 16, O_XT + rb * 36864 + m * 4096);
            }
            const float ma = rowsum16(sa) * RD576, mb = rowsum16(sb) * RD576;
            if (isx) {
                float qa = 0, qb = 0;
#pragma unroll
                for (int m = 0; m < 9; ++m)
#pragma unroll
                    for (int i = 0; i < 2; ++i) {
                        float t;
                        t = bflo(a[m][i]) - ma; qa += t * t; t = bfhi(a[m][i]) - ma; qa += t * t;
                        t = bflo(b[m][i]) - mb; qb += t * t; t = bfhi(b[m][i]) - mb; qb += t * t;
                    }
                const float ra = 1.0f / sqrtf(rowsum16(qa) * RD576 + EPS), rbb = 1.0f / sqrtf(rowsum16(qb) * RD576 + EPS);
                if (c == 0) { bs8<0>(u32x2{__float_as_uint(ra), __float_as_uint(-ma * ra)}, wr, r0 * 8, O_XS); bs8<0>(u32x2{__float_as_uint(rbb), __float_as_uint(-mb * rbb)}, wr, r1 * 8, O_XS); }
            } else if (c < 2) {
                const int k = c ? r1 : r0; const float m = c ? mb : ma;
                const int p = (k >> 3) * 32 + (k & 7) * 2;
                const unsigned hi = pk2(m, 0.f) & 0xffffu, lo = pk2(m - bflo(hi), 0.f) & 0xffffu;
                *(u16*)(ws + O_W2M + p) = (u16)hi; *(u16*)(ws + O_W2M + 16 + p) = (u16)lo;
            }
        } else if (u < 73) {
            const int g = 41 * xcd + u - 32;
            if (g < 324) {
                const int mat = g >= 162, r = g - 162 * mat, ks = r / 9, cg = r - 9 * ks;
                const rsrc_t mr = mkrs(mat ? w2 : w1, 576 * 1152);
                u32x4 t[4];
#pragma unroll
                for (int i = 0; i < 4; ++i) t[i] = bl16<0>(mr, (lane >> 3) * 1152 + (lane & 7) * 16, (32 * ks + 8 * i) * 1152 + cg * 128);
                const unsigned lw = lb + w * 4096;
#pragma unroll
                for (int i = 0; i < 4; ++i) ldsw<u32x4>(lw + i * 1024 + lane * 16, t[i]);
                const unsigned ta = lw + ((mat ? 8 : 4) * (lane >> 4) + ((lane & 15) >> 2)) * 128 + (lane & 3) * 8, tb = ta + (mat ? 512 : 2048);
                const u32x4 ones = {0x3f803f80u, 0x3f803f80u, 0x3f803f80u, 0x3f803f80u};
#pragma unroll
                for (int tt = 0; tt < 4; ++tt) {
                    const bf16x8 f = trfrag(ta + tt * 32, tb + tt * 32);
                    bs16<0>(__builtin_bit_cast(u32x4, f), wr, lane * 16, (mat ? O_W2T : O_W1T) + ((4 * cg + tt) * 18 + ks) * 1024);
                    if (!mat) {
                        const f32x4 cs = mfma16(f, asb(ones), f32x4{0, 0, 0, 0});
                        if ((lane & 15) == 0) bs16<0>(__builtin_bit_cast(u32x4, cs), wr, (lane >> 4) * 16, O_CSP + (ks * 576 + 64 * cg + 16 * tt) * 4);
                    }
                }
            }
        } else if (u == 82) {
            const int l = 9 * xcd + (lane >> 3);
            bs16<0>(u32x4{0, 0, 0, 0}, wr, (l >> 1) * GTS + (l & 1) * (2052 * 32) + (lane & 7) * 16, O_GB);
            const int l2 = 9 * xcd + 8;
            if (lane < 8) bs16<0>(u32x4{0, 0, 0, 0}, wr, (l2 >> 1) * GTS + (l2 & 1) * (2052 * 32) + lane * 16, O_GB);
        }
    }
    STAMP(0, 7);
}

#define IDS \
    extern __shared__ __attribute__((aligned(16))) char smem[]; \
    const unsigned lb = (unsigned)(uintptr_t)(LAS char*)smem; \
    const int wg = blockIdx.x, xcd = wg & 7, loc = wg >> 3, lane = threadIdx.x & 63; \
    const int kap = xcd & 3, rt = 8 * (xcd >> 2) + (loc >> 2), nt = loc & 3, rb = 4 * rt + kap, fc = 4 * kap + nt; \
    const int w = __builtin_amdgcn_readfirstlane(threadIdx.x >> 6); \
    const int lj = lane & 15, lq = lane >> 4; \
    const rsrc_t wr = mkrs(ws, O_END); \
    (void)lb; (void)rb; (void)fc; (void)lj; (void)lq;

constexpr int B_HF = 0, B_ST = 36864, B_LDS = B_ST + 2048;
template <bool EX> DEVI void gemm_small(const u32x4* W, const u32x4* We, unsigned fa, unsigned fe, f32x4* acc, f32x4& ae) {
    constexpr int PF = 3;
    u32x4 b[PF + 1][3];
    sfor<0, 18 + PF>([&](auto K) AI {
        constexpr int k = K, m = k - PF;
        if constexpr (k < 18) {
            b[k % (PF + 1)][0] = ldsr<u32x4>(fa + k * 2048); b[k % (PF + 1)][1] = ldsr<u32x4>(fa + k * 2048 + 1024);
            if constexpr (EX) b[k % (PF + 1)][2] = ldsr<u32x4>(fe + k * 2048);
        }
        if constexpr (m >= 0) {
            const f32x4 z = {0, 0, 0, 0};
            acc[0] = mfma16(asb(W[m]), asb(b[m % (PF + 1)][0]), m ? acc[0] : z); acc[1] = mfma16(asb(W[m]), asb(b[m % (PF + 1)][1]), m ? acc[1] : z);
            if constexpr (EX) ae = mfma16(asb(We[m]), asb(b[m % (PF + 1)][2]), m ? ae : z);
        }
        SB();
    });
}
__global__ __launch_bounds__(512, 2) void kB(char* __restrict__ ws) {
    IDS
    STAMP(1, 0);
    auto body = [&](auto ROLE) AI {
    constexpr int role = ROLE, NC = role < 2 ? 5 : 4;
    u32x4 Y[5], X[5], W[18], We[18];
    const unsigned l16 = lane * 16;
    int nu[5];
#pragma unroll
    for (int m = 0; m < 4; ++m) nu[m] = ((nt + m) & 3) * 8 + w;
    nu[4] = 32 + (w & 3);
    const u32x2 xs0 = bl8<0>(wr, lj * 8, O_XS + rb * 256), xs1 = bl8<0>(wr, lj * 8, O_XS + rb * 256 + 128);
#pragma unroll
    for (int m = 0; m < NC; ++m) { Y[m] = bl16<0>(wr, l16, O_YB + (rb * 36 + nu[m]) * 1024); X[m] = bl16<0>(wr, l16, O_XT + (rb * 36 + nu[m]) * 1024); SB(); }
#pragma unroll
    for (int k = 0; k < 18; ++k) {
        W[k] = bl16<0>(wr, l16, O_W1T + ((8 * nt + w) * 18 + k) * 1024);
        if constexpr (role == 0) We[k] = bl16<0>(wr, l16, O_W1T + ((32 + nt) * 18 + k) * 1024);
        SB();
    }
    u32x4 ca = bl16<0>(wr, lj * 2304 + lq * 16, O_CSP + (8 * nt + w) * 64), cb = bl16<0>(wr, (16 + (lj & 1)) * 2304 + lq * 16, O_CSP + (8 * nt + w) * 64), cea, ceb;
    if constexpr (role == 0) { cea = bl16<0>(wr, lj * 2304 + lq * 16, O_CSP + (32 + nt) * 64); ceb = bl16<0>(wr, (16 + (lj & 1)) * 2304 + lq * 16, O_CSP + (32 + nt) * 64); }
    SB();
    float s[2] = {0, 0}, sq[2] = {0, 0};
    const float rx[2] = {__uint_as_float(xs0[0]), __uint_as_float(xs1[0])}, mx[2] = {__uint_as_float(xs0[1]), __uint_as_float(xs1[1])};
    u32x4 hown, hext;
#pragma unroll
    for (int m = 0; m < NC; ++m) {
        u32x4 hp;
#pragma unroll
        for (int i = 0; i < 4; ++i) {
            const int mt = i >> 1;
            const float x0 = bflo(X[m][i]), x1 = bfhi(X[m][i]);
            const float h0 = __builtin_fmaf(__builtin_fmaf(x0, rx[mt], mx[mt]), bflo(Y[m][i]), x0), h1 = __builtin_fmaf(__builtin_fmaf(x1, rx[mt], mx[mt]), bfhi(Y[m][i]), x1);
            s[mt] += h0 + h1; sq[mt] += h0 * h0 + h1 * h1;
            hp[i] = pk2(h0, h1);
        }
        const unsigned a = lb + B_HF + (nu[m] >> 1) * 2048 + (nu[m] & 1) * 8 + l16;
        ldsw<u32x2>(a, u32x2{hp[0], hp[1]}); ldsw<u32x2>(a + 1024, u32x2{hp[2], hp[3]});
        if (m == 0) hown = hp;
        if (m == 4) hext = hp;
        SB();
    }
    {
        swap16f(s[0], s[1]); swap16f(sq[0], sq[1]);
        float t = s[0] + s[1], u = sq[0] + sq[1];
        swap32f(t, u);
        ldsw<float>(lb + B_ST + w * 256 + lane * 4, t + u);
    }
    bar();
    STAMP(1, 1);
    f32x4 acc[2], ae;
    unsigned ha = lb + B_HF + l16, he = ha + (w & 1) * 1024; OPAQ(ha); OPAQ(he);
    float tot = 0;
#pragma unroll
    for (int i = 0; i < 8; ++i) tot += ldsr<float>(lb + B_ST + i * 256 + lane * 4);
    float su = tot, qu = tot; swap32f(su, qu);
    const float mean = su * RD576, var = fmaxf(qu * RD576 - mean * mean, 0.f);
    const float r1 = 1.0f / sqrtf(var + EPS), r12 = r1 / sqrtf(var * r1 * r1 + EPS);
    float mm[2] = {mean, mean}, rr[2] = {r12, r12};
    swap16f(mm[0], mm[1]); swap16f(rr[0], rr[1]);
    SB();
    gemm_small<role == 0>(W, We, ha, he, acc, ae);
    float cs[4];
#pragma unroll
    for (int r = 0; r < 4; ++r) cs[r] = rowsum16(__uint_as_float(ca[r]) + (lj < 2 ? __uint_as_float(cb[r]) : 0.f));
    STAMP(1, 2);
    unsigned o[2][2];
#pragma unroll
    for (int mt = 0; mt < 2; ++mt) {
        float g[4];
#pragma unroll
        for (int r = 0; r < 4; ++r) g[r] = gelu((acc[mt][r] - mm[mt] * cs[r]) * rr[mt]);
        o[mt][0] = pk2(g[0], g[1]); o[mt][1] = pk2(g[2], g[3]);
    }
    swap16(o[0][0], o[1][0]); swap16(o[0][1], o[1][1]);
    bs16<XA>(u32x4{o[0][0], o[0][1], o[1][0], o[1][1]}, wr, (lane & 31) * 32 + (lane >> 5) * 16, O_GB + (8 * nt + w) * GTS + (4 + 32 * rb) * 32);
    if constexpr (role == 0) {
        float g[4];
        const float me = w ? mm[1] : mm[0], re = w ? rr[1] : rr[0];
#pragma unroll
        for (int r = 0; r < 4; ++r) g[r] = gelu((ae[r] - me * rowsum16(__uint_as_float(cea[r]) + (lj < 2 ? __uint_as_float(ceb[r]) : 0.f))) * re);
        bs8<XA>(u32x2{pk2(g[0], g[1]), pk2(g[2], g[3])}, wr, lj * 32 + lq * 8, O_GB + (32 + nt) * GTS + (4 + 32 * rb + 16 * w) * 32);
    }
    bs16<0>(hown, wr, l16, O_HT + (rb * 36 + nu[0]) * 1024);
    if constexpr (role < 2) { if (w == nt) bs16<0>(hext, wr, l16, O_HT + (rb * 36 + nu[4]) * 1024); }
    if (w == 7 && lane < 32) bs8<0>(u32x2{__float_as_uint(r1), __float_as_uint(-mean * r1)}, wr, lane * 8, O_HS + (nt * 2048 + rb * 32) * 8);
    };
    if (w < 2) body(std::integral_constant<int, 0>{}); else if (w < 4) body(std::integral_constant<int, 1>{}); else body(std::integral_constant<int, 2>{});
    STAMP(1, 7);
}

constexpr int C_GF = 0, C_MN = 36864, C_LDS = C_MN + 256;
__global__ __launch_bounds__(512, 2) void kC(char* __restrict__ ws) {
    IDS
    STAMP(2, 0);
    auto body = [&](auto ROLE) AI {
    constexpr int role = ROLE, NC = role < 2 ? 5 : 4;
    const unsigned l16 = lane * 16;
    u32x4 G[5], W[18], We[18];
    auto gaddr = [&](int nu) AI { const int gr = nu / 9, sh = gr == 0 ? 1 : gr == 1 ? -1 : gr == 2 ? 2 : -2; return O_GB + nu * GTS + (4 + 32 * rb - sh) * 32; };
#pragma unroll
    for (int m = 0; m < NC; ++m) { G[m] = bl16<XA>(wr, l16, gaddr(8 * m + w)); SB(); }
    if constexpr (role == 1) {
        if (lj < 2) {
#pragma unroll
            for (int k = 0; k < 18; ++k) We[k] = bl16<0>(wr, lq * 32 + lj * 16 + k * 128, O_W2M);
        }
    }
#pragma unroll
    for (int k = 0; k < 18; ++k) {
        W[k] = bl16<0>(wr, l16, O_W2T + ((8 * nt + w) * 18 + k) * 1024);
        if constexpr (role == 0) We[k] = bl16<0>(wr, l16, O_W2T + ((32 + nt) * 18 + k) * 1024);
        SB();
    }
    const u32x4 hp = bl16<0>(wr, l16, O_HT + (rb * 36 + 8 * nt + w) * 1024);
    u32x2 hpe;
    if constexpr (role == 0) hpe = bl8<0>(wr, l16 + (w & 1) * 8, O_HT + (rb * 36 + 32 + nt) * 1024);
    const u32x2 hs0 = bl8<0>(wr, lj * 8, O_HS + (nt * 2048 + rb * 32) * 8), hs1 = bl8<0>(wr, lj * 8, O_HS + (nt * 2048 + rb * 32 + 16) * 8);
    SB();
    const unsigned gw = lb + C_GF + (w >> 1) * 2048 + (lane >> 5) * 1024 + (((lane >> 1) & 15) + 16 * (2 * (w & 1) + (lane & 1))) * 16;
#pragma unroll
    for (int m = 0; m < NC; ++m) ldsw<u32x4>(gw + m * 8192, G[m]);
    bar();
    STAMP(2, 1);
    f32x4 acc[2], ae;
    unsigned ga = lb + C_GF + l16, ge = ga + (w & 1) * 1024; OPAQ(ga); OPAQ(ge);
    gemm_small<(role < 2)>(W, We, ga, ge, acc, ae);
    if constexpr (role == 1) { if (lane < 16) ldsw<float>(lb + C_MN + (w & 1) * 64 + lane * 4, ae[0] + ae[1]); }
    bar();
    STAMP(2, 2);
    const float mn[2] = {ldsr<float>(lb + C_MN + lj * 4), ldsr<float>(lb + C_MN + 64 + lj * 4)};
    const float r1[2] = {__uint_as_float(hs0[0]), __uint_as_float(hs1[0])}, m1[2] = {__uint_as_float(hs0[1]), __uint_as_float(hs1[1])};
    unsigned o[2][2]; float sq[2]; f32x4 res[2];
#pragma unroll
    for (int mt = 0; mt < 2; ++mt) {
        const float h[4] = {bflo(hp[2 * mt]), bfhi(hp[2 * mt]), bflo(hp[2 * mt + 1]), bfhi(hp[2 * mt + 1])};
        float xc[4];
#pragma unroll
        for (int r = 0; r < 4; ++r) { const float x1 = __builtin_fmaf(h[r], r1[mt], m1[mt]) + acc[mt][r]; xc[r] = x1 - mn[mt]; res[mt][r] = h[r] + x1; }
        sq[mt] = (xc[0] * xc[0] + xc[1] * xc[1]) + (xc[2] * xc[2] + xc[3] * xc[3]);
        o[mt][0] = pk2(xc[0], xc[1]); o[mt][1] = pk2(xc[2], xc[3]);
    }
    swap16(o[0][0], o[1][0]); swap16(o[0][1], o[1][1]);
    bs16<XA>(u32x4{o[0][0], o[0][1], o[1][0], o[1][1]}, wr, l16, O_XB + (rb * 36 + 8 * nt + w) * 1024);
    {
        swap16f(sq[0], sq[1]);
        float t = sq[0] + sq[1], u = t; swap32f(t, u);
        if (lane < 32) bs4<XA>(__float_as_uint(t + u), wr, lane * 4, O_SQ + ((8 * nt + w) * 2048 + rb * 32) * 4);
    }
    if constexpr (role == 0) {
        const float h[4] = {bflo(hpe[0]), bfhi(hpe[0]), bflo(hpe[1]), bfhi(hpe[1])};
        const float re = w ? r1[1] : r1[0], me = w ? m1[1] : m1[0], ne = w ? mn[1] : mn[0];
        float xc[4]; f32x4 rese;
#pragma unroll
        for (int r = 0; r < 4; ++r) { const float x1 = __builtin_fmaf(h[r], re, me) + ae[r]; xc[r] = x1 - ne; rese[r] = h[r] + x1; }
        bs8<XA>(u32x2{pk2(xc[0], xc[1]), pk2(xc[2], xc[3])}, wr, (lq >> 1) * 512 + lj * 16 + (lq & 1) * 8, O_XB + (rb * 36 + 32 + nt) * 1024 + w * 256);
        float t = (xc[0] * xc[0] + xc[1] * xc[1]) + (xc[2] * xc[2] + xc[3] * xc[3]), u = t;
        swap16f(t, u); t += u; u = t; swap32f(t, u);
        if (lane < 16) bs4<XA>(__float_as_uint(t + u), wr, lane * 4, O_SQ + ((32 + nt) * 2048 + rb * 32 + 16 * w) * 4);
        bs16<0>(__builtin_bit_cast(u32x4, rese), wr, l16, O_R + ((rb * 36 + 32 + nt) * 2 + w) * 1024);
    }
#pragma unroll
    for (int mt = 0; mt < 2; ++mt) bs16<0>(__builtin_bit_cast(u32x4, res[mt]), wr, l16, O_R + ((rb * 36 + 8 * nt + w) * 2 + mt) * 1024);
    };
    if (w < 2) body(std::integral_constant<int, 0>{}); else if (w < 4) body(std::integral_constant<int, 1>{}); else body(std::integral_constant<int, 2>{});
    STAMP(2, 7);
}

constexpr int D_SLOT = 36864, D_EX = 3 * D_SLOT, D_LDS = D_EX + 32768;
constexpr int D_RD = 3, D_BP = 14;
__global__ __launch_bounds__(512, 2) void kD(const char* __restrict__ w3, char* __restrict__ ws) {
    IDS
    STAMP(3, 0);
    const int wm = w & 3, hh = lane >> 5;
    unsigned ex = lb + D_EX + wm * 8192 + lane * 16, exl = lb + D_SLOT + wm * 4096 + lane * 16;
    OPAQ(ex); OPAQ(exl);
    if (w < 4) {
        u32x4 X[36];
#pragma unroll
        for (int i = 0; i < 36; ++i) X[i] = bl16<XA>(wr, lane * 16, O_XB + ((4 * rt + wm) * 36 + i) * 1024);
        const int r = 8 * hh + (lj >> 2);
        unsigned ta[3][2], ts;
#pragma unroll
        for (int s = 0; s < 3; ++s)
#pragma unroll
            for (int ab = 0; ab < 2; ++ab) { ta[s][ab] = lb + s * D_SLOT + r * 128 + ((ab ^ ((r >> 1) & 1)) * 64) + (lq & 1) * 32 + (lane & 3) * 8; OPAQ(ta[s][ab]); }
        ts = lb + 2 * D_SLOT + r * 64 + (lq & 1) * 32 + (lane & 3) * 8; OPAQ(ts);
        bar();
        STAMP(3, 1); CSTAMP(3, 3);
        __builtin_amdgcn_s_setprio(2);
        f32x16 acc[4]; bf16x8 A[D_RD + 1];
        sfor<0, 10>([&](auto S) AI { sfor<0, 36>([&](auto K) AI {
            constexpr int sr = S, kr = K, gr = 36 * sr + kr, g = gr - D_RD;
            if constexpr (gr < 324 + D_RD) {
                if constexpr (kr == D_BP) asm volatile("s_waitcnt lgkmcnt(%0)\n\ts_barrier" :: "i"((sr >= 2 && !(sr & 1)) ? 0 : 15) : "memory");
                if constexpr (gr < 288) { const unsigned a = ta[sr % 3][kr & 1] + (kr >> 1) * 2048; A[gr % (D_RD + 1)] = trfrag(a, a + 512); }
                else if constexpr (gr < 324) { const unsigned a = ts + kr * 1024; A[gr % (D_RD + 1)] = trfrag(a, a + 256); }
                if constexpr (g >= 0) {
                    constexpr int s = g / 36, i = g % 36, ks = s < 8 ? 18 * (s & 1) + (i >> 1) : i, ai = s < 8 ? ((s >> 1) & 1) * 2 + (i & 1) : 0;
                    f32x16 z = {};
                    acc[ai] = __builtin_amdgcn_mfma_f32_32x32x16_bf16(A[g % (D_RD + 1)], asb(X[ks]), ks == 0 ? z : acc[ai], 0, 0, 0);
                }
                if constexpr (sr >= 2 && sr < 9 && !(sr & 1) && kr >= D_BP - 8 && kr < D_BP) {
                    constexpr int q = kr - (D_BP - 8), ai = (((sr >> 1) - 1) & 1) * 2 + (q >> 2), qq = q & 3;
                    ldsw<f32x4>(ex + q * 1024, f32x4{acc[ai][4 * qq], acc[ai][4 * qq + 1], acc[ai][4 * qq + 2], acc[ai][4 * qq + 3]});
                }
                SB();
            }
        }); });
#pragma unroll
        for (int q = 0; q < 4; ++q) ldsw<f32x4>(exl + q * 1024, f32x4{acc[0][4 * q], acc[0][4 * q + 1], acc[0][4 * q + 2], acc[0][4 * q + 3]});
        __builtin_amdgcn_s_setprio(0);
        bar();
        STAMP(3, 2); CSTAMP(3, 4);
    } else {
        const int a9 = 9 * (fc >> 1), odd = fc & 1;
        const char* wp = w3 + (a9 + 5 * odd) * 128 + (size_t)wm * 8 * 9216;
        const char* wsg = w3 + (a9 + 4) * 128 + odd * 64 + (size_t)wm * 16 * 9216;
        const unsigned pvo = (lane >> 3) * 9216 + (((lane & 7) ^ (((lane >> 4) & 1) << 2)) * 16), svo = (lane >> 2) * 9216 + (lane & 3) * 16;
        auto issue = [&](int s, int i) AI {
            if (s < 8) dma16(lb + (s % 3) * D_SLOT + wm * 1024 + i * 4096, wp + (s >> 1) * 128 + (size_t)((s & 1) * 288 + i * 32) * 9216, pvo);
            else dma16(lb + (s % 3) * D_SLOT + wm * 1024 + i * 4096, wsg + (size_t)i * 64 * 9216, svo);
        };
#pragma unroll
        for (int i = 0; i < 9; ++i) issue(0, i);
        float sq = 0;
        {
            unsigned t[18];
#pragma unroll
            for (int i = 0; i < 18; ++i) t[i] = bl4<XA>(wr, (lane & 31) * 4 + hh * 18 * 8192, O_SQ + i * 8192 + (4 * rt + wm) * 128);
#pragma unroll
            for (int i = 0; i < 18; ++i) sq += __uint_as_float(t[i]);
        }
        float u = sq; swap32f(sq, u);
        const float sg = sqrtf((sq + u) * RD576 + EPS), rstd = 1.0f / sg;
        const float k0 = GC0 * rstd, k1 = GC1 * rstd * rstd * rstd;
        waitvm<0>();
#pragma unroll
        for (int i = 0; i < 9; ++i) issue(1, i);
        const unsigned hvo = (8 * rt + 2 * wm + (lq & 1)) * 144 * 1024 + (lj + 16 * hh) * 16;
        const unsigned hp = O_HB + (18 * (fc >> 1) + 10 * odd) * 1024, hsg = O_HB + (18 * (fc >> 1) + 8 + odd) * 1024;
        bar();
        STAMP(3, 1); CSTAMP(3, 3);
        f32x4 v[8];
        auto act = [&](f32x4* p, unsigned so) AI {
#pragma unroll
            for (int h = 0; h < 2; ++h) {
                float t[8];
#pragma unroll
                for (int j = 0; j < 8; ++j) {
                    const float x = p[2 * h + (j >> 2)][j & 3];
                    const float e = __builtin_amdgcn_exp2f(x * __builtin_fmaf(x * x, k1, k0));
                    t[j] = x * __builtin_amdgcn_rcpf(__builtin_fmaf(e, sg, sg));
                    OPAQ(t[j]);
                }
                bs16<XA>(u32x4{pk2(t[0], t[1]), pk2(t[2], t[3]), pk2(t[4], t[5]), pk2(t[6], t[7])}, wr, hvo + h * 512, so);
                SB();
            }
        };
        sfor<0, 9>([&](auto S) AI {
            constexpr int s = S;
            if constexpr (s + 1 < 9) waitvm<(s >= 3) ? 2 : 0>();
            bar();
            if constexpr (s >= 2 && !(s & 1)) {
#pragma unroll
                for (int q = 0; q < 8; ++q) v[q] = ldsr<f32x4>(ex + q * 1024);
            }
            if constexpr (s + 2 < 9) {
#pragma unroll
                for (int i = 0; i < 9; ++i) issue(s + 2, i);
            }
            SB();
            if constexpr (s >= 2) act(&v[(s & 1) * 4], hp + (s - 2) * 1024);
        });
        act(&v[4], hp + 7 * 1024);
        bar();
        STAMP(3, 2); CSTAMP(3, 4);
#pragma unroll
        for (int q = 0; q < 4; ++q) v[q] = ldsr<f32x4>(exl + q * 1024);
        act(&v[0], hsg);
    }
    STAMP(3, 7);
}

constexpr int E_STG = 32768, E_X = 3 * E_STG, E_LDS = E_X + 36864;
constexpr int E_RD = 3, E_BP = 6, E_HD = 8;
__global__ __launch_bounds__(512, 2) void kE(const char* __restrict__ w4, char* __restrict__ ws) {
    IDS
    STAMP(4, 0);
    const int wm = w & 3;
    int kb[4];
#pragma unroll
    for (int i = 0; i < 4; ++i) kb[i] = 36 * kap + 9 * ((nt + i) & 3);
    if (w < 4) {
        u32x4 H[E_HD][2];
        const unsigned hvo = lane * 16 + (8 * rt + 2 * wm) * 144 * 1024;
        auto ldH = [&](int j, int mt) AI { H[j % E_HD][mt] = bl16<XA>(wr, hvo + mt * 144 * 1024 + (j % 9 < 4 ? (j % 9) * 1024 : 0), O_HB + (kb[j / 9] + (j % 9 < 4 ? 0 : j % 9)) * 1024); };
#pragma unroll
        for (int j = 0; j < E_HD - 1; ++j) { ldH(j, 0); ldH(j, 1); }
        const int r = 16 * (lq >> 1) + 4 * (lq & 1) + (lj >> 2);
        unsigned am[2][8], ax[4];
#pragma unroll
        for (int c = 0; c < 8; ++c) { am[0][c] = lb + r * 256 + ((c ^ (r & 7)) << 5) + (lane & 3) * 8; am[1][c] = am[0][c] + 2 * E_STG; OPAQ(am[0][c]); OPAQ(am[1][c]); }
#pragma unroll
        for (int c = 0; c < 4; ++c) { ax[c] = lb + E_X + r * 128 + ((c ^ ((r >> 1) & 3)) << 5) + (lane & 3) * 8; OPAQ(ax[c]); }
        f32x4 acc[12][2];
        bar();
        STAMP(4, 1); CSTAMP(4, 3);
        __builtin_amdgcn_s_setprio(2);
        constexpr int NF = 9 * 12 + 27 * 8;
        bf16x8 A[E_RD + 1];
        sfor<0, NF + E_RD>([&](auto F) AI {
            constexpr int fr = F, fm = fr - E_RD;
            if constexpr (fm >= 0) {
                constexpr int j = fm < 108 ? fm / 12 : 9 + (fm - 108) / 8, c = fm < 108 ? fm % 12 : (fm - 108) % 8;
                acc[c][0] = mfma16(A[fm % (E_RD + 1)], asb(H[j % E_HD][0]), j == 0 ? f32x4{0, 0, 0, 0} : acc[c][0]);
            }
            s16x4 t0, t1;
            constexpr int jr = fr < 108 ? fr / 12 : 9 + (fr - 108) / 8, cr = fr < 108 ? fr % 12 : (fr - 108) % 8, st = jr >> 2;
            const unsigned a = cr < 8 ? am[st % 3 == 2][cr & 7] + (st % 3 == 1 ? E_STG : 0) + (jr & 3) * 8192 : ax[cr & 3] + jr * 4096;
            if constexpr (fr < NF) {
                if constexpr ((jr & 3) == 0 && cr == E_BP) asm volatile("s_barrier" ::: "memory");
                t0 = __builtin_amdgcn_ds_read_tr16_b64_v4i16((LAS s16x4*)(uintptr_t)a);
            }
            SB();
            if constexpr (fm >= 0) {
                constexpr int j = fm < 108 ? fm / 12 : 9 + (fm - 108) / 8, c = fm < 108 ? fm % 12 : (fm - 108) % 8;
                acc[c][1] = mfma16(A[fm % (E_RD + 1)], asb(H[j % E_HD][1]), j == 0 ? f32x4{0, 0, 0, 0} : acc[c][1]);
                if constexpr (j + E_HD - 1 < 36 && (c == 1 || c == 3)) ldH(j + E_HD - 1, c >> 1);
            }
            if constexpr (fr < NF) {
                t1 = __builtin_amdgcn_ds_read_tr16_b64_v4i16((LAS s16x4*)(uintptr_t)(a + (cr < 8 ? 2048 : 1024)));
                A[fr % (E_RD + 1)] = __builtin_bit_cast(bf16x8, __builtin_shufflevector(t0, t1, 0, 1, 2, 3, 4, 5, 6, 7));
            }
            SB();
        });
        __builtin_amdgcn_s_setprio(0);
        STAMP(4, 2); CSTAMP(4, 4);
#pragma unroll
        for (int c = 0; c < 12; ++c) {
            const u32x4 o = {pk2(acc[c][0][0], acc[c][0][1]), pk2(acc[c][0][2], acc[c][0][3]), pk2(acc[c][1][0], acc[c][1][1]), pk2(acc[c][1][2], acc[c][1][3])};
            if (c < 8) bs16<XA>(o, wr, lane * 16, O_PB + (((4 * rt + wm) * 32 + 8 * nt + c) * 4 + kap) * 1024);
            else bs16<XA>(o, wr, lane * 16, O_PX + (((4 * rt + wm) * 4 + c - 8) * 16 + fc) * 1024);
        }
    } else {
        const int drow = 4 * wm + lq;
        const unsigned dvo = drow * 1152 + ((lj ^ (2 * (drow & 7))) << 4);
        const char* w4m = w4 + nt * 256;
        auto dmaM = [&](int j, int i) AI { dma16(lb + ((j >> 2) % 3) * E_STG + (j & 3) * 8192 + (wm + 4 * i) * 1024, w4m + (size_t)((kb[j / 9] + j % 9) * 32 + 16 * i) * 1152, dvo); };
        const int xrow = lane >> 3;
        const unsigned xvo = xrow * 1152 + (((lane & 7) ^ (2 * ((xrow >> 1) & 3))) << 4);
        const char* w4x = w4 + 1024 + (size_t)(kb[0] * 32 + wm * 8) * 1152;
#pragma unroll
        for (int j = 0; j < 4; ++j) { dmaM(j, 0); dmaM(j, 1); dma16(lb + E_X + (wm + 4 * j) * 1024, w4x + (size_t)j * 32 * 1152, xvo); }
#pragma unroll
        for (int j = 4; j < 8; ++j) { dmaM(j, 0); dmaM(j, 1); dma16(lb + E_X + (wm + 4 * j) * 1024, w4x + (size_t)j * 32 * 1152, xvo); }
        dma16(lb + E_X + (wm + 32) * 1024, w4x + (size_t)8 * 32 * 1152, xvo);
        waitvm<13>();
        bar();
        STAMP(4, 1); CSTAMP(4, 3);
        sfor<0, 9>([&](auto S) AI {
            constexpr int s = S;
            waitvm<0>();
            bar();
            if constexpr (s + 2 < 9) {
#pragma unroll
                for (int j = 0; j < 4; ++j) { dmaM(4 * (s + 2) + j, 0); dmaM(4 * (s + 2) + j, 1); }
            }
        });
    }
    STAMP(4, 7);
}

__global__ __launch_bounds__(512, 2) void kF(char* __restrict__ ws, char* __restrict__ out) {
    IDS
    STAMP(5, 0);
    const unsigned l16 = lane * 16;
    const rsrc_t orr = mkrs(out, 2048 * 1152);
    u32x4 P[4], R[2], Q[16], Re[2];
#pragma unroll
    for (int i = 0; i < 4; ++i) P[i] = bl16<XA>(wr, l16, O_PB + ((rb * 32 + 8 * nt + w) * 4 + i) * 1024);
#pragma unroll
    for (int i = 0; i < 2; ++i) R[i] = bl16<0>(wr, l16, O_R + ((rb * 36 + 8 * nt + w) * 2 + i) * 1024);
    if (w == 7) {
#pragma unroll
        for (int i = 0; i < 16; ++i) Q[i] = bl16<XA>(wr, l16, O_PX + ((rb * 4 + nt) * 16 + i) * 1024);
#pragma unroll
        for (int i = 0; i < 2; ++i) Re[i] = bl16<0>(wr, l16, O_R + ((rb * 36 + 32 + nt) * 2 + i) * 1024);
    }
    auto fin = [&](u32x4* p, int n, u32x4* r, int tile) AI {
#pragma unroll
        for (int mt = 0; mt < 2; ++mt) {
            float a[4] = {0, 0, 0, 0};
            for (int i = 0; i < n; ++i) { a[0] += bflo(p[i][2 * mt]); a[1] += bfhi(p[i][2 * mt]); a[2] += bflo(p[i][2 * mt + 1]); a[3] += bfhi(p[i][2 * mt + 1]); }
            bs8<0>(u32x2{pk2(a[0] + __uint_as_float(r[mt][0]), a[1] + __uint_as_float(r[mt][1])), pk2(a[2] + __uint_as_float(r[mt][2]), a[3] + __uint_as_float(r[mt][3]))},
                   orr, lj * 1152 + lq * 8, (32 * rb + 16 * mt) * 1152 + tile * 32);
        }
    };
    fin(P, 4, R, 8 * nt + w);
    if (w == 7) fin(Q, 16, Re, 32 + nt);
    STAMP(5, 7);
}

extern "C" void kernel_launch(void* const* d_in, const int* in_sizes, int n_in, void* d_out, int out_size, void* d_ws, size_t ws_size, hipStream_t stream) {
    char* ws = (char*)d_ws;
    (void)hipFuncSetAttribute((const void*)k1, hipFuncAttributeMaxDynamicSharedMemorySize, K1_LDS);
    (void)hipFuncSetAttribute((const void*)kD, hipFuncAttributeMaxDynamicSharedMemorySize, D_LDS);
    (void)hipFuncSetAttribute((const void*)kE, hipFuncAttributeMaxDynamicSharedMemorySize, E_LDS);
    k1<<<256, 512, K1_LDS, stream>>>((const char*)d_in[0], (const char*)d_in[1], (const char*)d_in[2], (const char*)d_in[3], (const char*)d_in[4], (const u16*)d_in[5],
                                     (const char*)d_in[7], (const char*)d_in[8], ws);
    kB<<<256, 512, B_LDS, stream>>>(ws);
    kC<<<256, 512, C_LDS, stream>>>(ws);
    kD<<<256, 512, D_LDS, stream>>>((const char*)d_in[9], ws);
    kE<<<256, 512, E_LDS, stream>>>((const char*)d_in[10], ws);
    kF<<<256, 512, 0, stream>>>(ws, (char*)d_out);
}
```

```cpp
#include <hip/hip_runtime.h>
#include <stdint.h>
#include <utility>
#define LAS __attribute__((address_space(3)))
typedef short s16x4 __attribute__((ext_vector_type(4)));
typedef __bf16 bf16x8 __attribute__((ext_vector_type(8)));
typedef __bf16 bf16x2 __attribute__((ext_vector_type(2)));
typedef float f32x16 __attribute__((ext_vector_type(16)));
typedef float f32x4 __attribute__((ext_vector_type(4)));
typedef float f32x2 __attribute__((ext_vector_type(2)));
typedef unsigned u32x4 __attribute__((ext_vector_type(4)));
typedef unsigned u32x2 __attribute__((ext_vector_type(2)));
typedef unsigned long long u64;
typedef unsigned short u16;
typedef __amdgpu_buffer_rsrc_t rsrc_t;
#define DEVI __device__ __forceinline__
#define AI __attribute__((always_inline))
#define SB() __builtin_amdgcn_sched_barrier(0)
#define OPAQ(x) asm volatile("" : "+v"(x))

DEVI unsigned pk2(float a, float b) { f32x2 p = {a, b}; return __builtin_bit_cast(unsigned, __builtin_convertvector(p, bf16x2)); }
DEVI float bflo(unsigned x) { return __uint_as_float(x << 16); }
DEVI float bfhi(unsigned x) { return __uint_as_float(x & 0xffff0000u); }
DEVI bf16x8 asb(u32x4 x) { return __builtin_bit_cast(bf16x8, x); }
constexpr float GC0 = -2.0f * 0.7978845608028654f * 1.4426950408889634f, GC1 = GC0 * 0.044715f;
DEVI float gelu(float x) {
    float e = __builtin_amdgcn_exp2f(x * __builtin_fmaf(x * x, GC1, GC0));
    return x * __builtin_amdgcn_rcpf(1.0f + e);
}
DEVI void dma16(unsigned ldsaddr, const void* sbase, unsigned voff) {
    asm volatile("s_mov_b32 m0, %0\n\ts_nop 0\n\tglobal_load_lds_dwordx4 %1, %2" :: "s"(ldsaddr), "v"(voff), "s"(sbase) : "memory");
}
DEVI void dma16h(unsigned ldsaddr, const void* sbase, unsigned voff) {
    asm volatile("s_mov_b32 m0, %0\n\ts_mov_b32 exec_hi, 0\n\tglobal_load_lds_dwordx4 %1, %2\n\ts_mov_b32 exec_hi, -1" :: "s"(ldsaddr), "v"(voff), "s"(sbase) : "memory");
}
template <int N> DEVI void waitvm() { asm volatile("s_waitcnt vmcnt(%0)" :: "i"(N) : "memory"); }
DEVI void bar() { asm volatile("s_waitcnt lgkmcnt(0)\n\ts_barrier" ::: "memory"); }
DEVI bf16x8 trfrag(unsigned a0, unsigned a1) {
    s16x4 a = __builtin_amdgcn_ds_read_tr16_b64_v4i16((LAS s16x4*)(uintptr_t)a0);
    s16x4 b = __builtin_amdgcn_ds_read_tr16_b64_v4i16((LAS s16x4*)(uintptr_t)a1);
    return __builtin_bit_cast(bf16x8, __builtin_shufflevector(a, b, 0, 1, 2, 3, 4, 5, 6, 7));
}
template <int B, class F, int... I> DEVI void sfor_impl(F&& f, std::integer_sequence<int, I...>) { (f(std::integral_constant<int, B + I>{}), ...); }
template <int B, int E, class F> DEVI void sfor(F&& f) { sfor_impl<B>(f, std::make_integer_sequence<int, E - B>{}); }
template <class T> DEVI T ldsr(unsigned a) { return *(LAS T*)(uintptr_t)a; }
template <class T> DEVI void ldsw(unsigned a, T v) { *(LAS T*)(uintptr_t)a = v; }
DEVI rsrc_t mkrs(const void* p, unsigned bytes) { return __builtin_amdgcn_make_buffer_rsrc((void*)p, 0, bytes, 0x00020000); }
template <int AUX> DEVI u32x4 bl16(rsrc_t r, unsigned vo, unsigned so) { return __builtin_amdgcn_raw_buffer_load_b128(r, vo, so, AUX); }
template <int AUX> DEVI u32x2 bl8(rsrc_t r, unsigned vo, unsigned so) { return __builtin_amdgcn_raw_buffer_load_b64(r, vo, so, AUX); }
template <int AUX> DEVI unsigned bl4(rsrc_t r, unsigned vo, unsigned so) { return __builtin_amdgcn_raw_buffer_load_b32(r, vo, so, AUX); }
template <int AUX> DEVI void bs16(u32x4 v, rsrc_t r, unsigned vo, unsigned so) { __builtin_amdgcn_raw_buffer_store_b128(v, r, vo + so, 0, AUX); }
template <int AUX> DEVI void bs8(u32x2 v, rsrc_t r, unsigned vo, unsigned so) { __builtin_amdgcn_raw_buffer_store_b64(v, r, vo, so, AUX); }
template <int AUX> DEVI void bs4(unsigned v, rsrc_t r, unsigned vo, unsigned so) { __builtin_amdgcn_raw_buffer_store_b32(v, r, vo, so, AUX); }
DEVI void swap16(unsigned& a, unsigned& b) { auto r = __builtin_amdgcn_permlane16_swap(a, b, false, false); a = r[0]; b = r[1]; }
DEVI void swap32(unsigned& a, unsigned& b) { auto r = __builtin_amdgcn_permlane32_swap(a, b, false, false); a = r[0]; b = r[1]; }
DEVI void swap16f(float& a, float& b) { unsigned x = __float_as_uint(a), y = __float_as_uint(b); swap16(x, y); a = __uint_as_float(x); b = __uint_as_float(y); }
DEVI void swap32f(float& a, float& b) { unsigned x = __float_as_uint(a), y = __float_as_uint(b); swap32(x, y); a = __uint_as_float(x); b = __uint_as_float(y); }
template <int CTRL> DEVI float dppf(float x) { return __uint_as_float(__builtin_amdgcn_update_dpp(0u, __float_as_uint(x), CTRL, 0xf, 0xf, false)); }
DEVI float rowsum16(float x) { x += dppf<0x128>(x); x += dppf<0x124>(x); x += dppf<0x122>(x); x += dppf<0x121>(x); return x; }
DEVI f32x4 mfma16(bf16x8 a, bf16x8 b, f32x4 c) { return __builtin_amdgcn_mfma_f32_16x16x32_bf16(a, b, c, 0, 0, 0); }

constexpr unsigned CLSZ = 64 * 36 * 1024;
constexpr unsigned GTS = 2056 * 32;
constexpr unsigned O_FLAG = 0, O_YB = 65536, O_XT = O_YB + CLSZ, O_XS = O_XT + CLSZ, O_W1T = O_XS + 16384, O_W2T = O_W1T + 663552,
                   O_CSP = O_W2T + 663552, O_W2M = O_CSP + 41472, O_GB = O_W2M + 2304, O_HT = O_GB + 36 * GTS, O_HS = O_HT + CLSZ,
                   O_XB = O_HS + 65536, O_SQ = O_XB + CLSZ, O_R = O_SQ + 36 * 2048 * 4, O_HB = O_R + 2 * CLSZ, O_PB = O_HB + 128 * 144 * 1024,
                   O_PX = O_PB + 64 * 32 * 4 * 1024, O_END = O_PX + 64 * 4 * 16 * 1024, O_DBG = O_END;
constexpr float EPS = 1e-5f, RD576 = 1.0f / 576.0f;
#define XA 16
#define TDECL
#define TS(i) do {} while (0)
#define TDUMP(kid, n) do {} while (0)
#define STAMP(kid, i) do {} while (0)
#define CSTAMP(kid, i) do {} while (0)

constexpr int K1_KC = 0, K1_UR = 33792, K1_P = K1_UR + 18432, K1_LDS = K1_P + 32768;
__global__ __launch_bounds__(512, 2) void k1(const char* __restrict__ xq, const char* __restrict__ kk, const char* __restrict__ vv, const char* __restrict__ shk,
                                             const char* __restrict__ ssk, const u16* __restrict__ ssd, const char* __restrict__ w1, const char* __restrict__ w2, char* __restrict__ ws) {
    extern __shared__ __attribute__((aligned(16))) char smem[];
    const unsigned lb = (unsigned)(uintptr_t)(LAS char*)smem;
    const int wg = blockIdx.x, xcd = wg & 7, loc = wg >> 3, tid = threadIdx.x, lane = tid & 63;
    const int w = __builtin_amdgcn_readfirstlane(tid >> 6);
    const rsrc_t wr = mkrs(ws, O_END);
    STAMP(0, 0);
    if (loc < 18) {
        const int cw = 18 * xcd + loc;
        const int kch = tid >> 7, tau = tid & 127;
        const rsrc_t sr = mkrs(ssk, 576 * 4096);
        const unsigned so = (4 * cw + kch) * 4096 + tau * 32;
        u32x4 K0 = bl16<0>(sr, so, 0), K1 = bl16<0>(sr, so + 16, 0);
        unsigned K8 = bl4<0>(sr, so + 32, 0);
        const float dv = bflo(ssd[4 * cw + kch]);
        const rsrc_t kr = mkrs(kk, 2048 * 1152), vr = mkrs(vv, 2048 * 1152);
        u32x2 ks[4], vs[4];
        auto strip = [&](int g) AI { SB(); ks[g] = bl8<0>(kr, tid * 1152 + cw * 8, g * 512 * 1152); SB(); vs[g] = bl8<0>(vr, tid * 1152 + cw * 8, g * 512 * 1152); SB(); };
        strip(0);
        asm volatile("s_barrier" ::: "memory");
        strip(1);
        u64 halo[4][3];
#pragma unroll
        for (int g = 0; g < 4; ++g)
#pragma unroll
            for (int i = 0; i < 3; ++i) { int r = 512 * g + 64 * w - 1 - i; u64 t = *(const u64*)(kk + (size_t)(r < 0 ? 0 : r) * 1152 + cw * 8); halo[g][i] = r < 0 ? 0ull : t; }
        const u32x4 sa = *(const u32x4*)(shk + cw * 32), sb = *(const u32x4*)(shk + cw * 32 + 16);
        if (tid < 120) ldsw<u32x4>(lb + K1_UR + (tid / 30) * 4608 + 4096 + (tid % 30) * 16, u32x4{0, 0, 0, 0});
        if (tau == 0) K0[0] = (K0[0] & 0xffff0000u) | (pk2(bflo(K0[0]) + dv, 0.f) & 0xffffu);
        {
            const unsigned c0 = lb + K1_KC + kch * 8448 + 32 + tau * 32, c1 = c0 + 4224;
            ldsw<u32x4>(c0, K0); ldsw<u32x4>(c0 + 16, K1);
            ldsw<u32x4>(c1, u32x4{__builtin_amdgcn_alignbit(K0[1], K0[0], 16), __builtin_amdgcn_alignbit(K0[2], K0[1], 16), __builtin_amdgcn_alignbit(K0[3], K0[2], 16), __builtin_amdgcn_alignbit(K1[0], K0[3], 16)});
            ldsw<u32x4>(c1 + 16, u32x4{__builtin_amdgcn_alignbit(K1[1], K1[0], 16), __builtin_amdgcn_alignbit(K1[2], K1[1], 16), __builtin_amdgcn_alignbit(K1[3], K1[2], 16), __builtin_amdgcn_alignbit(K8, K1[3], 16)});
            if (tau == 0) {
                ldsw<u32x4>(c0 - 32, u32x4{0, 0, 0, 0}); ldsw<u32x4>(c0 - 16, u32x4{0, 0, 0, 0});
                ldsw<u32x4>(c1 - 32, u32x4{0, 0, 0, 0}); ldsw<u32x4>(c1 - 16, u32x4{0, 0, 0, K0[0] << 16});
            }
        }
        SB();
        bar();
        strip(2);
        STAMP(0, 1);
        const int ch = w & 3, hf = w >> 2, li = lane & 15, q = lane >> 4;
        u32x4 A[4][8];
        {
            const int r = (li + 1) & 1;
            unsigned aa = lb + K1_KC + ch * 8448 + r * 4224 + (128 * hf + li + 25 - 8 * q - r) * 2;
            OPAQ(aa);
#pragma unroll
            for (int c0 = 0; c0 < 4; ++c0)
#pragma unroll
                for (int c1 = 0; c1 < 8; ++c1) {
                    const unsigned a = aa + c1 * 512 + c0 * 64;
                    A[c0][c1] = u32x4{ldsr<unsigned>(a), ldsr<unsigned>(a + 4), ldsr<unsigned>(a + 8), ldsr<unsigned>(a + 12)};
                }
        }
        strip(3);
        f32x4 acc[8];
#pragma unroll
        for (int t = 0; t < 8; ++t) acc[t] = f32x4{0, 0, 0, 0};
        unsigned ba = lb + K1_UR + ch * 4608 + (2056 - 1792 - 16 * li + 128 * hf - 8 * q) * 2;
        OPAQ(ba);
        const unsigned ua = lb + K1_UR + (511 - tid) * 2;
        const unsigned pw = lb + K1_P + (hf * 4 + ch) * 2048 + li * 64 + q * 16;
        const unsigned pr = lb + K1_P + ((tid >> 4) * 32 + (tid & 15)) * 4;
        auto output = [&](int g) AI {
            if (tid < 256) {
                float y[2][4];
#pragma unroll
                for (int c = 0; c < 4; ++c)
#pragma unroll
                    for (int m = 0; m < 2; ++m) y[m][c] = ldsr<float>(pr + (g & 1) * 16384 + c * 2048 + m * 64) + ldsr<float>(pr + (g & 1) * 16384 + 8192 + c * 2048 + m * 64);
                bs16<0>(u32x4{pk2(y[0][0], y[0][1]), pk2(y[0][2], y[0][3]), pk2(y[1][0], y[1][1]), pk2(y[1][2], y[1][3])}, wr, (tid >> 4) * 36864 + (tid & 15) * 16, O_YB + g * 16 * 36864 + cw * 256);
            }
        };
        sfor<0, 4>([&](auto G) AI {
            constexpr int g = G;
            unsigned kx[4][2];
            kx[0][0] = ks[g][0]; kx[0][1] = ks[g][1];
#pragma unroll
            for (int i = 1; i < 4; ++i) {
                kx[i][0] = __builtin_amdgcn_update_dpp((unsigned)halo[g][i - 1], kx[i - 1][0], 0x138, 0xf, 0xf, false);
                kx[i][1] = __builtin_amdgcn_update_dpp((unsigned)(halo[g][i - 1] >> 32), kx[i - 1][1], 0x138, 0xf, 0xf, false);
            }
            float s0 = bflo(kx[0][0]) * bflo(sa[0]) + bflo(kx[1][0]) * bfhi(sa[0]) + bflo(kx[2][0]) * bflo(sa[1]) + bflo(kx[3][0]) * bfhi(sa[1]);
            float s1 = bfhi(kx[0][0]) * bflo(sa[2]) + bfhi(kx[1][0]) * bfhi(sa[2]) + bfhi(kx[2][0]) * bflo(sa[3]) + bfhi(kx[3][0]) * bfhi(sa[3]);
            float s2 = bflo(kx[0][1]) * bflo(sb[0]) + bflo(kx[1][1]) * bfhi(sb[0]) + bflo(kx[2][1]) * bflo(sb[1]) + bflo(kx[3][1]) * bfhi(sb[1]);
            float s3 = bfhi(kx[0][1]) * bflo(sb[2]) + bfhi(kx[1][1]) * bfhi(sb[2]) + bfhi(kx[2][1]) * bflo(sb[3]) + bfhi(kx[3][1]) * bfhi(sb[3]);
            const unsigned p01 = pk2(s0 * bflo(vs[g][0]), s1 * bfhi(vs[g][0])), p23 = pk2(s2 * bflo(vs[g][1]), s3 * bfhi(vs[g][1]));
            const unsigned u = ua + (3 - g) * 1024;
            ldsw<u16>(u, (u16)p01); ldsw<u16>(u + 4608, (u16)(p01 >> 16)); ldsw<u16>(u + 9216, (u16)p23); ldsw<u16>(u + 13824, (u16)(p23 >> 16));
            SB();
            bar();
            u32x4 B[8];
#pragma unroll
            for (int i = 0; i < 8; ++i) B[i] = ldsr<u32x4>(ba + (7 - 2 * g - (i >> 2)) * 512 + (i & 3) * 64);
            SB();
            if constexpr (g >= 1) output(g - 1);
            SB();
#pragma unroll
            for (int i = 0; i < 8; ++i) {
                const int b = 2 * g + (i >> 2);
#pragma unroll
                for (int c1 = 0; c1 + b < 8; ++c1) acc[b + c1] = mfma16(asb(A[i & 3][c1]), asb(B[i]), acc[b + c1]);
            }
            SB();
            ldsw<f32x4>(pw + (g & 1) * 16384, acc[2 * g]); ldsw<f32x4>(pw + (g & 1) * 16384 + 1024, acc[2 * g + 1]);
            STAMP(0, 2 + g);
        });
        bar();
        output(3);
    } else {
        const int u = (loc - 18) * 8 + w;
        if (u < 32 || (u >= 73 && u < 82)) {
            const bool isx = u < 32;
            const int rl = lane >> 4, c = lane & 15;
            const int rb = 32 * (xcd >> 2) + 4 * (u >> 2) + (xcd & 3), rho = 4 * (u & 3) + rl;
            const int r0 = isx ? 32 * rb + rho : 8 * (9 * xcd + u - 73) + rl, r1 = r0 + (isx ? 16 : 4);
            const rsrc_t xr = mkrs(isx ? xq : w2, isx ? 2048 * 1152 : 576 * 1152);
            u32x2 a[9], b[9];
#pragma unroll
            for (int m = 0; m < 9; ++m) { a[m] = bl8<0>(xr, r0 * 1152 + c * 8, m * 128); b[m] = bl8<0>(xr, r1 * 1152 + c * 8, m * 128); }
            float sa = 0, sb = 0;
#pragma unroll
            for (int m = 0; m < 9; ++m) {
                sa += (bflo(a[m][0]) + bfhi(a[m][0])) + (bflo(a[m][1]) + bfhi(a[m][1]));
                sb += (bflo(b[m][0]) + bfhi(b[m][0])) + (bflo(b[m][1]) + bfhi(b[m][1]));
                if (isx) bs16<0>(u32x4{a[m][0], a[m][1], b[m][0], b[m][1]}, wr, c * 256 + rho * 16, O_XT + rb * 36864 + m * 4096);
            }
            const float ma = rowsum16(sa) * RD576, mb = rowsum16(sb) * RD576;
            if (isx) {
                float qa = 0, qb = 0;
#pragma unroll
                for (int m = 0; m < 9; ++m)
#pragma unroll
                    for (int i = 0; i < 2; ++i) {
                        float t;
                        t = bflo(a[m][i]) - ma; qa += t * t; t = bfhi(a[m][i]) - ma; qa += t * t;
                        t = bflo(b[m][i]) - mb; qb += t * t; t = bfhi(b[m][i]) - mb; qb += t * t;
                    }
                const float ra = 1.0f / sqrtf(rowsum16(qa) * RD576 + EPS), rbb = 1.0f / sqrtf(rowsum16(qb) * RD576 + EPS);
                if (c == 0) { bs8<0>(u32x2{__float_as_uint(ra), __float_as_uint(-ma * ra)}, wr, r0 * 8, O_XS); bs8<0>(u32x2{__float_as_uint(rbb), __float_as_uint(-mb * rbb)}, wr, r1 * 8, O_XS); }
            } else if (c < 2) {
                const int k = c ? r1 : r0; const float m = c ? mb : ma;
                const int p = (k >> 3) * 32 + (k & 7) * 2;
                const unsigned hi = pk2(m, 0.f) & 0xffffu, lo = pk2(m - bflo(hi), 0.f) & 0xffffu;
                *(u16*)(ws + O_W2M + p) = (u16)hi; *(u16*)(ws + O_W2M + 16 + p) = (u16)lo;
            }
        } else if (u < 73) {
            const int g = 41 * xcd + u - 32;
            if (g < 324) {
                const int mat = g >= 162, r = g - 162 * mat, ks = r / 9, cg = r - 9 * ks;
                const rsrc_t mr = mkrs(mat ? w2 : w1, 576 * 1152);
                u32x4 t[4];
#pragma unroll
                for (int i = 0; i < 4; ++i) t[i] = bl16<0>(mr, (lane >> 3) * 1152 + (lane & 7) * 16, (32 * ks + 8 * i) * 1152 + cg * 128);
                const unsigned lw = lb + w * 4096;
#pragma unroll
                for (int i = 0; i < 4; ++i) ldsw<u32x4>(lw + i * 1024 + lane * 16, t[i]);
                const unsigned ta = lw + ((mat ? 8 : 4) * (lane >> 4) + ((lane & 15) >> 2)) * 128 + (lane & 3) * 8, tb = ta + (mat ? 512 : 2048);
                const u32x4 ones = {0x3f803f80u, 0x3f803f80u, 0x3f803f80u, 0x3f803f80u};
#pragma unroll
                for (int tt = 0; tt < 4; ++tt) {
                    const bf16x8 f = trfrag(ta + tt * 32, tb + tt * 32);
                    bs16<0>(__builtin_bit_cast(u32x4, f), wr, lane * 16, (mat ? O_W2T : O_W1T) + ((4 * cg + tt) * 18 + ks) * 1024);
                    if (!mat) {
                        const f32x4 cs = mfma16(f, asb(ones), f32x4{0, 0, 0, 0});
                        if ((lane & 15) == 0) bs16<0>(__builtin_bit_cast(u32x4, cs), wr, (lane >> 4) * 16, O_CSP + (ks * 576 + 64 * cg + 16 * tt) * 4);
                    }
                }
            }
        } else if (u == 83) {
            bs16<0>(u32x4{0, 0, 0, 0}, wr, lane * 16, O_FLAG + xcd * 2048); bs16<0>(u32x4{0, 0, 0, 0}, wr, lane * 16, O_FLAG + xcd * 2048 + 1024);
        } else if (u == 82) {
            const int l = 9 * xcd + (lane >> 3);
            bs16<0>(u32x4{0, 0, 0, 0}, wr, (l >> 1) * GTS + (l & 1) * (2052 * 32) + (lane & 7) * 16, O_GB);
            const int l2 = 9 * xcd + 8;
            if (lane < 8) bs16<0>(u32x4{0, 0, 0, 0}, wr, (l2 >> 1) * GTS + (l2 & 1) * (2052 * 32) + lane * 16, O_GB);
        }
    }
    STAMP(0, 7);
}

constexpr int SLOT = 18432, NSL = 6, L_EX = NSL * SLOT, L_F = L_EX, L_ST = L_EX + 36864, L_MN = L_ST + 2048, K2_LDS = L_MN + 256;
constexpr unsigned F_B = O_FLAG, F_C = O_FLAG + 66 * 128, F_D = F_C + 16 * 128, F_E = F_D + 16 * 128;
#define P_RD 3
#define P_EX 0
#define P_IL 0
#define P_PRIO 2
constexpr int RD = P_RD, BP = RD + 1, HD = 8;
constexpr int SPIN = 1 << 17;

template <bool EX> DEVI void gemm_small(const u32x4* W, const u32x4* We, unsigned fa, unsigned fe, f32x4* acc, f32x4& ae) {
    constexpr int PF = 3;
    u32x4 b[PF + 1][3];
    sfor<0, 18 + PF>([&](auto K) AI {
        constexpr int k = K, m = k - PF;
        if constexpr (k < 18) {
            b[k % (PF + 1)][0] = ldsr<u32x4>(fa + k * 2048); b[k % (PF + 1)][1] = ldsr<u32x4>(fa + k * 2048 + 1024);
            if constexpr (EX) b[k % (PF + 1)][2] = ldsr<u32x4>(fe + k * 2048);
        }
        if constexpr (m >= 0) {
            const f32x4 z = {0, 0, 0, 0};
            acc[0] = mfma16(asb(W[m]), asb(b[m % (PF + 1)][0]), m ? acc[0] : z); acc[1] = mfma16(asb(W[m]), asb(b[m % (PF + 1)][1]), m ? acc[1] : z);
            if constexpr (EX) ae = mfma16(asb(We[m]), asb(b[m % (PF + 1)][2]), m ? ae : z);
        }
        SB();
    });
}
DEVI void poll(rsrc_t wr, unsigned off, unsigned need) {
#pragma unroll 1
    for (int i = 0; i < SPIN; ++i) {
        const unsigned v = bl4<16>(wr, off, 0);
        if (__builtin_amdgcn_ballot_w64(v < need) == 0) break;
        asm volatile("s_sleep 1" ::: "memory");
    }
    asm volatile("" ::: "memory");
}
constexpr int e_mem(int j) { return j < 16 ? j >> 2 : (j - 16) / 5; }
constexpr int e_til(int j) { return j < 16 ? j & 3 : 4 + (j - 16) % 5; }
constexpr int e_first(int j) { int f = 0; for (int i = 0; i < j; ++i) f += e_mem(i) == 3 ? 12 : 8; return f; }
constexpr int e_step(int e) { int j = 0; while (j < 35 && e_first(j + 1) <= e) ++j; return j; }
constexpr int g_nd(int i) { return i < -5 ? 0 : i + 5 < 18 ? 5 : i + 5 < 36 ? 4 : 0; }
constexpr int g_half(int i) { return (i >= 5 && i <= 22) ? i - 5 : -1; }
constexpr int g_nx(int i) { return i == 20 ? 4 : (i == 21 || i == 22) ? 2 : i == 23 ? 1 : 0; }
constexpr int g_x0(int i) { return i == 20 ? 0 : i == 21 ? 4 : i == 22 ? 6 : 8; }
constexpr int g_pre(int i) { return (i == 17 || i == 26) ? 1 : 0; }
constexpr int g_oth(int i) { return g_nx(i) + (g_half(i) >= 0 ? 1 : 0); }
constexpr int g_all(int i) { return g_pre(i) + g_nd(i) + g_oth(i); }
constexpr int g_wait(int u) {
    int n = (g_half(u - 4) >= 0 ? 1 : 0) + g_all(u - 3) + g_all(u - 2) + g_all(u - 1);
    if (u == 15 || u == 23 || u == 25) n = g_all(u - 2) + g_all(u - 1);
    if (g_pre(u - 1)) n = g_all(u - 1) - 1;
    return n;
}

__global__ __launch_bounds__(512, 2) void k2(const char* __restrict__ w3, const char* __restrict__ w4, char* __restrict__ ws, char* __restrict__ out) {
    extern __shared__ __attribute__((aligned(16))) char smem[];
    const unsigned lb = (unsigned)(uintptr_t)(LAS char*)smem;
    const int wg = blockIdx.x, xcd = wg & 7, loc = wg >> 3, lane = threadIdx.x & 63;
    const int kap = xcd & 3, rt = 8 * (xcd >> 2) + (loc >> 2), nt = loc & 3, rb = 4 * rt + kap, fc = 4 * kap + nt;
    const int w = __builtin_amdgcn_readfirstlane(threadIdx.x >> 6), wm = w & 3;
    const int lj = lane & 15, lq = lane >> 4, hh = lane >> 5;
    const unsigned l16 = lane * 16;
    const rsrc_t wr = mkrs(ws, O_END);
    STAMP(1, 0);
    const int a9 = 9 * (fc >> 1), odd = fc & 1;
    const char* wp = w3 + (a9 + 5 * odd) * 128 + (size_t)wm * 36 * 9216;
    const char* wsg = w3 + (a9 + 4) * 128 + odd * 64 + (size_t)wm * 72 * 9216;
    const unsigned pvo = (lane >> 3) * 9216 + (((lane & 7) ^ (((lane >> 4) & 1) << 2)) * 16), svo = (lane >> 2) * 9216 + (lane & 3) * 16;
    auto dmaD = [&](int u, int i) AI {
        const unsigned dst = lb + (u % NSL) * SLOT + wm * 4608 + i * 1024;
        const char* src = u < 16 ? wp + (u >> 2) * 128 + (size_t)((u & 3) * 144 + i * 8) * 9216 : wsg + (size_t)((u & 1) * 288 + i * 16) * 9216;
        if (i < 4) dma16(dst, src, u < 16 ? pvo : svo); else dma16h(dst, src, u < 16 ? pvo : svo);
    };

    auto bc = [&](auto ROLE) AI {
    constexpr int role = ROLE, NC = role < 2 ? 5 : 4;
    u32x4 Y[5], X[5], W[18], We[18];
    int nu[5];
#pragma unroll
    for (int m = 0; m < 4; ++m) nu[m] = ((nt + m) & 3) * 8 + w;
    nu[4] = 32 + wm;
    const u32x2 xs0 = bl8<0>(wr, lj * 8, O_XS + rb * 256), xs1 = bl8<0>(wr, lj * 8, O_XS + rb * 256 + 128);
#pragma unroll
    for (int m = 0; m < NC; ++m) { Y[m] = bl16<0>(wr, l16, O_YB + (rb * 36 + nu[m]) * 1024); X[m] = bl16<0>(wr, l16, O_XT + (rb * 36 + nu[m]) * 1024); SB(); }
    asm volatile("s_barrier" ::: "memory");
    auto ldw = [&](unsigned base, int k) AI {
        W[k] = bl16<0>(wr, l16, base + ((8 * nt + w) * 18 + k) * 1024);
        if constexpr (role == 0) We[k] = bl16<0>(wr, l16, base + ((32 + nt) * 18 + k) * 1024);
        SB();
    };
    float s[2] = {0, 0}, sq[2] = {0, 0};
    const float rx[2] = {__uint_as_float(xs0[0]), __uint_as_float(xs1[0])}, mx[2] = {__uint_as_float(xs0[1]), __uint_as_float(xs1[1])};
    u32x4 hown;
    constexpr int WB = role == 0 ? 2 : 3;
#pragma unroll
    for (int m = 0; m < NC; ++m) {
#pragma unroll
        for (int k = WB * m; k < WB * m + WB; ++k) ldw(O_W1T, k);
        u32x4 hp;
#pragma unroll
        for (int i = 0; i < 4; ++i) {
            const int mt = i >> 1;
            const float x0 = bflo(X[m][i]), x1 = bfhi(X[m][i]);
            const float h0 = __builtin_fmaf(__builtin_fmaf(x0, rx[mt], mx[mt]), bflo(Y[m][i]), x0), h1 = __builtin_fmaf(__builtin_fmaf(x1, rx[mt], mx[mt]), bfhi(Y[m][i]), x1);
            s[mt] += h0 + h1; sq[mt] += h0 * h0 + h1 * h1;
            hp[i] = pk2(h0, h1);
        }
        const unsigned a = lb + L_F + (nu[m] >> 1) * 2048 + (nu[m] & 1) * 8 + l16;
        ldsw<u32x2>(a, u32x2{hp[0], hp[1]}); ldsw<u32x2>(a + 1024, u32x2{hp[2], hp[3]});
        if (m == 0) hown = hp;
        SB();
    }
    {
        swap16f(s[0], s[1]); swap16f(sq[0], sq[1]);
        float t = s[0] + s[1], u = sq[0] + sq[1];
        swap32f(t, u);
        ldsw<float>(lb + L_ST + w * 256 + lane * 4, t + u);
    }
    bar();
    STAMP(1, 1);
#pragma unroll
    for (int k = WB * NC; k < 18; ++k) ldw(O_W1T, k);
    u32x4 ca = bl16<0>(wr, lj * 2304 + lq * 16, O_CSP + (8 * nt + w) * 64), cb = bl16<0>(wr, (16 + (lj & 1)) * 2304 + lq * 16, O_CSP + (8 * nt + w) * 64), cea, ceb;
    if constexpr (role == 0) { cea = bl16<0>(wr, lj * 2304 + lq * 16, O_CSP + (32 + nt) * 64); ceb = bl16<0>(wr, (16 + (lj & 1)) * 2304 + lq * 16, O_CSP + (32 + nt) * 64); }
    SB();
    f32x4 acc[2], ae;
    unsigned fa = lb + L_F + l16, fe = fa + (w & 1) * 1024; OPAQ(fa); OPAQ(fe);
    float tot = 0;
#pragma unroll
    for (int i = 0; i < 8; ++i) tot += ldsr<float>(lb + L_ST + i * 256 + lane * 4);
    u32x2 hext;
    if constexpr (role == 0) hext = ldsr<u32x2>(fe + ((32 + nt) >> 1) * 2048 + (nt & 1) * 8);
    float su = tot, qu = tot; swap32f(su, qu);
    const float mean = su * RD576, var = fmaxf(qu * RD576 - mean * mean, 0.f);
    const float r1s = 1.0f / sqrtf(var + EPS), r12 = r1s / sqrtf(var * r1s * r1s + EPS);
    float mm[2] = {mean, mean}, rr[2] = {r12, r12}, r1[2] = {r1s, r1s};
    swap16f(mm[0], mm[1]); swap16f(rr[0], rr[1]); swap16f(r1[0], r1[1]);
    SB();
    gemm_small<role == 0>(W, We, fa, fe, acc, ae);
    STAMP(1, 2);
    {
        float cs[4];
#pragma unroll
        for (int r = 0; r < 4; ++r) cs[r] = rowsum16(__uint_as_float(ca[r]) + (lj < 2 ? __uint_as_float(cb[r]) : 0.f));
        unsigned o[2][2];
#pragma unroll
        for (int mt = 0; mt < 2; ++mt) {
            float g[4];
#pragma unroll
            for (int r = 0; r < 4; ++r) g[r] = gelu((acc[mt][r] - mm[mt] * cs[r]) * rr[mt]);
            o[mt][0] = pk2(g[0], g[1]); o[mt][1] = pk2(g[2], g[3]);
        }
        swap16(o[0][0], o[1][0]); swap16(o[0][1], o[1][1]);
        bs16<XA>(u32x4{o[0][0], o[0][1], o[1][0], o[1][1]}, wr, (lane & 31) * 32 + hh * 16, O_GB + (8 * nt + w) * GTS + (4 + 32 * rb) * 32);
        if constexpr (role == 0) {
            float g[4];
            const float me = w ? mm[1] : mm[0], re = w ? rr[1] : rr[0];
#pragma unroll
            for (int r = 0; r < 4; ++r) g[r] = gelu((ae[r] - me * rowsum16(__uint_as_float(cea[r]) + (lj < 2 ? __uint_as_float(ceb[r]) : 0.f))) * re);
            unsigned a0 = pk2(g[0], g[1]), a1 = pk2(g[2], g[3]), b0 = a0, b1 = a1;
            swap16(a0, b0); swap16(a1, b1);
            if (!(lq & 1)) bs16<XA>(u32x4{a0, a1, b0, b1}, wr, lj * 32 + hh * 16, O_GB + (32 + nt) * GTS + (4 + 32 * rb + 16 * w) * 32);
        }
    }
    waitvm<0>();
    bar();
    if (w == 0 && lane == 0) bs4<16>(1u, wr, 0, F_B + (rb + 1) * 128 + nt * 4);
    STAMP(1, 3);
    if constexpr (role == 1) {
        if (lj < 2) {
#pragma unroll
            for (int k = 0; k < 18; ++k) We[k] = bl16<0>(wr, lq * 32 + lj * 16 + k * 128, O_W2M);
        }
    }
#pragma unroll
    for (int k = 0; k < 18; ++k) ldw(O_W2T, k);
    {
        const int pr = rb + (lane >> 2);
        poll(wr, F_B + (lane < 12 ? pr * 128 + (lane & 3) * 4 : 0), (lane < 12 && pr >= 1 && pr <= 64) ? 1u : 0u);
    }
    STAMP(1, 4);
    u32x4 G[5];
    auto gaddr = [&](int n) AI { const int gr = n / 9, sh = gr == 0 ? 1 : gr == 1 ? -1 : gr == 2 ? 2 : -2; return O_GB + n * GTS + (4 + 32 * rb - sh) * 32; };
#pragma unroll
    for (int m = 0; m < NC; ++m) { G[m] = bl16<XA>(wr, l16, gaddr(8 * m + w)); SB(); }
    const unsigned gw = lb + L_F + (w >> 1) * 2048 + hh * 1024 + (((lane >> 1) & 15) + 16 * (2 * (w & 1) + (lane & 1))) * 16;
#pragma unroll
    for (int m = 0; m < NC; ++m) ldsw<u32x4>(gw + m * 8192, G[m]);
    bar();
    STAMP(1, 5);
    if constexpr (role == 2) {
#pragma unroll
        for (int u = 0; u < 2; ++u)
#pragma unroll
            for (int i = 0; i < 5; ++i) dmaD(u, i);
    }
    gemm_small<(role < 2)>(W, We, fa, fe, acc, ae);
    if constexpr (role == 1) { if (lane < 16) ldsw<float>(lb + L_MN + (w & 1) * 64 + lane * 4, ae[0] + ae[1]); }
    bar();
    STAMP(1, 6);
    {
        const float mn[2] = {ldsr<float>(lb + L_MN + lj * 4), ldsr<float>(lb + L_MN + 64 + lj * 4)};
        unsigned o[2][2]; float q2[2]; f32x4 res[2];
#pragma unroll
        for (int mt = 0; mt < 2; ++mt) {
            const float h[4] = {bflo(hown[2 * mt]), bfhi(hown[2 * mt]), bflo(hown[2 * mt + 1]), bfhi(hown[2 * mt + 1])};
            float xc[4];
#pragma unroll
            for (int r = 0; r < 4; ++r) { const float x1 = __builtin_fmaf(h[r] - mm[mt], r1[mt], acc[mt][r]); xc[r] = x1 - mn[mt]; res[mt][r] = h[r] + x1; }
            q2[mt] = (xc[0] * xc[0] + xc[1] * xc[1]) + (xc[2] * xc[2] + xc[3] * xc[3]);
            o[mt][0] = pk2(xc[0], xc[1]); o[mt][1] = pk2(xc[2], xc[3]);
        }
        swap16(o[0][0], o[1][0]); swap16(o[0][1], o[1][1]);
        bs16<XA>(u32x4{o[0][0], o[0][1], o[1][0], o[1][1]}, wr, l16, O_XB + (rb * 36 + 8 * nt + w) * 1024);
        auto quad = [&](float t) AI { return u32x4{__float_as_uint(t), __float_as_uint(dppf<0x55>(t)), __float_as_uint(dppf<0xAA>(t)), __float_as_uint(dppf<0xFF>(t))}; };
        {
            swap16f(q2[0], q2[1]);
            float t = q2[0] + q2[1], u = t; swap32f(t, u);
            const u32x4 qv = quad(t + u);
            if (lane < 32 && !(lane & 3)) bs16<XA>(qv, wr, lane * 4, O_SQ + ((8 * nt + w) * 2048 + rb * 32) * 4);
        }
        f32x4 rese;
        if constexpr (role == 0) {
            const float h[4] = {bflo(hext[0]), bfhi(hext[0]), bflo(hext[1]), bfhi(hext[1])};
            const float re = w ? r1[1] : r1[0], me = w ? mm[1] : mm[0], ne = w ? mn[1] : mn[0];
            float xc[4];
#pragma unroll
            for (int r = 0; r < 4; ++r) { const float x1 = __builtin_fmaf(h[r] - me, re, ae[r]); xc[r] = x1 - ne; rese[r] = h[r] + x1; }
            unsigned a0 = pk2(xc[0], xc[1]), a1 = pk2(xc[2], xc[3]), b0 = a0, b1 = a1;
            swap16(a0, b0); swap16(a1, b1);
            if (!(lq & 1)) bs16<XA>(u32x4{a0, a1, b0, b1}, wr, hh * 512 + lj * 16, O_XB + (rb * 36 + 32 + nt) * 1024 + w * 256);
            float t = (xc[0] * xc[0] + xc[1] * xc[1]) + (xc[2] * xc[2] + xc[3] * xc[3]), u = t;
            swap16f(t, u); t += u; u = t; swap32f(t, u);
            const u32x4 qv = quad(t + u);
            if (lane < 16 && !(lane & 3)) bs16<XA>(qv, wr, lane * 4, O_SQ + ((32 + nt) * 2048 + rb * 32 + 16 * w) * 4);
        }
        waitvm<0>();
        bar();
        if (w == 0 && lane == 0) bs4<16>(1u, wr, 0, F_C + rt * 128 + (kap * 4 + nt) * 4);
        if constexpr (role == 0) bs16<0>(__builtin_bit_cast(u32x4, rese), wr, l16, O_R + ((rb * 36 + 32 + nt) * 2 + w) * 1024);
#pragma unroll
        for (int mt = 0; mt < 2; ++mt) bs16<0>(__builtin_bit_cast(u32x4, res[mt]), wr, l16, O_R + ((rb * 36 + 8 * nt + w) * 2 + mt) * 1024);
    }
    };
    if (w < 2) bc(std::integral_constant<int, 0>{}); else if (w < 4) bc(std::integral_constant<int, 1>{}); else bc(std::integral_constant<int, 2>{});
    STAMP(1, 7);

    unsigned kb[4], kq[4];
#pragma unroll
    for (int i = 0; i < 4; ++i) { const int m = (nt + i) & 3; kb[i] = 36 * kap + 18 * (m >> 1) + 10 * (m & 1); kq[i] = 36 * kap + 18 * (m >> 1) + 8 + (m & 1); }
    unsigned ex = lb + L_EX + wm * 8192 + (P_EX ? lane * 8 : l16);
    OPAQ(ex);
    if (w < 4) {
        poll(wr, F_C + rt * 128 + (wm * 4 + (lane & 3)) * 4, 1u);
        STAMP(2, 0);
        u32x4 X[36];
#pragma unroll
        for (int i = 0; i < 36; ++i) X[i] = bl16<XA>(wr, l16, O_XB + ((4 * rt + wm) * 36 + i) * 1024);
        const int r = 8 * hh + (lj >> 2);
        unsigned ta[2][2], ts;
#pragma unroll
        for (int s = 0; s < 2; ++s)
#pragma unroll
            for (int ab = 0; ab < 2; ++ab) { ta[s][ab] = lb + s * 3 * SLOT + r * 128 + ((ab ^ ((r >> 1) & 1)) * 64) + (lq & 1) * 32 + (lane & 3) * 8; OPAQ(ta[s][ab]); }
        ts = lb + 3 * SLOT + r * 64 + (lq & 1) * 32 + (lane & 3) * 8; OPAQ(ts);
        const int re = 16 * (lq >> 1) + 4 * (lq & 1) + (lj >> 2);
        unsigned am[2][8], ax[4];
        u32x4 H[HD][2];
        const unsigned hvo = l16 + (8 * rt + 2 * wm) * 144 * 1024;
        auto ldH = [&](int j, int mt) AI {
            const int t = e_til(j);
            H[j % HD][mt] = bl16<XA>(wr, hvo + mt * 144 * 1024 + (t & 3) * 1024, O_HB + (t < 8 ? kb[e_mem(j)] + (t & 4) : kq[e_mem(j)]) * 1024);
        };
        __builtin_amdgcn_s_setprio(P_PRIO);
        f32x16 acc[4]; f32x4 ec[12][2]; bf16x8 A[RD + 1];
        auto exw = [&](const f32x16& a, int q, int i) AI {
            const int qq = q & 3;
            if (P_EX == 0) ldsw<f32x4>(ex + q * 1024, f32x4{a[4 * qq], a[4 * qq + 1], a[4 * qq + 2], a[4 * qq + 3]});
            else ldsw<f32x2>(ex + q * 1024 + i * 512, f32x2{a[4 * qq + 2 * i], a[4 * qq + 2 * i + 1]});
        };
        sfor<0, 19>([&](auto C) AI { sfor<0, 36>([&](auto I) AI {
            constexpr int fr = 36 * C + I, fm = fr - RD;
            if constexpr (fr < 648 + RD) {
            constexpr int em = fm - 324, jm = em >= 0 ? e_step(em) : 0, cm = em >= 0 ? em - e_first(jm) : 0;
            constexpr bool z0 = cm < 8 ? jm == 0 : jm == 12;
            if constexpr (em >= 0) ec[cm][0] = mfma16(A[fm % (RD + 1)], asb(H[jm % HD][0]), z0 ? f32x4{0, 0, 0, 0} : ec[cm][0]);
            if constexpr (fm >= 0 && fm < 324) {
                constexpr int u = fm / 18, o = fm % 18, ks = u < 16 ? 9 * (u & 3) + (o >> 1) : 18 * (u & 1) + o, ai = u < 16 ? ((u >> 2) & 1) * 2 + (o & 1) : 0;
                f32x16 z = {};
                acc[ai] = __builtin_amdgcn_mfma_f32_32x32x16_bf16(A[fm % (RD + 1)], asb(X[ks]), ks == 0 ? z : acc[ai], 0, 0, 0);
            }
            SB();
            if constexpr (fm >= 72 && fm < 324) {
                constexpr int p = fm / 72 - 1, o = fm % 72;
                if constexpr (o >= 2 && o <= 17 && (P_EX != 0 || !(o & 1))) { constexpr int q = (o - 2) >> 1; exw(acc[(p & 1) * 2 + (q >> 2)], q, o & 1); SB(); }
            }
            if constexpr (em >= 4 && em < 12 && (P_EX != 0 || !(em & 1))) { exw(acc[0], (em - 4) >> 1, em & 1); SB(); }
            if constexpr (fr == 300) {
#pragma unroll
                for (int c = 0; c < 8; ++c) { am[0][c] = lb + re * 256 + ((c ^ (re & 7)) << 5) + (lane & 3) * 8; am[1][c] = am[0][c] + 3 * SLOT; OPAQ(am[0][c]); OPAQ(am[1][c]); }
#pragma unroll
                for (int c = 0; c < 4; ++c) { ax[c] = lb + L_EX + re * 128 + ((c ^ ((re >> 1) & 3)) << 5) + (lane & 3) * 8; OPAQ(ax[c]); }
            }
            s16x4 t0, t1;
            unsigned a = 0, a2 = 0;
            if constexpr (fr < 324) {
                constexpr int u = fr / 18, o = fr % 18, sl = u % NSL;
                if constexpr (o == BP) asm volatile("s_waitcnt lgkmcnt(%0)\n\ts_barrier" :: "i"((u >= 5 && (u & 3) == 1) ? 4 : 15) : "memory");
                if constexpr (u < 16) { a = ta[sl / 3][o & 1] + (sl % 3) * SLOT + (o >> 1) * 2048; a2 = a + 512; }
                else { a = ts + (sl - 3) * SLOT + o * 1024; a2 = a + 256; }
            } else if constexpr (fr < 648) {
                constexpr int er = fr - 324, jr = e_step(er), cr = er - e_first(jr), sl = (18 + (jr >> 1)) % NSL;
                if constexpr (!(jr & 1) && cr == BP) asm volatile("s_waitcnt lgkmcnt(%0)\n\ts_barrier" :: "i"(jr == 2 ? 4 : 15) : "memory");
                if constexpr (cr < 8) { a = am[sl / 3][cr & 7] + (sl % 3) * SLOT + (jr & 1) * 8192; a2 = a + 2048; }
                else { a = ax[cr & 3] + e_til(jr) * 4096; a2 = a + 1024; }
            }
            if constexpr (fr < 648) t0 = __builtin_amdgcn_ds_read_tr16_b64_v4i16((LAS s16x4*)(uintptr_t)a);
            if constexpr (em >= 0) {
                SB();
                ec[cm][1] = mfma16(A[fm % (RD + 1)], asb(H[jm % HD][1]), z0 ? f32x4{0, 0, 0, 0} : ec[cm][1]);
                SB();
            }
            if constexpr (fr < 648) {
                t1 = __builtin_amdgcn_ds_read_tr16_b64_v4i16((LAS s16x4*)(uintptr_t)a2);
                A[fr % (RD + 1)] = __builtin_bit_cast(bf16x8, __builtin_shufflevector(t0, t1, 0, 1, 2, 3, 4, 5, 6, 7));
            }
            if constexpr (fr >= 304 && fr < 320 && !(fr & 1)) ldH((fr - 304) >> 2, ((fr - 304) >> 1) & 1);
            if constexpr (em >= 2 && em < 8) ldH(4 + ((em - 2) >> 1), em & 1);
            if constexpr (em >= 0 && (cm == 3 || cm == 5)) {
                constexpr int mt = cm == 5;
                if constexpr (jm >= 1 && jm + 6 < 16) ldH(jm + 6, mt);
                if constexpr (jm == 14 || jm == 15) { ldH(16 + 2 * (jm - 14), mt); ldH(17 + 2 * (jm - 14), mt); }
                if constexpr (jm == 16) ldH(20, mt);
                if constexpr (jm >= 18 && jm < 22) { ldH(21 + 2 * (jm - 18), mt); ldH(22 + 2 * (jm - 18), mt); }
                if constexpr (jm >= 22 && jm + 7 < 36) ldH(jm + 7, mt);
            }
            SB();
            }
        }); });
        __builtin_amdgcn_s_setprio(0);
        STAMP(2, 2);
#pragma unroll
        for (int c = 0; c < 12; ++c) {
            const u32x4 o = {pk2(ec[c][0][0], ec[c][0][1]), pk2(ec[c][0][2], ec[c][0][3]), pk2(ec[c][1][0], ec[c][1][1]), pk2(ec[c][1][2], ec[c][1][3])};
            if (c < 8) bs16<XA>(o, wr, l16, O_PB + (((4 * rt + wm) * 32 + 8 * nt + c) * 4 + kap) * 1024);
            else bs16<XA>(o, wr, l16, O_PX + (((4 * rt + wm) * 4 + c - 8) * 16 + fc) * 1024);
        }
        waitvm<0>();
    } else {
#pragma unroll
        for (int u = 2; u < 5; ++u)
#pragma unroll
            for (int i = 0; i < 5; ++i) dmaD(u, i);
        poll(wr, F_C + rt * 128 + (wm * 4 + (lane & 3)) * 4, 1u);
        STAMP(2, 0);
        unsigned sqv[18];
        {
            const char* sb = ws + O_SQ + (4 * rt + wm) * 128;
            const unsigned so = (lane & 31) * 4 + hh * 18 * 8192;
#pragma unroll
            for (int i = 0; i < 18; ++i) asm volatile("global_load_dword %0, %1, %2 sc1" : "=v"(sqv[i]) : "v"(so), "s"(sb + i * 8192) : "memory");
        }
        const int drow = 4 * wm + lq;
        const unsigned dvo = drow * 1152 + ((lj ^ (2 * (drow & 7))) << 4);
        const char* w4m = w4 + nt * 256;
        auto ksE = [&](int j) AI { return e_til(j) < 8 ? kb[e_mem(j)] + e_til(j) : kq[e_mem(j)]; };
        auto dmaE = [&](int u, int i) AI { const int j = 2 * (u - 18) + (i >> 1);
            dma16(lb + (u % NSL) * SLOT + (j & 1) * 8192 + (wm + 4 * (i & 1)) * 1024, w4m + (size_t)(ksE(j) * 32 + 16 * (i & 1)) * 1152, dvo); };
        const int xrow = lane >> 3;
        const unsigned xvo = xrow * 1152 + (((lane & 7) ^ (2 * ((xrow >> 1) & 3))) << 4);
        auto dmaX = [&](int t) AI { dma16(lb + L_EX + t * 4096 + wm * 1024, w4 + 1024 + (size_t)((t < 8 ? kb[3] + t : kq[3]) * 32 + wm * 8) * 1152, xvo); };
        const unsigned hvo = (8 * rt + 2 * wm + (lq & 1)) * 144 * 1024 + (lj + 16 * hh) * 16 + O_HB;
        const unsigned fdo = F_D + rt * 128 + kap * 16;
        float k0, k1, sg;
        f32x4 v[8];
        unsigned pv = 0;
        TDECL; TS(0);
        sfor<0, 36>([&](auto U) AI {
            constexpr int u = U;
            if constexpr (u >= 4) waitvm<g_wait(u)>();
            if constexpr (g_pre(u - 1)) {
                OPAQ(pv);
                const unsigned need = u == 18 ? 1u : 2u;
                if (__builtin_amdgcn_ballot_w64(pv < need) != 0) poll(wr, fdo + (lane & 3) * 4, need);
            }
            TS(2 * u + 1);
            bar();
            TS(2 * u + 2);
            if constexpr (u == 15 || u == 25) { if (w == 4 && lane == 0) bs4<16>(u == 15 ? 1u : 2u, wr, 0, fdo + nt * 4); }
            if constexpr (g_pre(u)) asm volatile("global_load_dword %0, %1, %2 sc1" : "=v"(pv) : "v"((lane & 3) * 4), "s"(ws + fdo) : "memory");
            auto exr = [&](int q) AI {
                if (P_EX == 0) v[q] = ldsr<f32x4>(ex + q * 1024);
                else { const f32x2 a = ldsr<f32x2>(ex + q * 1024); SB(); const f32x2 b = ldsr<f32x2>(ex + q * 1024 + 512); SB(); v[q] = f32x4{a[0], a[1], b[0], b[1]}; }
            };
            if constexpr (u >= 5 && u <= 19) {
                constexpr int ph = (u - 5) & 3;
                if constexpr (ph == 0) { exr(0); exr(1); exr(2); exr(3); }
                if constexpr (ph == 1 || ph == 2) { exr(2 + 2 * ph); exr(3 + 2 * ph); }
                if constexpr (u == 19) { exr(0); exr(1); exr(2); exr(3); }
            }
            auto dm = [&](int i) AI { if (i < g_nd(u)) { if (u + 5 < 18) dmaD(u + 5, i); else dmaE(u + 5, i); } else if (i < g_nd(u) + g_nx(u)) dmaX(g_x0(u) + i - g_nd(u)); SB(); };
            if constexpr (g_half(u) < 0 || P_IL == 0) {
#pragma unroll
                for (int i = 0; i < 8; ++i) dm(i);
            }
            if constexpr (u == 4) {
                float sq = 0;
#pragma unroll
                for (int i = 0; i < 18; ++i) { OPAQ(sqv[i]); sq += __uint_as_float(sqv[i]); }
                float t = sq; swap32f(sq, t);
                sg = sqrtf((sq + t) * RD576 + EPS);
                const float rstd = 1.0f / sg;
                k0 = GC0 * rstd; k1 = GC1 * rstd * rstd * rstd;
            }
            if constexpr (g_half(u) >= 0) {
                constexpr int hf = g_half(u), tile = hf >> 1;
                float t[8], x[8];
#pragma unroll
                for (int j = 0; j < 8; ++j) x[j] = v[2 * (hf & 3) + (j >> 2)][j & 3];
                if (P_IL) dm(0);
#pragma unroll
                for (int j = 0; j < 8; ++j) { t[j] = x[j] * x[j]; OPAQ(t[j]); }
#pragma unroll
                for (int j = 0; j < 8; ++j) { t[j] = __builtin_fmaf(t[j], k1, k0); OPAQ(t[j]); }
                if (P_IL) { SB(); dm(1); dm(5); }
#pragma unroll
                for (int j = 0; j < 8; ++j) { t[j] = t[j] * x[j]; OPAQ(t[j]); }
#pragma unroll
                for (int j = 0; j < 4; ++j) { t[j] = __builtin_amdgcn_exp2f(t[j]); OPAQ(t[j]); }
                if (P_IL) { SB(); dm(2); dm(6); }
#pragma unroll
                for (int j = 4; j < 8; ++j) { t[j] = __builtin_amdgcn_exp2f(t[j]); OPAQ(t[j]); }
#pragma unroll
                for (int j = 0; j < 8; ++j) { t[j] = __builtin_fmaf(t[j], sg, sg); OPAQ(t[j]); }
                if (P_IL) { SB(); dm(3); dm(7); }
#pragma unroll
                for (int j = 0; j < 8; ++j) { t[j] = __builtin_amdgcn_rcpf(t[j]); OPAQ(t[j]); }
                if (P_IL) { SB(); dm(4); }
#pragma unroll
                for (int j = 0; j < 8; ++j) { t[j] = t[j] * x[j]; OPAQ(t[j]); }
                bs16<XA>(u32x4{pk2(t[0], t[1]), pk2(t[2], t[3]), pk2(t[4], t[5]), pk2(t[6], t[7])}, wr, hvo + (hf & 1) * 512, (tile < 8 ? kb[0] + tile : kq[0]) * 1024);
                SB();
            }
        });
        TDUMP(2, 32);
    }
    bar();
    if (w == 0 && lane == 0) bs4<16>(1u, wr, 0, F_E + rt * 128 + fc * 4);
    STAMP(2, 3);
    {
        const rsrc_t orr = mkrs(out, 2048 * 1152);
        u32x4 P[4], R[2], Q[16], Re[2];
#pragma unroll
        for (int i = 0; i < 2; ++i) R[i] = bl16<16>(wr, l16, O_R + ((rb * 36 + 8 * nt + w) * 2 + i) * 1024);
        if (w == 7) {
#pragma unroll
            for (int i = 0; i < 2; ++i) Re[i] = bl16<16>(wr, l16, O_R + ((rb * 36 + 32 + nt) * 2 + i) * 1024);
        }
        poll(wr, F_E + rt * 128 + lj * 4, (w == 7 || (lj & 3) == nt) ? 1u : 0u);
        STAMP(2, 4);
#pragma unroll
        for (int i = 0; i < 4; ++i) P[i] = bl16<XA>(wr, l16, O_PB + ((rb * 32 + 8 * nt + w) * 4 + i) * 1024);
        if (w == 7) {
#pragma unroll
            for (int i = 0; i < 16; ++i) Q[i] = bl16<XA>(wr, l16, O_PX + ((rb * 4 + nt) * 16 + i) * 1024);
        }
        auto fin = [&](u32x4* p, int n, u32x4* r, int tile) AI {
#pragma unroll
            for (int mt = 0; mt < 2; ++mt) {
                float a[4] = {0, 0, 0, 0};
                for (int i = 0; i < n; ++i) { a[0] += bflo(p[i][2 * mt]); a[1] += bfhi(p[i][2 * mt]); a[2] += bflo(p[i][2 * mt + 1]); a[3] += bfhi(p[i][2 * mt + 1]); }
                bs8<0>(u32x2{pk2(a[0] + __uint_as_float(r[mt][0]), a[1] + __uint_as_float(r[mt][1])), pk2(a[2] + __uint_as_float(r[mt][2]), a[3] + __uint_as_float(r[mt][3]))},
                       orr, lj * 1152 + lq * 8, (32 * rb + 16 * mt) * 1152 + tile * 32);
            }
        };
        fin(P, 4, R, 8 * nt + w);
        if (w == 7) fin(Q, 16, Re, 32 + nt);
    }
    STAMP(2, 7);
}

extern "C" void kernel_launch(void* const* d_in, const int* in_sizes, int n_in, void* d_out, int out_size, void* d_ws, size_t ws_size, hipStream_t stream) {
    char* ws = (char*)d_ws;
    (void)hipFuncSetAttribute((const void*)k1, hipFuncAttributeMaxDynamicSharedMemorySize, K1_LDS);
    (void)hipFuncSetAttribute((const void*)k2, hipFuncAttributeMaxDynamicSharedMemorySize, K2_LDS);
    k1<<<256, 512, K1_LDS, stream>>>((const char*)d_in[0], (const char*)d_in[1], (const char*)d_in[2], (const char*)d_in[3], (const char*)d_in[4], (const u16*)d_in[5],
                                     (const char*)d_in[7], (const char*)d_in[8], ws);
    k2<<<256, 512, K2_LDS, stream>>>((const char*)d_in[9], (const char*)d_in[10], ws, (char*)d_out);
}
```

```cpp
#include <hip/hip_runtime.h>
#include <stdint.h>
#include <utility>
#define LAS __attribute__((address_space(3)))
typedef short s16x4 __attribute__((ext_vector_type(4)));
typedef __bf16 bf16x8 __attribute__((ext_vector_type(8)));
typedef __bf16 bf16x2 __attribute__((ext_vector_type(2)));
typedef float f32x16 __attribute__((ext_vector_type(16)));
typedef float f32x4 __attribute__((ext_vector_type(4)));
typedef float f32x2 __attribute__((ext_vector_type(2)));
typedef unsigned u32x4 __attribute__((ext_vector_type(4)));
typedef unsigned u32x2 __attribute__((ext_vector_type(2)));
typedef unsigned long long u64;
typedef unsigned short u16;
typedef __amdgpu_buffer_rsrc_t rsrc_t;
#define DEVI __device__ __forceinline__
#define AI __attribute__((always_inline))
#define SB() __builtin_amdgcn_sched_barrier(0)
#define OPAQ(x) asm volatile("" : "+v"(x))

DEVI unsigned pk2(float a, float b) { f32x2 p = {a, b}; return __builtin_bit_cast(unsigned, __builtin_convertvector(p, bf16x2)); }
DEVI float bflo(unsigned x) { return __uint_as_float(x << 16); }
DEVI float bfhi(unsigned x) { return __uint_as_float(x & 0xffff0000u); }
DEVI bf16x8 asb(u32x4 x) { return __builtin_bit_cast(bf16x8, x); }
constexpr float GC0 = -2.0f * 0.7978845608028654f * 1.4426950408889634f, GC1 = GC0 * 0.044715f;
DEVI float gelu(float x) {
    float e = __builtin_amdgcn_exp2f(x * __builtin_fmaf(x * x, GC1, GC0));
    return x * __builtin_amdgcn_rcpf(1.0f + e);
}
DEVI void dma16(unsigned ldsaddr, const void* sbase, unsigned voff) {
    asm volatile("s_mov_b32 m0, %0\n\ts_nop 0\n\tglobal_load_lds_dwordx4 %1, %2" :: "s"(ldsaddr), "v"(voff), "s"(sbase) : "memory");
}
DEVI void dma16h(unsigned ldsaddr, const void* sbase, unsigned voff) {
    asm volatile("s_mov_b32 m0, %0\n\ts_mov_b32 exec_hi, 0\n\tglobal_load_lds_dwordx4 %1, %2\n\ts_mov_b32 exec_hi, -1" :: "s"(ldsaddr), "v"(voff), "s"(sbase) : "memory");
}
template <int N> DEVI void waitvm() { asm volatile("s_waitcnt vmcnt(%0)" :: "i"(N) : "memory"); }
DEVI void bar() { asm volatile("s_waitcnt lgkmcnt(0)\n\ts_barrier" ::: "memory"); }
DEVI bf16x8 trfrag(unsigned a0, unsigned a1) {
    s16x4 a = __builtin_amdgcn_ds_read_tr16_b64_v4i16((LAS s16x4*)(uintptr_t)a0);
    s16x4 b = __builtin_amdgcn_ds_read_tr16_b64_v4i16((LAS s16x4*)(uintptr_t)a1);
    return __builtin_bit_cast(bf16x8, __builtin_shufflevector(a, b, 0, 1, 2, 3, 4, 5, 6, 7));
}
template <int B, class F, int... I> DEVI void sfor_impl(F&& f, std::integer_sequence<int, I...>) { (f(std::integral_constant<int, B + I>{}), ...); }
template <int B, int E, class F> DEVI void sfor(F&& f) { sfor_impl<B>(f, std::make_integer_sequence<int, E - B>{}); }
template <class T> DEVI T ldsr(unsigned a) { return *(LAS T*)(uintptr_t)a; }
template <class T> DEVI void ldsw(unsigned a, T v) { *(LAS T*)(uintptr_t)a = v; }
DEVI rsrc_t mkrs(const void* p, unsigned bytes) { return __builtin_amdgcn_make_buffer_rsrc((void*)p, 0, bytes, 0x00020000); }
template <int AUX> DEVI u32x4 bl16(rsrc_t r, unsigned vo, unsigned so) { return __builtin_amdgcn_raw_buffer_load_b128(r, vo, so, AUX); }
template <int AUX> DEVI u32x2 bl8(rsrc_t r, unsigned vo, unsigned so) { return __builtin_amdgcn_raw_buffer_load_b64(r, vo, so, AUX); }
template <int AUX> DEVI unsigned bl4(rsrc_t r, unsigned vo, unsigned so) { return __builtin_amdgcn_raw_buffer_load_b32(r, vo, so, AUX); }
template <int AUX> DEVI void bs16(u32x4 v, rsrc_t r, unsigned vo, unsigned so) { __builtin_amdgcn_raw_buffer_store_b128(v, r, vo + so, 0, AUX); }
template <int AUX> DEVI void bs8(u32x2 v, rsrc_t r, unsigned vo, unsigned so) { __builtin_amdgcn_raw_buffer_store_b64(v, r, vo, so, AUX); }
template <int AUX> DEVI void bs4(unsigned v, rsrc_t r, unsigned vo, unsigned so) { __builtin_amdgcn_raw_buffer_store_b32(v, r, vo, so, AUX); }
DEVI void swap16(unsigned& a, unsigned& b) { auto r = __builtin_amdgcn_permlane16_swap(a, b, false, false); a = r[0]; b = r[1]; }
DEVI void swap32(unsigned& a, unsigned& b) { auto r = __builtin_amdgcn_permlane32_swap(a, b, false, false); a = r[0]; b = r[1]; }
DEVI void swap16f(float& a, float& b) { unsigned x = __float_as_uint(a), y = __float_as_uint(b); swap16(x, y); a = __uint_as_float(x); b = __uint_as_float(y); }
DEVI void swap32f(float& a, float& b) { unsigned x = __float_as_uint(a), y = __float_as_uint(b); swap32(x, y); a = __uint_as_float(x); b = __uint_as_float(y); }
template <int CTRL> DEVI float dppf(float x) { return __uint_as_float(__builtin_amdgcn_update_dpp(0u, __float_as_uint(x), CTRL, 0xf, 0xf, false)); }
DEVI float rowsum16(float x) { x += dppf<0x128>(x); x += dppf<0x124>(x); x += dppf<0x122>(x); x += dppf<0x121>(x); return x; }
DEVI f32x4 mfma16(bf16x8 a, bf16x8 b, f32x4 c) { return __builtin_amdgcn_mfma_f32_16x16x32_bf16(a, b, c, 0, 0, 0); }

constexpr unsigned CLSZ = 64 * 36 * 1024;
constexpr unsigned GTS = 2056 * 32;
constexpr unsigned O_FLAG = 0, O_YB = 65536, O_XT = O_YB + CLSZ, O_XS = O_XT + CLSZ, O_W1T = O_XS + 16384, O_W2T = O_W1T + 663552,
                   O_CSP = O_W2T + 663552, O_W2M = O_CSP + 41472, O_GB = O_W2M + 2304, O_HT = O_GB + 36 * GTS, O_HS = O_HT + CLSZ,
                   O_XB = O_HS + 65536, O_SQ = O_XB + CLSZ, O_R = O_SQ + 36 * 2048 * 4, O_HB = O_R + 2 * CLSZ, O_PB = O_HB + 128 * 144 * 1024,
                   O_PX = O_PB + 64 * 32 * 4 * 1024, O_END = O_PX + 64 * 4 * 16 * 1024, O_DBG = O_END;
constexpr float EPS = 1e-5f, RD576 = 1.0f / 576.0f;
#define XA 16
#define TDECL
#define BDECL
#define BT(i) do {} while (0)
#define BDUMP(kid) do {} while (0)
#define TS(i) do {} while (0)
#define TDUMP(kid, n) do {} while (0)
#define STAMP(kid, i) do {} while (0)
#define CSTAMP(kid, i) do {} while (0)

#define P_KPF 1
constexpr int K1_KC = 0, K1_UR = 33792, K1_P = K1_UR + 18432, K1_LDS = K1_P + 32768;
__global__ __launch_bounds__(512, 2) void k1(const char* __restrict__ xq, const char* __restrict__ kk, const char* __restrict__ vv, const char* __restrict__ shk,
                                             const char* __restrict__ ssk, const u16* __restrict__ ssd, const char* __restrict__ w1, const char* __restrict__ w2,
                                             const char* __restrict__ w3, const char* __restrict__ w4, char* __restrict__ ws) {
    extern __shared__ __attribute__((aligned(256))) char smem[];
    const unsigned lb = (unsigned)(uintptr_t)(LAS char*)smem;
    const int wg = blockIdx.x, xcd = wg & 7, loc = wg >> 3, tid = threadIdx.x, lane = tid & 63;
    const int w = __builtin_amdgcn_readfirstlane(tid >> 6);
    const rsrc_t wr = mkrs(ws, O_END);
    STAMP(0, 0);
    if (loc < 18) {
        const int cw = 18 * xcd + loc;
        const int kch = tid >> 7, tau = tid & 127;
        const rsrc_t sr = mkrs(ssk, 576 * 4096);
        const unsigned so = (4 * cw + kch) * 4096 + tau * 32;
        u32x4 K0 = bl16<0>(sr, so, 0), K1 = bl16<0>(sr, so + 16, 0);
        unsigned K8 = bl4<0>(sr, so + 32, 0);
        const float dv = bflo(ssd[4 * cw + kch]);
        const rsrc_t kr = mkrs(kk, 2048 * 1152), vr = mkrs(vv, 2048 * 1152);
        u32x2 ks[4], vs[4];
        auto strip = [&](int g) AI { SB(); ks[g] = bl8<0>(kr, tid * 1152 + cw * 8, g * 512 * 1152); SB(); vs[g] = bl8<0>(vr, tid * 1152 + cw * 8, g * 512 * 1152); SB(); };
        strip(0);
        asm volatile("s_barrier" ::: "memory");
        strip(1);
        u64 halo[4][3];
#pragma unroll
        for (int g = 0; g < 4; ++g)
#pragma unroll
            for (int i = 0; i < 3; ++i) { int r = 512 * g + 64 * w - 1 - i; u64 t = *(const u64*)(kk + (size_t)(r < 0 ? 0 : r) * 1152 + cw * 8); halo[g][i] = r < 0 ? 0ull : t; }
        const u32x4 sa = *(const u32x4*)(shk + cw * 32), sb = *(const u32x4*)(shk + cw * 32 + 16);
        if (tid < 120) ldsw<u32x4>(lb + K1_UR + (tid / 30) * 4608 + 4096 + (tid % 30) * 16, u32x4{0, 0, 0, 0});
        if (tau == 0) K0[0] = (K0[0] & 0xffff0000u) | (pk2(bflo(K0[0]) + dv, 0.f) & 0xffffu);
        {
            const unsigned c0 = lb + K1_KC + kch * 8448 + 32 + tau * 32, c1 = c0 + 4224;
            ldsw<u32x4>(c0, K0); ldsw<u32x4>(c0 + 16, K1);
            ldsw<u32x4>(c1, u32x4{__builtin_amdgcn_alignbit(K0[1], K0[0], 16), __builtin_amdgcn_alignbit(K0[2], K0[1], 16), __builtin_amdgcn_alignbit(K0[3], K0[2], 16), __builtin_amdgcn_alignbit(K1[0], K0[3], 16)});
            ldsw<u32x4>(c1 + 16, u32x4{__builtin_amdgcn_alignbit(K1[1], K1[0], 16), __builtin_amdgcn_alignbit(K1[2], K1[1], 16), __builtin_amdgcn_alignbit(K1[3], K1[2], 16), __builtin_amdgcn_alignbit(K8, K1[3], 16)});
            if (tau == 0) {
                ldsw<u32x4>(c0 - 32, u32x4{0, 0, 0, 0}); ldsw<u32x4>(c0 - 16, u32x4{0, 0, 0, 0});
                ldsw<u32x4>(c1 - 32, u32x4{0, 0, 0, 0}); ldsw<u32x4>(c1 - 16, u32x4{0, 0, 0, K0[0] << 16});
            }
        }
        SB();
        bar();
        strip(2);
        STAMP(0, 1);
        const int ch = w & 3, hf = w >> 2, li = lane & 15, q = lane >> 4;
        u32x4 A[4][8];
        {
            const int r = (li + 1) & 1;
            unsigned aa = lb + K1_KC + ch * 8448 + r * 4224 + (128 * hf + li + 25 - 8 * q - r) * 2;
            OPAQ(aa);
#pragma unroll
            for (int c0 = 0; c0 < 4; ++c0)
#pragma unroll
                for (int c1 = 0; c1 < 8; ++c1) {
                    const unsigned a = aa + c1 * 512 + c0 * 64;
                    A[c0][c1] = u32x4{ldsr<unsigned>(a), ldsr<unsigned>(a + 4), ldsr<unsigned>(a + 8), ldsr<unsigned>(a + 12)};
                }
        }
        strip(3);
        f32x4 acc[8];
#pragma unroll
        for (int t = 0; t < 8; ++t) acc[t] = f32x4{0, 0, 0, 0};
        unsigned ba = lb + K1_UR + ch * 4608 + (2056 - 1792 - 16 * li + 128 * hf - 8 * q) * 2;
        OPAQ(ba);
        const unsigned ua = lb + K1_UR + (511 - tid) * 2;
        const unsigned pw = lb + K1_P + (hf * 4 + ch) * 2048 + li * 64 + q * 16;
        const unsigned pr = lb + K1_P + ((tid >> 4) * 32 + (tid & 15)) * 4;
        auto output = [&](int g) AI {
            if (tid < 256) {
                float y[2][4];
#pragma unroll
                for (int c = 0; c < 4; ++c)
#pragma unroll
                    for (int m = 0; m < 2; ++m) y[m][c] = ldsr<float>(pr + (g & 1) * 16384 + c * 2048 + m * 64) + ldsr<float>(pr + (g & 1) * 16384 + 8192 + c * 2048 + m * 64);
                bs16<0>(u32x4{pk2(y[0][0], y[0][1]), pk2(y[0][2], y[0][3]), pk2(y[1][0], y[1][1]), pk2(y[1][2], y[1][3])}, wr, (tid >> 4) * 36864 + (tid & 15) * 16, O_YB + g * 16 * 36864 + cw * 256);
            }
        };
        sfor<0, 4>([&](auto G) AI {
            constexpr int g = G;
            unsigned kx[4][2];
            kx[0][0] = ks[g][0]; kx[0][1] = ks[g][1];
#pragma unroll
            for (int i = 1; i < 4; ++i) {
                kx[i][0] = __builtin_amdgcn_update_dpp((unsigned)halo[g][i - 1], kx[i - 1][0], 0x138, 0xf, 0xf, false);
                kx[i][1] = __builtin_amdgcn_update_dpp((unsigned)(halo[g][i - 1] >> 32), kx[i - 1][1], 0x138, 0xf, 0xf, false);
            }
            float s0 = bflo(kx[0][0]) * bflo(sa[0]) + bflo(kx[1][0]) * bfhi(sa[0]) + bflo(kx[2][0]) * bflo(sa[1]) + bflo(kx[3][0]) * bfhi(sa[1]);
            float s1 = bfhi(kx[0][0]) * bflo(sa[2]) + bfhi(kx[1][0]) * bfhi(sa[2]) + bfhi(kx[2][0]) * bflo(sa[3]) + bfhi(kx[3][0]) * bfhi(sa[3]);
            float s2 = bflo(kx[0][1]) * bflo(sb[0]) + bflo(kx[1][1]) * bfhi(sb[0]) + bflo(kx[2][1]) * bflo(sb[1]) + bflo(kx[3][1]) * bfhi(sb[1]);
            float s3 = bfhi(kx[0][1]) * bflo(sb[2]) + bfhi(kx[1][1]) * bfhi(sb[2]) + bfhi(kx[2][1]) * bflo(sb[3]) + bfhi(kx[3][1]) * bfhi(sb[3]);
            const unsigned p01 = pk2(s0 * bflo(vs[g][0]), s1 * bfhi(vs[g][0])), p23 = pk2(s2 * bflo(vs[g][1]), s3 * bfhi(vs[g][1]));
            const unsigned u = ua + (3 - g) * 1024;
            ldsw<u16>(u, (u16)p01); ldsw<u16>(u + 4608, (u16)(p01 >> 16)); ldsw<u16>(u + 9216, (u16)p23); ldsw<u16>(u + 13824, (u16)(p23 >> 16));
            SB();
            bar();
            u32x4 B[8];
#pragma unroll
            for (int i = 0; i < 8; ++i) B[i] = ldsr<u32x4>(ba + (7 - 2 * g - (i >> 2)) * 512 + (i & 3) * 64);
            SB();
            if constexpr (g >= 1) output(g - 1);
            SB();
#pragma unroll
            for (int i = 0; i < 8; ++i) {
                const int b = 2 * g + (i >> 2);
#pragma unroll
                for (int c1 = 0; c1 + b < 8; ++c1) acc[b + c1] = mfma16(asb(A[i & 3][c1]), asb(B[i]), acc[b + c1]);
            }
            SB();
            ldsw<f32x4>(pw + (g & 1) * 16384, acc[2 * g]); ldsw<f32x4>(pw + (g & 1) * 16384 + 1024, acc[2 * g + 1]);
            STAMP(0, 2 + g);
        });
        bar();
        output(3);
    } else {
        const int u = (loc - 18) * 8 + w;
        if (u < 32 || (u >= 73 && u < 82)) {
            const bool isx = u < 32;
            const int rl = lane >> 4, c = lane & 15;
            const int rb = 32 * (xcd >> 2) + 4 * (u >> 2) + (xcd & 3), rho = 4 * (u & 3) + rl;
            const int r0 = isx ? 32 * rb + rho : 8 * (9 * xcd + u - 73) + rl, r1 = r0 + (isx ? 16 : 4);
            const rsrc_t xr = mkrs(isx ? xq : w2, isx ? 2048 * 1152 : 576 * 1152);
            u32x2 a[9], b[9];
#pragma unroll
            for (int m = 0; m < 9; ++m) { a[m] = bl8<0>(xr, r0 * 1152 + c * 8, m * 128); b[m] = bl8<0>(xr, r1 * 1152 + c * 8, m * 128); }
            float sa = 0, sb = 0;
#pragma unroll
            for (int m = 0; m < 9; ++m) {
                sa += (bflo(a[m][0]) + bfhi(a[m][0])) + (bflo(a[m][1]) + bfhi(a[m][1]));
                sb += (bflo(b[m][0]) + bfhi(b[m][0])) + (bflo(b[m][1]) + bfhi(b[m][1]));
                if (isx) bs16<0>(u32x4{a[m][0], a[m][1], b[m][0], b[m][1]}, wr, c * 256 + rho * 16, O_XT + rb * 36864 + m * 4096);
            }
            const float ma = rowsum16(sa) * RD576, mb = rowsum16(sb) * RD576;
            if (isx) {
                float qa = 0, qb = 0;
#pragma unroll
                for (int m = 0; m < 9; ++m)
#pragma unroll
                    for (int i = 0; i < 2; ++i) {
                        float t;
                        t = bflo(a[m][i]) - ma; qa += t * t; t = bfhi(a[m][i]) - ma; qa += t * t;
                        t = bflo(b[m][i]) - mb; qb += t * t; t = bfhi(b[m][i]) - mb; qb += t * t;
                    }
                const float ra = 1.0f / sqrtf(rowsum16(qa) * RD576 + EPS), rbb = 1.0f / sqrtf(rowsum16(qb) * RD576 + EPS);
                if (c == 0) { bs8<0>(u32x2{__float_as_uint(ra), __float_as_uint(-ma * ra)}, wr, r0 * 8, O_XS); bs8<0>(u32x2{__float_as_uint(rbb), __float_as_uint(-mb * rbb)}, wr, r1 * 8, O_XS); }
            } else if (c < 2) {
                const int k = c ? r1 : r0; const float m = c ? mb : ma;
                const int p = (k >> 3) * 32 + (k & 7) * 2;
                const unsigned hi = pk2(m, 0.f) & 0xffffu, lo = pk2(m - bflo(hi), 0.f) & 0xffffu;
                *(u16*)(ws + O_W2M + p) = (u16)hi; *(u16*)(ws + O_W2M + 16 + p) = (u16)lo;
            }
        } else if (u < 73) {
            const int g = 41 * xcd + u - 32;
            if (g < 324) {
                const int mat = g >= 162, r = g - 162 * mat, ks = r / 9, cg = r - 9 * ks;
                const rsrc_t mr = mkrs(mat ? w2 : w1, 576 * 1152);
                u32x4 t[4];
#pragma unroll
                for (int i = 0; i < 4; ++i) t[i] = bl16<0>(mr, (lane >> 3) * 1152 + (lane & 7) * 16, (32 * ks + 8 * i) * 1152 + cg * 128);
                const unsigned lw = lb + w * 4096;
#pragma unroll
                for (int i = 0; i < 4; ++i) ldsw<u32x4>(lw + i * 1024 + lane * 16, t[i]);
                const unsigned ta = lw + ((mat ? 8 : 4) * (lane >> 4) + ((lane & 15) >> 2)) * 128 + (lane & 3) * 8, tb = ta + (mat ? 512 : 2048);
                const u32x4 ones = {0x3f803f80u, 0x3f803f80u, 0x3f803f80u, 0x3f803f80u};
#pragma unroll
                for (int tt = 0; tt < 4; ++tt) {
                    const bf16x8 f = trfrag(ta + tt * 32, tb + tt * 32);
                    bs16<0>(__builtin_bit_cast(u32x4, f), wr, lane * 16, (mat ? O_W2T : O_W1T) + ((4 * cg + tt) * 18 + ks) * 1024);
                    if (!mat) {
                        const f32x4 cs = mfma16(f, asb(ones), f32x4{0, 0, 0, 0});
                        if ((lane & 15) == 0) bs16<0>(__builtin_bit_cast(u32x4, cs), wr, (lane >> 4) * 16, O_CSP + ((4 * cg + tt) * 18 + ks) * 64);
                    }
                }
            }
        } else if (u >= 84) {
            const rsrc_t r3 = mkrs(w3, 576 * 9216), r4 = mkrs(w4, 4608 * 1152);
            for (int i = 0; i < P_KPF; ++i) asm volatile("s_sleep 16");
            unsigned t[12], a = 0;
#pragma unroll
            for (int i = 0; i < 6; ++i) {
                const unsigned l = (i * 28 + u - 84) * 64 + lane, row = l / 18;
                t[i] = bl4<0>(r3, row * 9216 + (l - row * 18) * 128 + (xcd & 3) * 2304, 0);
            }
#pragma unroll
            for (int i = 0; i < 6; ++i) t[6 + i] = bl4<0>(r4, ((i * 28 + u - 84) * 64 + lane) * 128 + (xcd & 3) * (1152 * 1152), 0);
#pragma unroll
            for (int i = 0; i < 12; ++i) a ^= t[i];
            asm volatile("" :: "v"(a));
        } else if (u == 83) {
            bs16<0>(u32x4{0, 0, 0, 0}, wr, lane * 16, O_FLAG + xcd * 2048); bs16<0>(u32x4{0, 0, 0, 0}, wr, lane * 16, O_FLAG + xcd * 2048 + 1024);
        } else if (u == 82) {
            const int l = 9 * xcd + (lane >> 3);
            bs16<0>(u32x4{0, 0, 0, 0}, wr, (l >> 1) * GTS + (l & 1) * (2052 * 32) + (lane & 7) * 16, O_GB);
            const int l2 = 9 * xcd + 8;
            if (lane < 8) bs16<0>(u32x4{0, 0, 0, 0}, wr, (l2 >> 1) * GTS + (l2 & 1) * (2052 * 32) + lane * 16, O_GB);
        }
    }
    STAMP(0, 7);
}

constexpr int SLOT = 18432, NSL = 6, L_EX = NSL * SLOT, L_F = L_EX, L_ST = L_EX + 36864, L_MN = L_ST + 2048, K2_LDS = L_MN + 1024, L_PE = 5 * SLOT;
constexpr unsigned F_B = O_FLAG, F_C = O_FLAG + 66 * 128, F_D = F_C + 16 * 128, F_E = F_D + 16 * 128;
#define P_RD 4
#define P_B0 0
#define P_WBS {0, 2, 5, 8, 11, 14}
#define P_KB 8
#define P_KC 3
#define P_W2A 6
#define P_W2B 6
#define P_W2C 6
#define P_PRA 3
#define P_PRB 4
#define P_X0 10
#define P_PRIO 2
constexpr int RD = P_RD, BP = RD + 1, HD = 8;
constexpr int SPIN = 1 << 17;

template <class HK> DEVI void gemm_small(const u32x4* W, unsigned fa, f32x4* acc, HK&& hook) {
    constexpr int PF = 3;
    u32x4 b[PF + 1][2];
    sfor<0, 18 + PF>([&](auto K) AI {
        constexpr int k = K, m = k - PF;
        if constexpr (m >= 0) {
            const f32x4 z = {0, 0, 0, 0};
            acc[0] = mfma16(asb(W[m]), asb(b[m % (PF + 1)][0]), m ? acc[0] : z); acc[1] = mfma16(asb(W[m]), asb(b[m % (PF + 1)][1]), m ? acc[1] : z);
            SB();
        }
        if constexpr (k < 18) { b[k % (PF + 1)][0] = ldsr<u32x4>(fa + k * 2048); b[k % (PF + 1)][1] = ldsr<u32x4>(fa + k * 2048 + 1024); SB(); }
        if constexpr (m >= 0) { hook(std::integral_constant<int, m>{}); SB(); }
    });
}
DEVI void poll(rsrc_t wr, unsigned off, unsigned need) {
#pragma unroll 1
    for (int i = 0; i < SPIN; ++i) {
        const unsigned v = bl4<16>(wr, off, 0);
        if (__builtin_amdgcn_ballot_w64(v < need) == 0) break;
        asm volatile("s_sleep 1" ::: "memory");
    }
    asm volatile("" ::: "memory");
}
constexpr int e_mem(int j) { return j < 4 ? 0 : j < 13 ? 1 + (j - 4) / 3 : j < 16 ? 0 : j < 25 ? 1 + (j - 16) / 3 : j < 27 ? 0 : j < 33 ? 1 + (j - 27) / 2 : j - 32; }
constexpr int e_til(int j) { return j < 4 ? j : j < 13 ? (j - 4) % 3 : j < 16 ? j - 9 : j < 25 ? 3 + (j - 16) % 3 : j < 27 ? j - 18 : j < 33 ? 6 + (j - 27) % 2 : 8; }
constexpr int e_first(int j) { int f = 0; for (int i = 0; i < j; ++i) f += e_mem(i) == 3 ? 12 : 8; return f; }
#define P_FB {12, 18, 24, 28}
#define P_QB {17, 23, 29, 32}
#define P_SD 2
#define P_TP 1
constexpr int FB[4] = P_FB, QB[4] = P_QB, LH[4] = {5, 11, 15, 17}, LJ[5] = {4, 16, 27, 33, 99};
constexpr int f_lvl(int u) { for (int l = 0; l < 4; ++l) if (FB[l] == u) return l + 1; return 0; }
constexpr int q_lvl(int u) { for (int l = 0; l < 4; ++l) if (QB[l] == u) return l + 1; return 0; }
constexpr signed char ACT_D[18][4] = {{0,1,8,-1},{16,9,24,-1},{32,17,40,-1},{25,2,48,10},{33,18,41,-1},{26,3,49,11},{34,19,42,-1},{27,4,50,12},{35,20,43,-1},{28,5,51,13},
                                      {36,21,44,-1},{29,6,52,14},{37,22,45,-1},{30,7,53,15},{38,23,46,-1},{31,54,56,-1},{39,57,47,-1},{55,58,59,64}};
constexpr int NE = 33;
constexpr signed char ACT_E[NE][2] = {{0,8},{16,1},{24,9},{32,17},{40,2},{25,48},{33,10},{41,18},{26,49},{56,34},{42,3},{50,11},{19,4},{27,12},{35,20},{43,5},{28,51},{57,36},{44,13},{52,21},{29,6},{37,14},{45,22},{30,53},{58,38},{46,7},{54,15},{23,-1},{31,-1},{39,-1},{47,-1},{55,59},{64,-1}};
constexpr int b_slot(int u) { return u < 18 ? 18 * u + 1 : 324 + e_first(2 * (u - 18)) + 1; }
constexpr int mx(int a, int b) { return a > b ? a : b; }
constexpr int h_pos(int j, int mt) {
    int p = j < 8 ? 290 + 4 * j + 2 * mt : 324 + e_first(j - 7) + 2 + 2 * mt;
    if (e_mem(j) == 0) { if (j == 26) p = mx(p, b_slot(FB[3]) + 1 + 2 * mt); return p; }
    for (int l = 0; l < 4; ++l) if (j >= LJ[l] && j < LJ[l + 1]) p = mx(p, b_slot(QB[l]) + 1 + 2 * (j - LJ[l]) + mt);
    return p;
}
constexpr int s_pos(int h) { return h < 14 ? 90 + 18 * h : 325 + NE * (h - 13) - 1; }
constexpr int m_wait(int h, int u) {
    int n = 0;
    for (int i = h + 1; i < 18; ++i) n += s_pos(i) < b_slot(u);
    for (int j = 0; j < 36; ++j) for (int mt = 0; mt < 2; ++mt) n += h_pos(j, mt) >= s_pos(h) && h_pos(j, mt) < b_slot(u);
    return n;
}
constexpr int x_at(int s) { return (!(s & 1) && P_X0 + s / 2 < 36) ? P_X0 + s / 2 : -1; }
struct HTab { int at[2][648], st[330], fi[37], vm[648]; bool ok;
    constexpr HTab() : at{}, st{}, fi{}, vm{}, ok(true) {
        int cnt[649] = {}, pre[649] = {}, done[649] = {};
        for (int s = 0; s < 648; ++s) { at[0][s] = at[1][s] = -1; vm[s] = 63; }
        for (int j = 0; j < 37; ++j) fi[j] = e_first(j);
        for (int h = 0; h < 18; ++h) ++cnt[s_pos(h)];
        for (int l = 0; l < 72; ++l) { const int p = h_pos(l >> 1, l & 1); ++cnt[p]; if (at[0][p] < 0) at[0][p] = l; else if (at[1][p] < 0) at[1][p] = l; else ok = false; if (p >= 323 + fi[l >> 1]) ok = false; }
        for (int s = 0; s < 648; ++s) pre[s + 1] = pre[s] + cnt[s];
        for (int e = 0, j = 0; e < 330; ++e) { while (j < 35 && fi[j + 1] <= e) ++j; st[e] = j; }
        for (int j = 0; j < 36; ++j) {
            const int p = h_pos(j, 1), s = 323 + fi[j];
            if (p < h_pos(j, 0)) ok = false;
            const int n = pre[s] - pre[p + 1] + (at[0][p] == 2 * j + 1 && at[1][p] >= 0 ? 1 : 0);
            if (n >= 63) ok = false;
            vm[s] = n; done[s] = pre[s] - n;
        }
        for (int l = 0; l < 4; ++l) { const int s = b_slot(FB[l]); done[s] = pre[s] - m_wait(LH[l], FB[l]); if (s_pos(LH[l]) >= s || FB[l] + P_SD >= QB[l]) ok = false; }
        for (int s = 1; s < 648; ++s) done[s] = mx(done[s], done[s - 1]);
        for (int j = 0; j < 36; ++j) if (e_mem(j) == 0 && j >= 4 && done[h_pos(j, 0)] <= pre[s_pos(2 * e_til(j) + 1)]) ok = false;
        for (int k = 1; k < 36; ++k) { int n = P_X0; for (int s = 0; s < 2 * k - 1; ++s) n += x_at(s) >= 0; vm[2 * k - 1] = n - 1 - k; if (n <= k) ok = false; }
        vm[289] = pre[289] - pre[s_pos(7)] - 1;
    } };
constexpr HTab HT{};
static_assert(HT.ok, "schedule");
constexpr int wcnt(int vm, int lgkm) { return (vm & 15) | ((vm >> 4) << 14) | 0x70 | (lgkm << 8); }
constexpr int g_nd(int i) { return i + 5 < 18 ? 5 : i + 5 < 36 ? 4 : 0; }
constexpr int g_nx(int i) { return (i >= 4 && i < 13) ? 1 : 0; }
constexpr int g_pre(int i) { return q_lvl(i + P_SD) ? 1 : 0; }
#define P_PF 8
constexpr int g_pf(int i) { return P_PF == 0 ? -1 : (i >= P_PF && i <= P_PF + 12 && !((i - P_PF) & 1)) ? (i - P_PF) / 2 : i == FB[2] ? 7 : i == FB[3] ? 8 : -1; }
constexpr int g_all(int i) { return g_pre(i) + g_nd(i) + g_nx(i) + (g_pf(i) >= 0); }
constexpr int g_wait(int u) {
    int n = g_nx(u - 4) + (g_pf(u - 4) >= 0) + g_all(u - 3) + g_all(u - 2) + g_all(u - 1);
    if (q_lvl(u)) { n = -1; for (int i = 1; i <= P_SD; ++i) n += g_all(u - i); }
    return n;
}

__global__ __launch_bounds__(512, 2) void k2(const char* __restrict__ w3, const char* __restrict__ w4, char* __restrict__ ws, char* __restrict__ out) {
    extern __shared__ __attribute__((aligned(256))) char smem[];
    const unsigned lb = (unsigned)(uintptr_t)(LAS char*)smem;
    const int wg = blockIdx.x, xcd = wg & 7, loc = wg >> 3, lane = threadIdx.x & 63;
    const int kap = xcd & 3, rt = 8 * (xcd >> 2) + (loc >> 2), nt = loc & 3, rb = 4 * rt + kap, fc = 4 * kap + nt;
    const int w = __builtin_amdgcn_readfirstlane(threadIdx.x >> 6), wm = w & 3;
    const int lj = lane & 15, lq = lane >> 4, hh = lane >> 5;
    const unsigned l16 = lane * 16;
    const rsrc_t wr = mkrs(ws, O_END);
    BDECL; BT(0);
    STAMP(1, 0);
    const int a9 = 9 * (fc >> 1), odd = fc & 1;
    const char* wp = w3 + (a9 + 5 * odd) * 128 + (size_t)wm * 36 * 9216;
    const char* wsg = w3 + (a9 + 4) * 128 + odd * 64 + (size_t)wm * 72 * 9216;
    const unsigned pvo = (lane >> 3) * 9216 + (((lane & 7) ^ (((lane >> 4) & 1) << 2)) * 16), svo = (lane >> 2) * 9216 + (lane & 3) * 16;
    auto dmaD = [&](int u, int i) AI {
        const unsigned dst = lb + (u % NSL) * SLOT + wm * 4608 + i * 1024;
        const char* src = u < 16 ? wp + (u >> 2) * 128 + (size_t)((u & 3) * 144 + i * 8) * 9216 : wsg + (size_t)((u & 1) * 288 + i * 16) * 9216;
        if (i < 4) dma16(dst, src, u < 16 ? pvo : svo); else dma16h(dst, src, u < 16 ? pvo : svo);
    };

    {
    constexpr unsigned OOB = 0x80000000u;
    u32x4 Y[5], X[5], W1[21], W2[24];
    int nu[5];
#pragma unroll
    for (int m = 0; m < 4; ++m) nu[m] = ((nt + m) & 3) * 8 + w;
    nu[4] = 32 + (w >> 1);
    const unsigned h16 = l16 + (w & 1) * 512, h16v = lane < 32 ? h16 : OOB, e16 = w < 2 ? l16 : OOB;
    BT(1);
    const u32x2 xs0 = bl8<0>(wr, lj * 8, O_XS + rb * 256), xs1 = bl8<0>(wr, lj * 8, O_XS + rb * 256 + 128);
#pragma unroll
    for (int m = 0; m < 5; ++m) { Y[m] = bl16<0>(wr, m < 4 ? l16 : h16v, O_YB + (rb * 36 + nu[m]) * 1024); X[m] = bl16<0>(wr, m < 4 ? l16 : h16v, O_XT + (rb * 36 + nu[m]) * 1024); SB(); }
    BT(2);
    if (P_B0) asm volatile("s_barrier" ::: "memory");
    BT(3);
    auto ldw = [&](u32x4* W, unsigned base, int k) AI {
        if (k < 0) W[k] = bl16<0>(wr, (lj < 2 && (k < -1 || w < 2)) ? lq * 32 + lj * 16 : OOB, O_W2M + (w + 8 * (k + 3)) * 128);
        else if (k < 3) W[k] = bl16<0>(wr, k < 2 ? l16 : e16, base + ((32 + nt) * 18 + w + 8 * k) * 1024);
        else W[k] = bl16<0>(wr, l16, base + ((8 * nt + w) * 18 + k - 3) * 1024);
        SB();
    };
    auto ldw2 = [&](int k) AI { ldw(W2 + 3, O_W2T, k - 3); };
    auto prime = [&](int i) AI { dmaD(2 * (i / 5) + (w >> 2), i % 5); SB(); };
    f32x2 s[2] = {{0, 0}, {0, 0}}, sq[2] = {{0, 0}, {0, 0}};
    const float rx[2] = {__uint_as_float(xs0[0]), __uint_as_float(xs1[0])}, mx[2] = {__uint_as_float(xs0[1]), __uint_as_float(xs1[1])};
    u32x4 hown, ca, cb, cea, ceb;
    constexpr int WBS[6] = P_WBS;
#pragma unroll
    for (int m = 0; m < 5; ++m) {
#pragma unroll
        for (int k = m ? WBS[m - 1] : 0; k < WBS[m]; ++k) ldw(W1, O_W1T, k);
        BT(4 + 2 * m);
        u32x4 hp;
#pragma unroll
        for (int i = 0; i < 4; ++i) {
            const int mt = i >> 1;
            const f32x2 x = {bflo(X[m][i]), bfhi(X[m][i])}, y = {bflo(Y[m][i]), bfhi(Y[m][i])};
            const f32x2 h = __builtin_elementwise_fma(__builtin_elementwise_fma(x, f32x2{rx[mt], rx[mt]}, f32x2{mx[mt], mx[mt]}), y, x);
            s[mt] += h; sq[mt] = __builtin_elementwise_fma(h, h, sq[mt]);
            hp[i] = pk2(h[0], h[1]);
        }
        OPAQ(s[0]); OPAQ(s[1]); OPAQ(sq[0]); OPAQ(sq[1]);
        const unsigned a = lb + L_F + (nu[m] >> 1) * 2048 + (nu[m] & 1) * 8 + (m < 4 ? l16 : h16);
        if (m < 4 || lane < 32) { ldsw<u32x2>(a, u32x2{hp[0], hp[1]}); ldsw<u32x2>(a + 1024, u32x2{hp[2], hp[3]}); }
        if (m == 0) hown = hp;
        SB();
        BT(5 + 2 * m);
    }
    {
        float s0 = s[0][0] + s[0][1], s1 = s[1][0] + s[1][1], q0 = sq[0][0] + sq[0][1], q1 = sq[1][0] + sq[1][1];
        swap16f(s0, s1); swap16f(q0, q1);
        float t = s0 + s1, u = q0 + q1;
        swap32f(t, u);
        ldsw<float>(lb + L_ST + w * 256 + lane * 4, t + u);
    }
    SB();
    {
        const unsigned co = lj * 64 + lq * 16, co2 = lj < 2 ? co + 1024 : OOB;
        ca = bl16<0>(wr, co, O_CSP + (8 * nt + w) * 1152); cb = bl16<0>(wr, co2, O_CSP + (8 * nt + w) * 1152);
        cea = bl16<0>(wr, w < 2 ? co : OOB, O_CSP + (32 + nt) * 1152); ceb = bl16<0>(wr, w < 2 ? co2 : OOB, O_CSP + (32 + nt) * 1152);
        SB();
    }
#pragma unroll
    for (int k = WBS[4]; k < WBS[5]; ++k) ldw(W1, O_W1T, k);
    bar();
    BT(14);
    f32x4 acc[2];
    unsigned fa = lb + L_F + l16; OPAQ(fa);
    const unsigned fx = fa + w * 2048, fx2 = fa + (16 + (w & 1)) * 2048, pw = lb + L_PE + w * 2048 + l16, pr = lb + L_PE + (w & 1) * 1024 + l16;
    u32x4 xb[3][2];
    auto slices = [&](const u32x4* We) AI {
#pragma unroll
        for (int i = 0; i < 3; ++i)
#pragma unroll
            for (int mt = 0; mt < 2; ++mt) acc[mt] = mfma16(asb(We[i]), asb(xb[i][mt]), i ? acc[mt] : f32x4{0, 0, 0, 0});
    };
    auto xread = [&]() AI {
#pragma unroll
        for (int mt = 0; mt < 2; ++mt) { xb[0][mt] = ldsr<u32x4>(fx + mt * 1024); xb[1][mt] = ldsr<u32x4>(fx + 16384 + mt * 1024); xb[2][mt] = ldsr<u32x4>(fx2 + mt * 1024); }
    };
    auto psum = [&]() AI { f32x4 t[8];
#pragma unroll
        for (int i = 0; i < 8; ++i) t[i] = ldsr<f32x4>(pr + i * 2048);
        return ((t[0] + t[1]) + (t[2] + t[3])) + ((t[4] + t[5]) + (t[6] + t[7])); };
    auto gelu4 = [&](f32x4 z) AI {
        const f32x4 u = z * __builtin_elementwise_fma(z * z, f32x4{GC1, GC1, GC1, GC1}, f32x4{GC0, GC0, GC0, GC0});
        f32x4 e;
#pragma unroll
        for (int r = 0; r < 4; ++r) e[r] = __builtin_amdgcn_exp2f(u[r]);
        e += 1.0f;
#pragma unroll
        for (int r = 0; r < 4; ++r) e[r] = __builtin_amdgcn_rcpf(e[r]);
        return z * e; };
    xread();
    float tv[8];
#pragma unroll
    for (int i = 0; i < 8; ++i) tv[i] = ldsr<float>(lb + L_ST + i * 256 + lane * 4);
    const u32x2 hext = ldsr<u32x2>(fa + (w & 1) * 1024 + ((32 + nt) >> 1) * 2048 + (nt & 1) * 8);
    SB();
    BT(15);
    slices(W1);
    ldsw<f32x4>(pw, acc[0]); ldsw<f32x4>(pw + 1024, acc[1]);
    SB();
    BT(16);
    float mm[2], rr[2], r1[2];
    f32x4 cz[2];
    gemm_small(W1 + 3, fa, acc, [&](auto K) AI {
        constexpr int k = K;
        if constexpr (k + WBS[5] < 21) ldw(W1, O_W1T, k + WBS[5]);
        if constexpr (k == 0) {
            float su = ((tv[0] + tv[1]) + (tv[2] + tv[3])) + ((tv[4] + tv[5]) + (tv[6] + tv[7])), qu = su; swap32f(su, qu);
            const float mean = su * RD576, var = fmaxf(qu * RD576 - mean * mean, 0.f);
            const float r1s = __builtin_amdgcn_rsqf(var + EPS), r12 = r1s * __builtin_amdgcn_rsqf(var * r1s * r1s + EPS);
            mm[0] = mm[1] = mean; rr[0] = rr[1] = r12; r1[0] = r1[1] = r1s;
            swap16f(mm[0], mm[1]); swap16f(rr[0], rr[1]); swap16f(r1[0], r1[1]);
            OPAQ(mm[0]); OPAQ(rr[0]); OPAQ(r1[0]);
        }
        if constexpr (k == P_KB) asm volatile("s_barrier" ::: "memory");
        if constexpr (k >= 4 && k < 8) {
            constexpr int r = k - 4;
            const float c = rowsum16(__uint_as_float(ca[r]) + __uint_as_float(cb[r]));
            cz[0][r] = -mm[0] * rr[0] * c; cz[1][r] = -mm[1] * rr[1] * c; OPAQ(cz[0][r]); OPAQ(cz[1][r]);
        }
        if constexpr (k == P_KB + 1) {
            if (w < 2) {
                const f32x4 ae = psum();
                const float me = w ? mm[1] : mm[0], re = w ? rr[1] : rr[0];
                f32x4 z;
#pragma unroll
                for (int r = 0; r < 4; ++r) z[r] = (ae[r] - me * rowsum16(__uint_as_float(cea[r]) + __uint_as_float(ceb[r]))) * re;
                const f32x4 g = gelu4(z);
                unsigned a0 = pk2(g[0], g[1]), a1 = pk2(g[2], g[3]), b0 = a0, b1 = a1;
                swap16(a0, b0); swap16(a1, b1);
                if (!(lq & 1)) bs16<XA>(u32x4{a0, a1, b0, b1}, wr, lj * 32 + hh * 16, O_GB + (32 + nt) * GTS + (4 + 32 * rb + 16 * w) * 32);
            }
        }
        if constexpr (k % 3 == 0) BT(17 + k / 3);
        if constexpr (k >= 18 - P_W2A) ldw2(k - (18 - P_W2A));
    });
    BT(23);
    {
        unsigned o[2][2];
#pragma unroll
        for (int mt = 0; mt < 2; ++mt) {
            const f32x4 g = gelu4(__builtin_elementwise_fma(acc[mt], f32x4{rr[mt], rr[mt], rr[mt], rr[mt]}, cz[mt]));
            o[mt][0] = pk2(g[0], g[1]); o[mt][1] = pk2(g[2], g[3]);
        }
        swap16(o[0][0], o[1][0]); swap16(o[0][1], o[1][1]);
        bs16<XA>(u32x4{o[0][0], o[0][1], o[1][0], o[1][1]}, wr, (lane & 31) * 32 + hh * 16, O_GB + (8 * nt + w) * GTS + (4 + 32 * rb) * 32);
    }
    SB();
    BT(24);
#pragma unroll
    for (int k = P_W2A; k < P_W2A + P_W2B; ++k) ldw2(k);
    BT(25);
    waitvm<P_W2B>();
    BT(26);
    bar();
    BT(27);
    if (w == 0 && lane == 0) bs4<16>(1u, wr, 0, F_B + (rb + 1) * 128 + nt * 4);
    STAMP(1, 3);
#pragma unroll
    for (int k = P_W2A + P_W2B; k < P_W2A + P_W2B + P_W2C; ++k) ldw2(k);
    BT(28);
    {
        const int pr = rb + (lane >> 2);
        poll(wr, F_B + (lane < 12 ? pr * 128 + (lane & 3) * 4 : 0), (lane < 12 && pr >= 1 && pr <= 64) ? 1u : 0u);
    }
    BT(29);
    u32x4 G[5];
    auto gaddr = [&](int n) AI { const int gr = n / 9, sh = gr == 0 ? 1 : gr == 1 ? -1 : gr == 2 ? 2 : -2; return O_GB + n * GTS + (4 + 32 * rb - sh) * 32; };
#pragma unroll
    for (int m = 0; m < 5; ++m) { G[m] = bl16<XA>(wr, m < 4 ? l16 : h16v, gaddr(m < 4 ? 8 * m + w : 32 + (w >> 1))); SB(); }
    const unsigned gw = lb + L_F + (((lane >> 1) & 15) + 16 * (lane & 1)) * 16;
#pragma unroll
    for (int m = 0; m < 4; ++m) ldsw<u32x4>(gw + (w >> 1) * 2048 + hh * 1024 + (w & 1) * 512 + m * 8192, G[m]);
    if (lane < 32) ldsw<u32x4>(gw + (16 + (w >> 2)) * 2048 + (w & 1) * 1024 + ((w >> 1) & 1) * 512, G[4]);
    SB();
#pragma unroll
    for (int k = P_W2A + P_W2B + P_W2C; k < 24; ++k) ldw2(k);
    BT(30);
    bar();
    BT(31);
    xread();
    SB();
    slices(W2 + 3);
    ldsw<f32x4>(pw, acc[0]); ldsw<f32x4>(pw + 1024, acc[1]);
    slices(W2);
    {
        float a = acc[0][0] + acc[0][1], b = acc[1][0] + acc[1][1];
        swap16f(a, b);
        if (lane < 32) ldsw<float>(lb + L_MN + w * 128 + lane * 4, a);
    }
    SB();
    BT(32);
    float mn[2];
    f32x4 hr[2], rese;
    auto quad = [&](float t) AI { return u32x4{__float_as_uint(t), __float_as_uint(dppf<0x55>(t)), __float_as_uint(dppf<0xAA>(t)), __float_as_uint(dppf<0xFF>(t))}; };
    gemm_small(W2 + 6, fa, acc, [&](auto K) AI {
        constexpr int k = K;
        if constexpr (k == P_KC) {
            asm volatile("s_barrier" ::: "memory");
#pragma unroll
            for (int i = 0; i < 4; ++i) tv[i] = ldsr<float>(lb + L_MN + i * 256 + lane * 4);
        }
        if constexpr (k == P_KC + 2) {
            float t = (tv[0] + tv[1]) + (tv[2] + tv[3]), u = t; swap32f(t, u);
            mn[0] = mn[1] = t + u; swap16f(mn[0], mn[1]); OPAQ(mn[0]);
        }
        if constexpr (k == P_KC + 3) {
            if (w < 2) {
                const f32x4 ae = psum();
                const float h[4] = {bflo(hext[0]), bfhi(hext[0]), bflo(hext[1]), bfhi(hext[1])};
                const float re = w ? r1[1] : r1[0], me = w ? mm[1] : mm[0], ne = w ? mn[1] : mn[0];
                float xc[4];
#pragma unroll
                for (int r = 0; r < 4; ++r) { const float x1 = __builtin_fmaf(h[r] - me, re, ae[r]); xc[r] = x1 - ne; rese[r] = h[r] + x1; }
                unsigned a0 = pk2(xc[0], xc[1]), a1 = pk2(xc[2], xc[3]), b0 = a0, b1 = a1;
                swap16(a0, b0); swap16(a1, b1);
                if (!(lq & 1)) bs16<XA>(u32x4{a0, a1, b0, b1}, wr, hh * 512 + lj * 16, O_XB + (rb * 36 + 32 + nt) * 1024 + w * 256);
                float t = (xc[0] * xc[0] + xc[1] * xc[1]) + (xc[2] * xc[2] + xc[3] * xc[3]), u = t;
                swap16f(t, u); t += u; u = t; swap32f(t, u);
                const u32x4 qv = quad(t + u);
                if (lane < 16 && !(lane & 3)) bs16<XA>(qv, wr, lane * 4, O_SQ + (rb * 36 + 32 + nt) * 128 + 64 * w);
                bs16<0>(__builtin_bit_cast(u32x4, rese), wr, l16, O_R + ((rb * 36 + 32 + nt) * 2 + w) * 1024);
            }
        }
        if constexpr (k >= P_KC + 4 && k < P_KC + 8) {
            constexpr int i = k - (P_KC + 4), mt = i >> 1, r = 2 * (i & 1);
            const float h0 = bflo(hown[i]), h1 = bfhi(hown[i]);
            cz[mt][r] = __builtin_fmaf(h0 - mm[mt], r1[mt], -mn[mt]); cz[mt][r + 1] = __builtin_fmaf(h1 - mm[mt], r1[mt], -mn[mt]);
            hr[mt][r] = h0 + mn[mt]; hr[mt][r + 1] = h1 + mn[mt];
            OPAQ(cz[mt][r]); OPAQ(cz[mt][r + 1]); OPAQ(hr[mt][r]); OPAQ(hr[mt][r + 1]);
        }
        if constexpr (k >= 18 - P_PRA) prime(k - (18 - P_PRA));
    });
    BT(33);
    {
        unsigned o[2][2]; float q2[2]; f32x4 xc[2];
#pragma unroll
        for (int mt = 0; mt < 2; ++mt) { xc[mt] = acc[mt] + cz[mt]; o[mt][0] = pk2(xc[mt][0], xc[mt][1]); o[mt][1] = pk2(xc[mt][2], xc[mt][3]); }
        swap16(o[0][0], o[1][0]); swap16(o[0][1], o[1][1]);
        bs16<XA>(u32x4{o[0][0], o[0][1], o[1][0], o[1][1]}, wr, l16, O_XB + (rb * 36 + 8 * nt + w) * 1024);
        SB();
#pragma unroll
        for (int mt = 0; mt < 2; ++mt) { const f32x4 t = xc[mt] * xc[mt]; q2[mt] = (t[0] + t[1]) + (t[2] + t[3]); }
        {
            swap16f(q2[0], q2[1]);
            float t = q2[0] + q2[1], u = t; swap32f(t, u);
            const u32x4 qv = quad(t + u);
            if (lane < 32 && !(lane & 3)) bs16<XA>(qv, wr, lane * 4, O_SQ + (rb * 36 + 8 * nt + w) * 128);
        }
        SB();
        BT(34);
#pragma unroll
        for (int i = P_PRA; i < P_PRA + P_PRB; ++i) prime(i);
        BT(35);
        waitvm<P_PRB>();
        BT(36);
        bar();
        BT(37);
        if (w == 0 && lane == 0) bs4<16>(1u, wr, 0, F_C + rt * 128 + (kap * 4 + nt) * 4);
#pragma unroll
        for (int i = P_PRA + P_PRB; i < 10; ++i) prime(i);
#pragma unroll
        for (int mt = 0; mt < 2; ++mt) bs16<0>(__builtin_bit_cast(u32x4, xc[mt] + hr[mt]), wr, l16, O_R + ((rb * 36 + 8 * nt + w) * 2 + mt) * 1024);
    }
    }
    BT(38);
    BDUMP(1);
    STAMP(1, 7);

    unsigned kb[4], kq[4];
#pragma unroll
    for (int i = 0; i < 4; ++i) { const int m = (nt + i) & 3; kb[i] = 36 * kap + 18 * (m >> 1) + 10 * (m & 1); kq[i] = 36 * kap + 18 * (m >> 1) + 8 + (m & 1); }
    const unsigned kpa = lb + L_ST + wm * 512 + (lane & 31) * 16;
    if (w < 4) {
        poll(wr, F_C + rt * 128 + (wm * 4 + (lane & 3)) * 4, 1u);
        STAMP(2, 0);
        u32x4 X[36];
#pragma unroll
        for (int i = 0; i < P_X0; ++i) X[i] = bl16<XA>(wr, l16, O_XB + ((4 * rt + wm) * 36 + i) * 1024);
        asm volatile("s_barrier" ::: "memory");
        const int r = 8 * hh + (lj >> 2);
        unsigned ta[2][2], ts;
#pragma unroll
        for (int s = 0; s < 2; ++s)
#pragma unroll
            for (int ab = 0; ab < 2; ++ab) { ta[s][ab] = lb + s * 3 * SLOT + r * 128 + ((ab ^ ((r >> 1) & 1)) * 64) + (lq & 1) * 32 + (lane & 3) * 8; OPAQ(ta[s][ab]); }
        ts = lb + 3 * SLOT + r * 64 + (lq & 1) * 32 + (lane & 3) * 8; OPAQ(ts);
        const int re = 16 * (lq >> 1) + 4 * (lq & 1) + (lj >> 2);
        unsigned am[2][8], ax[4];
        const unsigned amb = lb + re * 256 + ((re & 7) << 5) + (lane & 3) * 8, axb = lb + L_EX + re * 128 + (((re >> 1) & 3) << 5) + (lane & 3) * 8;
        u32x4 H[HD][2];
        const unsigned hvo = l16 + (8 * rt + 2 * wm) * 144 * 1024;
        unsigned hso = (8 * rt + 2 * wm + (lq & 1)) * 144 * 1024 + (lj + 16 * hh) * 16 + O_HB + kb[0] * 1024;
        OPAQ(hso);
        auto ldH = [&](int j, int mt) AI {
            const int t = e_til(j);
            H[j % HD][mt] = bl16<XA>(wr, hvo + mt * 144 * 1024 + (t & 3) * 1024, O_HB + (t < 8 ? kb[e_mem(j)] + (t & 4) : kq[e_mem(j)]) * 1024);
        };
        __builtin_amdgcn_s_setprio(P_PRIO);
        f32x16 acc[4]; f32x4 ec[12][2]; bf16x8 A[RD + 1];
        f32x4 kp; float t[8]; u32x4 hp;
        sfor<0, 19>([&](auto C) AI { sfor<0, 36>([&](auto I) AI {
            constexpr int fr = 36 * C + I, fm = fr - RD;
            if constexpr (fr < 648 + RD) {
            constexpr int em = fm - 324, jm = em >= 0 ? HT.st[em] : 0, cm = em >= 0 ? em - HT.fi[jm] : 0;
            constexpr bool z0 = cm < 8 ? jm == 0 : jm == 10;
            constexpr int gm = fm < 73 ? 0 : fm < 325 ? 1 : fm < 325 + 4 * NE ? 2 : 0;
            constexpr int gh = gm == 1 ? (fm - 73) / 18 : gm == 2 ? 14 + (fm - 325) / NE : 0, go = gm == 1 ? (fm - 73) % 18 : gm == 2 ? (fm - 325) % NE : 0, gt = gh >> 1, gi = gt < 8 ? (gt & 3) : 0;
            constexpr bool formA = gm == 2 && ((ACT_E[go][0] >> 3) == 3 || (ACT_E[go][0] >> 3) == 5);
            auto act = [&](auto K) AI {
                constexpr int op = gm == 1 ? ACT_D[go][K] : (gm == 2 && K < 2) ? ACT_E[go][K & 1] : -1;
                if constexpr (op >= 0) {
                    constexpr int kd = op >> 3, e = op & 7;
                    const float x = acc[gi][8 * (gh & 1) + e];
                    if constexpr (kd == 0) t[e] = x * x;
                    if constexpr (kd == 1) t[e] = __builtin_fmaf(t[e], kp[1], kp[0]);
                    if constexpr (kd == 2 || kd == 6) t[e] = t[e] * x;
                    if constexpr (kd == 3) t[e] = __builtin_amdgcn_exp2f(t[e]);
                    if constexpr (kd == 4) t[e] = __builtin_fmaf(t[e], kp[2], kp[2]);
                    if constexpr (kd == 5) t[e] = __builtin_amdgcn_rcpf(t[e]);
                    if constexpr (kd < 7) OPAQ(t[e]);
                    if constexpr (kd == 7) { hp[e] = pk2(t[2 * e], t[2 * e + 1]); OPAQ(hp[e]); }
                    if constexpr (kd == 8) { static_assert(s_pos(gh) == fm, "store slot"); bs16<XA>(hp, wr, hso, (gt < 8 ? gt * 1024 : (kq[0] - kb[0]) * 1024) + (gh & 1) * 512); }
                    SB();
                }
            };
            using I0 = std::integral_constant<int, 0>; using I1 = std::integral_constant<int, 1>; using I2 = std::integral_constant<int, 2>; using I3 = std::integral_constant<int, 3>;
            auto mf1 = [&]() AI { ec[cm][1] = mfma16(A[(fm + RD + 1) % (RD + 1)], asb(H[jm % HD][1]), z0 ? f32x4{0, 0, 0, 0} : ec[cm][1]); SB(); };
            if constexpr (em >= 0) ec[cm][0] = mfma16(A[fm % (RD + 1)], asb(H[jm % HD][0]), z0 ? f32x4{0, 0, 0, 0} : ec[cm][0]);
            if constexpr (fm >= 0 && fm < 324) {
                constexpr int u = fm / 18, o = fm % 18, ks = u < 16 ? 9 * (u & 3) + (o >> 1) : 18 * (u & 1) + o, ai = u < 16 ? ((u >> 2) & 1) * 2 + (o & 1) : 0;
                f32x16 z = {};
                acc[ai] = __builtin_amdgcn_mfma_f32_32x32x16_bf16(A[fm % (RD + 1)], asb(X[ks]), ks == 0 ? z : acc[ai], 0, 0, 0);
            }
            SB();
            if constexpr (formA) { act(I0{}); act(I1{}); mf1(); }
            s16x4 t0, t1;
            unsigned a = 0, a2 = 0;
            constexpr int er = fr - 324, jr = er >= 0 ? HT.st[er] : 0, cr = er >= 0 ? er - HT.fi[jr] : 0;
            constexpr int bu = fr < 324 ? (fr % 18 == BP ? fr / 18 : -1) : (fr < 648 && !(jr & 1) && cr == BP) ? 18 + jr / 2 : -1;
            constexpr int nl = fr <= 648 ? 2 * (RD - 2) : 2 * (646 - fm) > 0 ? 2 * (646 - fm) : 0;
            if constexpr (bu >= 0) {
                static_assert(b_slot(bu) == fm, "barrier slot");
                constexpr int vw = f_lvl(bu) ? m_wait(LH[(f_lvl(bu) + 3) & 3], bu) : 63;
                if constexpr (vw < 63) waitvm<vw>();
                asm volatile("s_barrier" ::: "memory");
                SB();
            }
            if constexpr (fm >= 0 && fm < 647) { __builtin_amdgcn_s_waitcnt(wcnt(HT.vm[fm], nl)); SB(); }
            if constexpr (fr < 324) {
                constexpr int u = fr / 18, o = fr % 18, sl = u % NSL;
                if constexpr (u < 16) { a = ta[sl / 3][o & 1] + (sl % 3) * SLOT + (o >> 1) * 2048; a2 = a + 512; }
                else { a = ts + (sl - 3) * SLOT + o * 1024; a2 = a + 256; }
            } else if constexpr (fr < 648) {
                constexpr int sl = (18 + (jr >> 1)) % NSL;
                if constexpr (cr < 8) { a = am[sl / 3][cr & 7] + (sl % 3) * SLOT + (jr & 1) * 8192; a2 = a + 2048; }
                else { a = ax[cr & 3] + e_til(jr) * 4096; a2 = a + 1024; }
            }
            if constexpr (fr < 648) { t0 = __builtin_amdgcn_ds_read_tr16_b64_v4i16((LAS s16x4*)(uintptr_t)a); SB(); }
            if constexpr (em >= 0 && !formA) { act(I0{}); if constexpr (gm == 1) act(I1{}); mf1(); }
            if constexpr (fr < 648) {
                t1 = __builtin_amdgcn_ds_read_tr16_b64_v4i16((LAS s16x4*)(uintptr_t)a2);
                A[fr % (RD + 1)] = __builtin_bit_cast(bf16x8, __builtin_shufflevector(t0, t1, 0, 1, 2, 3, 4, 5, 6, 7));
                SB();
            }
            if constexpr (bu == 3) { kp = ldsr<f32x4>(kpa); SB(); }
            if constexpr (em < 0) { act(I0{}); act(I1{}); }
            if constexpr (em >= 0 && gm == 2 && !formA) act(I1{});
            act(I2{}); act(I3{});
            if constexpr (fm >= 299 && fm < 315 && (fm & 1)) { constexpr int c = (fm - 299) >> 1; am[0][c] = amb ^ (c << 5); OPAQ(am[0][c]); }
            if constexpr (fm >= 330 && fm < 346 && !(fm & 1)) { constexpr int c = (fm - 330) >> 1; am[1][c] = am[0][c] + 3 * SLOT; OPAQ(am[1][c]); }
            if constexpr (fm >= 390 && fm < 398 && !(fm & 1)) { constexpr int c = (fm - 390) >> 1; ax[c] = axb ^ (c << 5); OPAQ(ax[c]); }
            if constexpr (fm >= 0 && x_at(fm) >= 0) X[x_at(fm)] = bl16<XA>(wr, l16, O_XB + ((4 * rt + wm) * 36 + x_at(fm)) * 1024);
            constexpr int l0 = fm >= 0 ? HT.at[0][fm] : -1, l1 = fm >= 0 ? HT.at[1][fm] : -1;
            if constexpr (l0 >= 0) ldH(l0 >> 1, l0 & 1);
            if constexpr (l1 >= 0) ldH(l1 >> 1, l1 & 1);
            SB();
            }
        }); });
        __builtin_amdgcn_s_setprio(0);
        STAMP(2, 2);
#pragma unroll
        for (int c = 0; c < 12; ++c) {
            const u32x4 o = {pk2(ec[c][0][0], ec[c][0][1]), pk2(ec[c][0][2], ec[c][0][3]), pk2(ec[c][1][0], ec[c][1][1]), pk2(ec[c][1][2], ec[c][1][3])};
            if (c < 8) bs16<XA>(o, wr, l16, O_PB + (((4 * rt + wm) * 32 + 8 * nt + c) * 4 + kap) * 1024);
            else bs16<XA>(o, wr, l16, O_PX + (((4 * rt + wm) * 4 + c - 8) * 16 + fc) * 1024);
        }
        waitvm<0>();
    } else {
#pragma unroll
        for (int i = 0; i < 5; ++i) dmaD(4, i);
        poll(wr, F_C + rt * 128 + (wm * 4 + (lane & 3)) * 4, 1u);
        STAMP(2, 0);
        asm volatile("s_barrier" ::: "memory");
        f32x4 sqv[5];
        {
            const char* sb = ws + O_SQ + (4 * rt + wm) * 4608;
#pragma unroll
            for (int i = 0; i < 5; ++i) asm volatile("global_load_dwordx4 %0, %1, %2 offset:%3 sc1" : "=v"(sqv[i]) : "v"(i < 4 ? l16 : (l16 & 511) + 1024), "s"(sb), "i"(i < 4 ? i * 1024 : 3072) : "memory");
        }
        const int drow = 4 * wm + lq;
        const unsigned dvo = drow * 1152 + ((lj ^ (2 * (drow & 7))) << 4);
        const char* w4m = w4 + nt * 256;
        auto ksE = [&](int j) AI { return e_til(j) < 8 ? kb[e_mem(j)] + e_til(j) : kq[e_mem(j)]; };
        auto dmaE = [&](int u, int i) AI { const int j = 2 * (u - 18) + (i >> 1);
            dma16(lb + (u % NSL) * SLOT + (j & 1) * 8192 + (wm + 4 * (i & 1)) * 1024, w4m + (size_t)(ksE(j) * 32 + 16 * (i & 1)) * 1152, dvo); };
        const int xrow = lane >> 3;
        const unsigned xvo = xrow * 1152 + (((lane & 7) ^ (2 * ((xrow >> 1) & 3))) << 4);
        auto dmaX = [&](int t) AI { dma16(lb + L_EX + t * 4096 + wm * 1024, w4 + 1024 + (size_t)((t < 8 ? kb[3] + t : kq[3]) * 32 + wm * 8) * 1152, xvo); };
        const unsigned fdo = F_D + rt * 128 + kap * 16;
        unsigned pv = 0, pfd = 0;
        const unsigned pfo = (8 * rt + 2 * wm + ((lane >> 3) & 1)) * 144 * 1024 + (lane & 7) * 128;
        TDECL; TS(0);
        sfor<0, 36>([&](auto U) AI {
            constexpr int u = U;
            if constexpr (u >= 3) waitvm<g_wait(u)>();
            if constexpr (u == 3) {
#pragma unroll
                for (int i = 0; i < 5; ++i) OPAQ(sqv[i]);
                f32x4 sq = (sqv[0] + sqv[1]) + (sqv[2] + sqv[3]) + 0.5f * sqv[4];
                float t = 0;
#pragma unroll
                for (int r = 0; r < 4; ++r) {
                    float a = sq[r] + dppf<0x128>(sq[r]), b = a;
                    swap16f(a, b); a += b; b = a; swap32f(a, b); a += b;
                    if (((lane >> 3) & 3) == r) t = a;
                }
                const float sg = sqrtf(t * RD576 + EPS), rstd = 1.0f / sg;
                if (lane < 32) ldsw<f32x4>(lb + L_ST + wm * 512 + ((lane & 7) * 4 + (lane >> 3)) * 16, f32x4{GC0 * rstd, GC1 * rstd * rstd * rstd, sg, 0.f});
            }
            if constexpr (q_lvl(u) != 0) {
                OPAQ(pv);
                TS(72 + q_lvl(u));
                if (__builtin_amdgcn_ballot_w64(pv < (unsigned)q_lvl(u)) != 0) poll(wr, fdo + (lane & 3) * 4, q_lvl(u));
            }
            TS(2 * u + 1);
            bar();
            TS(2 * u + 2);
            if constexpr (f_lvl(u) != 0) { if (w == 4 && lane == 0) bs4<16>(f_lvl(u), wr, 0, fdo + nt * 4); }
            if constexpr (g_pre(u)) asm volatile("global_load_dword %0, %1, %2 sc1" : "=v"(pv) : "v"((lane & 3) * 4), "s"(ws + fdo) : "memory");
#pragma unroll
            for (int i = 0; i < g_nd(u); ++i) { if (u + 5 < 18) dmaD(u + 5, i); else dmaE(u + 5, i); SB(); }
            if constexpr (g_nx(u)) dmaX(u - 4);
            if constexpr (g_pf(u) >= 0) asm volatile("global_load_dword %0, %1, %2 sc1" : "+v"(pfd) : "v"(pfo), "s"(ws + O_HB + (size_t)(g_pf(u) < 8 ? kb[0] + g_pf(u) : kq[0]) * 1024) : "memory");
            SB();
        });
        waitvm<0>();
        OPAQ(pfd);
        TDUMP(2, 32);
    }
    bar();
    if (w == 0 && lane == 0) bs4<16>(1u, wr, 0, F_E + rt * 128 + fc * 4);
    STAMP(2, 3);
    {
        const rsrc_t orr = mkrs(out, 2048 * 1152);
        u32x4 P[4], R[2], Q[16], Re[2];
#pragma unroll
        for (int i = 0; i < 2; ++i) R[i] = bl16<16>(wr, l16, O_R + ((rb * 36 + 8 * nt + w) * 2 + i) * 1024);
        if (w == 7) {
#pragma unroll
            for (int i = 0; i < 2; ++i) Re[i] = bl16<16>(wr, l16, O_R + ((rb * 36 + 32 + nt) * 2 + i) * 1024);
        }
        poll(wr, F_E + rt * 128 + lj * 4, (w == 7 || (lj & 3) == nt) ? 1u : 0u);
        STAMP(2, 4);
#pragma unroll
        for (int i = 0; i < 4; ++i) P[i] = bl16<XA>(wr, l16, O_PB + ((rb * 32 + 8 * nt + w) * 4 + i) * 1024);
        if (w == 7) {
#pragma unroll
            for (int i = 0; i < 16; ++i) Q[i] = bl16<XA>(wr, l16, O_PX + ((rb * 4 + nt) * 16 + i) * 1024);
        }
        auto fin = [&](u32x4* p, int n, u32x4* r, int tile) AI {
#pragma unroll
            for (int mt = 0; mt < 2; ++mt) {
                float a[4] = {0, 0, 0, 0};
                for (int i = 0; i < n; ++i) { a[0] += bflo(p[i][2 * mt]); a[1] += bfhi(p[i][2 * mt]); a[2] += bflo(p[i][2 * mt + 1]); a[3] += bfhi(p[i][2 * mt + 1]); }
                bs8<0>(u32x2{pk2(a[0] + __uint_as_float(r[mt][0]), a[1] + __uint_as_float(r[mt][1])), pk2(a[2] + __uint_as_float(r[mt][2]), a[3] + __uint_as_float(r[mt][3]))},
                       orr, lj * 1152 + lq * 8, (32 * rb + 16 * mt) * 1152 + tile * 32);
            }
        };
        fin(P, 4, R, 8 * nt + w);
        if (w == 7) fin(Q, 16, Re, 32 + nt);
    }
    STAMP(2, 7);
}

extern "C" void kernel_launch(void* const* d_in, const int* in_sizes, int n_in, void* d_out, int out_size, void* d_ws, size_t ws_size, hipStream_t stream) {
    char* ws = (char*)d_ws;
    (void)hipFuncSetAttribute((const void*)k1, hipFuncAttributeMaxDynamicSharedMemorySize, K1_LDS);
    (void)hipFuncSetAttribute((const void*)k2, hipFuncAttributeMaxDynamicSharedMemorySize, K2_LDS);
    k1<<<256, 512, K1_LDS, stream>>>((const char*)d_in[0], (const char*)d_in[1], (const char*)d_in[2], (const char*)d_in[3], (const char*)d_in[4], (const u16*)d_in[5],
                                     (const char*)d_in[7], (const char*)d_in[8], (const char*)d_in[9], (const char*)d_in[10], ws);
    k2<<<256, 512, K2_LDS, stream>>>((const char*)d_in[9], (const char*)d_in[10], ws, (char*)d_out);
}
```

```cpp
#define SIDEQ (0)
#include <hip/hip_runtime.h>
#include <stdint.h>
#include <utility>
#define LAS __attribute__((address_space(3)))
typedef short s16x4 __attribute__((ext_vector_type(4)));
typedef __bf16 bf16x8 __attribute__((ext_vector_type(8)));
typedef __bf16 bf16x2 __attribute__((ext_vector_type(2)));
typedef float f32x16 __attribute__((ext_vector_type(16)));
typedef float f32x4 __attribute__((ext_vector_type(4)));
typedef float f32x2 __attribute__((ext_vector_type(2)));
typedef unsigned u32x4 __attribute__((ext_vector_type(4)));
typedef unsigned u32x2 __attribute__((ext_vector_type(2)));
typedef unsigned long long u64;
typedef unsigned short u16;
typedef __amdgpu_buffer_rsrc_t rsrc_t;
#define DEVI __device__ __forceinline__
#define AI __attribute__((always_inline))
#define SB() __builtin_amdgcn_sched_barrier(0)
#define OPAQ(x) asm volatile("" : "+v"(x))

DEVI unsigned pk2(float a, float b) { f32x2 p = {a, b}; return __builtin_bit_cast(unsigned, __builtin_convertvector(p, bf16x2)); }
DEVI float bflo(unsigned x) { return __uint_as_float(x << 16); }
DEVI float bfhi(unsigned x) { return __uint_as_float(x & 0xffff0000u); }
DEVI bf16x8 asb(u32x4 x) { return __builtin_bit_cast(bf16x8, x); }
constexpr float GC0 = -2.0f * 0.7978845608028654f * 1.4426950408889634f, GC1 = GC0 * 0.044715f;
DEVI float gelu(float x) {
    float e = __builtin_amdgcn_exp2f(x * __builtin_fmaf(x * x, GC1, GC0));
    return x * __builtin_amdgcn_rcpf(1.0f + e);
}
DEVI void dma16(unsigned ldsaddr, const void* sbase, unsigned voff) {
    asm volatile("s_mov_b32 m0, %0\n\ts_nop 0\n\tglobal_load_lds_dwordx4 %1, %2" :: "s"(ldsaddr), "v"(voff), "s"(sbase) : "memory");
}
DEVI void dma16h(unsigned ldsaddr, const void* sbase, unsigned voff) {
    asm volatile("s_mov_b32 m0, %0\n\ts_mov_b32 exec_hi, 0\n\tglobal_load_lds_dwordx4 %1, %2\n\ts_mov_b32 exec_hi, -1" :: "s"(ldsaddr), "v"(voff), "s"(sbase) : "memory");
}
template <int N> DEVI void waitvm() { asm volatile("s_waitcnt vmcnt(%0)" :: "i"(N) : "memory"); }
DEVI void bar() { asm volatile("s_waitcnt lgkmcnt(0)\n\ts_barrier" ::: "memory"); }
DEVI bf16x8 trfrag(unsigned a0, unsigned a1) {
    s16x4 a = __builtin_amdgcn_ds_read_tr16_b64_v4i16((LAS s16x4*)(uintptr_t)a0);
    s16x4 b = __builtin_amdgcn_ds_read_tr16_b64_v4i16((LAS s16x4*)(uintptr_t)a1);
    return __builtin_bit_cast(bf16x8, __builtin_shufflevector(a, b, 0, 1, 2, 3, 4, 5, 6, 7));
}
template <int B, class F, int... I> DEVI void sfor_impl(F&& f, std::integer_sequence<int, I...>) { (f(std::integral_constant<int, B + I>{}), ...); }
template <int B, int E, class F> DEVI void sfor(F&& f) { sfor_impl<B>(f, std::make_integer_sequence<int, E - B>{}); }
template <class T> DEVI T ldsr(unsigned a) { return *(LAS T*)(uintptr_t)a; }
template <class T> DEVI void ldsw(unsigned a, T v) { *(LAS T*)(uintptr_t)a = v; }
DEVI rsrc_t mkrs(const void* p, unsigned bytes) { return __builtin_amdgcn_make_buffer_rsrc((void*)p, 0, bytes, 0x00020000); }
template <int AUX> DEVI u32x4 bl16(rsrc_t r, unsigned vo, unsigned so) { return __builtin_amdgcn_raw_buffer_load_b128(r, vo, so, AUX); }
template <int AUX> DEVI u32x2 bl8(rsrc_t r, unsigned vo, unsigned so) { return __builtin_amdgcn_raw_buffer_load_b64(r, vo, so, AUX); }
template <int AUX> DEVI unsigned bl4(rsrc_t r, unsigned vo, unsigned so) { return __builtin_amdgcn_raw_buffer_load_b32(r, vo, so, AUX); }
template <int AUX> DEVI void bs16(u32x4 v, rsrc_t r, unsigned vo, unsigned so) { __builtin_amdgcn_raw_buffer_store_b128(v, r, vo + so, 0, AUX); }
template <int AUX> DEVI void bs8(u32x2 v, rsrc_t r, unsigned vo, unsigned so) { __builtin_amdgcn_raw_buffer_store_b64(v, r, vo, so, AUX); }
template <int AUX> DEVI void bs4(unsigned v, rsrc_t r, unsigned vo, unsigned so) { __builtin_amdgcn_raw_buffer_store_b32(v, r, vo, so, AUX); }
DEVI void swap16(unsigned& a, unsigned& b) { auto r = __builtin_amdgcn_permlane16_swap(a, b, false, false); a = r[0]; b = r[1]; }
DEVI void swap32(unsigned& a, unsigned& b) { auto r = __builtin_amdgcn_permlane32_swap(a, b, false, false); a = r[0]; b = r[1]; }
DEVI void swap16f(float& a, float& b) { unsigned x = __float_as_uint(a), y = __float_as_uint(b); swap16(x, y); a = __uint_as_float(x); b = __uint_as_float(y); }
DEVI void swap32f(float& a, float& b) { unsigned x = __float_as_uint(a), y = __float_as_uint(b); swap32(x, y); a = __uint_as_float(x); b = __uint_as_float(y); }
template <int CTRL> DEVI float dppf(float x) { return __uint_as_float(__builtin_amdgcn_update_dpp(0u, __float_as_uint(x), CTRL, 0xf, 0xf, false)); }
DEVI float rowsum16(float x) { x += dppf<0x128>(x); x += dppf<0x124>(x); x += dppf<0x122>(x); x += dppf<0x121>(x); return x; }
DEVI f32x4 mfma16(bf16x8 a, bf16x8 b, f32x4 c) { return __builtin_amdgcn_mfma_f32_16x16x32_bf16(a, b, c, 0, 0, 0); }

constexpr unsigned CLSZ = 64 * 36 * 1024;
constexpr unsigned GTS = 2056 * 32;
constexpr unsigned O_FLAG = 0, O_YB = 65536, O_XT = O_YB + CLSZ, O_XS = O_XT + CLSZ, O_W1T = O_XS + 16384, O_W2T = O_W1T + 663552,
                   O_CSP = O_W2T + 663552, O_W2M = O_CSP + 41472, O_GB = O_W2M + 2304, O_HT = O_GB + 36 * GTS, O_HS = O_HT + CLSZ,
                   O_XB = O_HS + 65536, O_SQ = O_XB + CLSZ, O_R = O_SQ + 36 * 2048 * 4, O_HB = O_R + 2 * CLSZ, O_PB = O_HB + 128 * 144 * 1024,
                   O_PX = O_PB + 64 * 32 * 4 * 1024, O_END = O_PX + 64 * 4 * 16 * 1024, O_DBG = O_END;
constexpr float EPS = 1e-5f, RD576 = 1.0f / 576.0f;
#define XA 16
#define TDECL
#define BDECL
#define BT(i) do {} while (0)
#define BDUMP(kid) do {} while (0)
#define TS(i) do {} while (0)
#define TDUMP(kid, n) do {} while (0)
#define STAMP(kid, i) do { if ((threadIdx.x & 63) == 0) { ((u64*)(ws + O_DBG))[(((kid) * 256 + blockIdx.x) * 8 + (threadIdx.x >> 6)) * 8 + (i)] = __builtin_amdgcn_s_memrealtime(); } __builtin_amdgcn_s_waitcnt(0xC07F); } while (0)
#define CSTAMP(kid, i) do {} while (0)

#define P_KPF 1
constexpr int K1_KC = 0, K1_UR = 33792, K1_P = K1_UR + 18432, K1_LDS = K1_P + 32768;
__global__ __launch_bounds__(512, 2) void k1(const char* __restrict__ xq, const char* __restrict__ kk, const char* __restrict__ vv, const char* __restrict__ shk,
                                             const char* __restrict__ ssk, const u16* __restrict__ ssd, const char* __restrict__ w1, const char* __restrict__ w2,
                                             const char* __restrict__ w3, const char* __restrict__ w4, char* __restrict__ ws) {
    extern __shared__ __attribute__((aligned(256))) char smem[];
    const unsigned lb = (unsigned)(uintptr_t)(LAS char*)smem;
    const int wg = blockIdx.x, xcd = wg & 7, loc = wg >> 3, tid = threadIdx.x, lane = tid & 63;
    const int w = __builtin_amdgcn_readfirstlane(tid >> 6);
    const rsrc_t wr = mkrs(ws, O_END);
    STAMP(0, 0);
    if (loc < 18) {
        const int cw = 18 * xcd + loc;
        const int kch = tid >> 7, tau = tid & 127;
        const rsrc_t sr = mkrs(ssk, 576 * 4096);
        const unsigned so = (4 * cw + kch) * 4096 + tau * 32;
        u32x4 K0 = bl16<0>(sr, so, 0), K1 = bl16<0>(sr, so + 16, 0);
        unsigned K8 = bl4<0>(sr, so + 32, 0);
        const float dv = bflo(ssd[4 * cw + kch]);
        const rsrc_t kr = mkrs(kk, 2048 * 1152), vr = mkrs(vv, 2048 * 1152);
        u32x2 ks[4], vs[4];
        auto strip = [&](int g) AI { SB(); ks[g] = bl8<0>(kr, tid * 1152 + cw * 8, g * 512 * 1152); SB(); vs[g] = bl8<0>(vr, tid * 1152 + cw * 8, g * 512 * 1152); SB(); };
        strip(0);
        asm volatile("s_barrier" ::: "memory");
        strip(1);
        u64 halo[4][3];
#pragma unroll
        for (int g = 0; g < 4; ++g)
#pragma unroll
            for (int i = 0; i < 3; ++i) { int r = 512 * g + 64 * w - 1 - i; u64 t = *(const u64*)(kk + (size_t)(r < 0 ? 0 : r) * 1152 + cw * 8); halo[g][i] = r < 0 ? 0ull : t; }
        const u32x4 sa = *(const u32x4*)(shk + cw * 32), sb = *(const u32x4*)(shk + cw * 32 + 16);
        if (tid < 120) ldsw<u32x4>(lb + K1_UR + (tid / 30) * 4608 + 4096 + (tid % 30) * 16, u32x4{0, 0, 0, 0});
        if (tau == 0) K0[0] = (K0[0] & 0xffff0000u) | (pk2(bflo(K0[0]) + dv, 0.f) & 0xffffu);
        {
            const unsigned c0 = lb + K1_KC + kch * 8448 + 32 + tau * 32, c1 = c0 + 4224;
            ldsw<u32x4>(c0, K0); ldsw<u32x4>(c0 + 16, K1);
            ldsw<u32x4>(c1, u32x4{__builtin_amdgcn_alignbit(K0[1], K0[0], 16), __builtin_amdgcn_alignbit(K0[2], K0[1], 16), __builtin_amdgcn_alignbit(K0[3], K0[2], 16), __builtin_amdgcn_alignbit(K1[0], K0[3], 16)});
            ldsw<u32x4>(c1 + 16, u32x4{__builtin_amdgcn_alignbit(K1[1], K1[0], 16), __builtin_amdgcn_alignbit(K1[2], K1[1], 16), __builtin_amdgcn_alignbit(K1[3], K1[2], 16), __builtin_amdgcn_alignbit(K8, K1[3], 16)});
            if (tau == 0) {
                ldsw<u32x4>(c0 - 32, u32x4{0, 0, 0, 0}); ldsw<u32x4>(c0 - 16, u32x4{0, 0, 0, 0});
                ldsw<u32x4>(c1 - 32, u32x4{0, 0, 0, 0}); ldsw<u32x4>(c1 - 16, u32x4{0, 0, 0, K0[0] << 16});
            }
        }
        SB();
        bar();
        strip(2);
        STAMP(0, 1);
        const int ch = w & 3, hf = w >> 2, li = lane & 15, q = lane >> 4;
        u32x4 A[4][8];
        {
            const int r = (li + 1) & 1;
            unsigned aa = lb + K1_KC + ch * 8448 + r * 4224 + (128 * hf + li + 25 - 8 * q - r) * 2;
            OPAQ(aa);
#pragma unroll
            for (int c0 = 0; c0 < 4; ++c0)
#pragma unroll
                for (int c1 = 0; c1 < 8; ++c1) {
                    const unsigned a = aa + c1 * 512 + c0 * 64;
                    A[c0][c1] = u32x4{ldsr<unsigned>(a), ldsr<unsigned>(a + 4), ldsr<unsigned>(a + 8), ldsr<unsigned>(a + 12)};
                }
        }
        strip(3);
        f32x4 acc[8];
#pragma unroll
        for (int t = 0; t < 8; ++t) acc[t] = f32x4{0, 0, 0, 0};
        unsigned ba = lb + K1_UR + ch * 4608 + (2056 - 1792 - 16 * li + 128 * hf - 8 * q) * 2;
        OPAQ(ba);
        const unsigned ua = lb + K1_UR + (511 - tid) * 2;
        const unsigned pw = lb + K1_P + (hf * 4 + ch) * 2048 + li * 64 + q * 16;
        const unsigned pr = lb + K1_P + ((tid >> 4) * 32 + (tid & 15)) * 4;
        auto output = [&](int g) AI {
            if (tid < 256) {
                float y[2][4];
#pragma unroll
                for (int c = 0; c < 4; ++c)
#pragma unroll
                    for (int m = 0; m < 2; ++m) y[m][c] = ldsr<float>(pr + (g & 1) * 16384 + c * 2048 + m * 64) + ldsr<float>(pr + (g & 1) * 16384 + 8192 + c * 2048 + m * 64);
                bs16<0>(u32x4{pk2(y[0][0], y[0][1]), pk2(y[0][2], y[0][3]), pk2(y[1][0], y[1][1]), pk2(y[1][2], y[1][3])}, wr, (tid >> 4) * 36864 + (tid & 15) * 16, O_YB + g * 16 * 36864 + cw * 256);
            }
        };
        sfor<0, 4>([&](auto G) AI {
            constexpr int g = G;
            unsigned kx[4][2];
            kx[0][0] = ks[g][0]; kx[0][1] = ks[g][1];
#pragma unroll
            for (int i = 1; i < 4; ++i) {
                kx[i][0] = __builtin_amdgcn_update_dpp((unsigned)halo[g][i - 1], kx[i - 1][0], 0x138, 0xf, 0xf, false);
                kx[i][1] = __builtin_amdgcn_update_dpp((unsigned)(halo[g][i - 1] >> 32), kx[i - 1][1], 0x138, 0xf, 0xf, false);
            }
            float s0 = bflo(kx[0][0]) * bflo(sa[0]) + bflo(kx[1][0]) * bfhi(sa[0]) + bflo(kx[2][0]) * bflo(sa[1]) + bflo(kx[3][0]) * bfhi(sa[1]);
            float s1 = bfhi(kx[0][0]) * bflo(sa[2]) + bfhi(kx[1][0]) * bfhi(sa[2]) + bfhi(kx[2][0]) * bflo(sa[3]) + bfhi(kx[3][0]) * bfhi(sa[3]);
            float s2 = bflo(kx[0][1]) * bflo(sb[0]) + bflo(kx[1][1]) * bfhi(sb[0]) + bflo(kx[2][1]) * bflo(sb[1]) + bflo(kx[3][1]) * bfhi(sb[1]);
            float s3 = bfhi(kx[0][1]) * bflo(sb[2]) + bfhi(kx[1][1]) * bfhi(sb[2]) + bfhi(kx[2][1]) * bflo(sb[3]) + bfhi(kx[3][1]) * bfhi(sb[3]);
            const unsigned p01 = pk2(s0 * bflo(vs[g][0]), s1 * bfhi(vs[g][0])), p23 = pk2(s2 * bflo(vs[g][1]), s3 * bfhi(vs[g][1]));
            const unsigned u = ua + (3 - g) * 1024;
            ldsw<u16>(u, (u16)p01); ldsw<u16>(u + 4608, (u16)(p01 >> 16)); ldsw<u16>(u + 9216, (u16)p23); ldsw<u16>(u + 13824, (u16)(p23 >> 16));
            SB();
            bar();
            u32x4 B[8];
#pragma unroll
            for (int i = 0; i < 8; ++i) B[i] = ldsr<u32x4>(ba + (7 - 2 * g - (i >> 2)) * 512 + (i & 3) * 64);
            SB();
            if constexpr (g >= 1) output(g - 1);
            SB();
#pragma unroll
            for (int i = 0; i < 8; ++i) {
                const int b = 2 * g + (i >> 2);
#pragma unroll
                for (int c1 = 0; c1 + b < 8; ++c1) acc[b + c1] = mfma16(asb(A[i & 3][c1]), asb(B[i]), acc[b + c1]);
            }
            SB();
            ldsw<f32x4>(pw + (g & 1) * 16384, acc[2 * g]); ldsw<f32x4>(pw + (g & 1) * 16384 + 1024, acc[2 * g + 1]);
            STAMP(0, 2 + g);
        });
        bar();
        output(3);
    } else {
        const int u = (loc - 18) * 8 + w;
        if (u < 32 || (u >= 73 && u < 82)) {
            const bool isx = u < 32;
            const int rl = lane >> 4, c = lane & 15;
            const int rb = 32 * (xcd >> 2) + 4 * (u >> 2) + (xcd & 3), rho = 4 * (u & 3) + rl;
            const int r0 = isx ? 32 * rb + rho : 8 * (9 * xcd + u - 73) + rl, r1 = r0 + (isx ? 16 : 4);
            const rsrc_t xr = mkrs(isx ? xq : w2, isx ? 2048 * 1152 : 576 * 1152);
            u32x2 a[9], b[9];
#pragma unroll
            for (int m = 0; m < 9; ++m) { a[m] = bl8<0>(xr, r0 * 1152 + c * 8, m * 128); b[m] = bl8<0>(xr, r1 * 1152 + c * 8, m * 128); }
            float sa = 0, sb = 0;
#pragma unroll
            for (int m = 0; m < 9; ++m) {
                sa += (bflo(a[m][0]) + bfhi(a[m][0])) + (bflo(a[m][1]) + bfhi(a[m][1]));
                sb += (bflo(b[m][0]) + bfhi(b[m][0])) + (bflo(b[m][1]) + bfhi(b[m][1]));
                if (isx) bs16<0>(u32x4{a[m][0], a[m][1], b[m][0], b[m][1]}, wr, c * 256 + rho * 16, O_XT + rb * 36864 + m * 4096);
            }
            const float ma = rowsum16(sa) * RD576, mb = rowsum16(sb) * RD576;
            if (isx) {
                float qa = 0, qb = 0;
#pragma unroll
                for (int m = 0; m < 9; ++m)
#pragma unroll
                    for (int i = 0; i < 2; ++i) {
                        float t;
                        t = bflo(a[m][i]) - ma; qa += t * t; t = bfhi(a[m][i]) - ma; qa += t * t;
                        t = bflo(b[m][i]) - mb; qb += t * t; t = bfhi(b[m][i]) - mb; qb += t * t;
                    }
                const float ra = 1.0f / sqrtf(rowsum16(qa) * RD576 + EPS), rbb = 1.0f / sqrtf(rowsum16(qb) * RD576 + EPS);
                if (c == 0) { bs8<0>(u32x2{__float_as_uint(ra), __float_as_uint(-ma * ra)}, wr, r0 * 8, O_XS); bs8<0>(u32x2{__float_as_uint(rbb), __float_as_uint(-mb * rbb)}, wr, r1 * 8, O_XS); }
            } else if (c < 2) {
                const int k = c ? r1 : r0; const float m = c ? mb : ma;
                const int p = (k >> 3) * 32 + (k & 7) * 2;
                const unsigned hi = pk2(m, 0.f) & 0xffffu, lo = pk2(m - bflo(hi), 0.f) & 0xffffu;
                *(u16*)(ws + O_W2M + p) = (u16)hi; *(u16*)(ws + O_W2M + 16 + p) = (u16)lo;
            }
        } else if (u < 73) {
            const int g = 41 * xcd + u - 32;
            if (g < 324) {
                const int mat = g >= 162, r = g - 162 * mat, ks = r / 9, cg = r - 9 * ks;
                const rsrc_t mr = mkrs(mat ? w2 : w1, 576 * 1152);
                u32x4 t[4];
#pragma unroll
                for (int i = 0; i < 4; ++i) t[i] = bl16<0>(mr, (lane >> 3) * 1152 + (lane & 7) * 16, (32 * ks + 8 * i) * 1152 + cg * 128);
                const unsigned lw = lb + w * 4096;
#pragma unroll
                for (int i = 0; i < 4; ++i) ldsw<u32x4>(lw + i * 1024 + lane * 16, t[i]);
                const unsigned ta = lw + ((mat ? 8 : 4) * (lane >> 4) + ((lane & 15) >> 2)) * 128 + (lane & 3) * 8, tb = ta + (mat ? 512 : 2048);
                const u32x4 ones = {0x3f803f80u, 0x3f803f80u, 0x3f803f80u, 0x3f803f80u};
#pragma unroll
                for (int tt = 0; tt < 4; ++tt) {
                    const bf16x8 f = trfrag(ta + tt * 32, tb + tt * 32);
                    bs16<0>(__builtin_bit_cast(u32x4, f), wr, lane * 16, (mat ? O_W2T : O_W1T) + ((4 * cg + tt) * 18 + ks) * 1024);
                    if (!mat) {
                        const f32x4 cs = mfma16(f, asb(ones), f32x4{0, 0, 0, 0});
                        if ((lane & 15) == 0) bs16<0>(__builtin_bit_cast(u32x4, cs), wr, (lane >> 4) * 16, O_CSP + ((4 * cg + tt) * 18 + ks) * 64);
                    }
                }
            }
        } else if (u >= 84) {
            const rsrc_t r3 = mkrs(w3, 576 * 9216), r4 = mkrs(w4, 4608 * 1152);
            for (int i = 0; i < P_KPF; ++i) asm volatile("s_sleep 16");
            unsigned t[12], a = 0;
#pragma unroll
            for (int i = 0; i < 6; ++i) {
                const unsigned l = (i * 28 + u - 84) * 64 + lane, row = l / 18;
                t[i] = bl4<0>(r3, row * 9216 + (l - row * 18) * 128 + (xcd & 3) * 2304, 0);
            }
#pragma unroll
            for (int i = 0; i < 6; ++i) t[6 + i] = bl4<0>(r4, ((i * 28 + u - 84) * 64 + lane) * 128 + (xcd & 3) * (1152 * 1152), 0);
#pragma unroll
            for (int i = 0; i < 12; ++i) a ^= t[i];
            asm volatile("" :: "v"(a));
        } else if (u == 83) {
            bs16<0>(u32x4{0, 0, 0, 0}, wr, lane * 16, O_FLAG + xcd * 2048); bs16<0>(u32x4{0, 0, 0, 0}, wr, lane * 16, O_FLAG + xcd * 2048 + 1024);
        } else if (u == 82) {
            const int l = 9 * xcd + (lane >> 3);
            bs16<0>(u32x4{0, 0, 0, 0}, wr, (l >> 1) * GTS + (l & 1) * (2052 * 32) + (lane & 7) * 16, O_GB);
            const int l2 = 9 * xcd + 8;
            if (lane < 8) bs16<0>(u32x4{0, 0, 0, 0}, wr, (l2 >> 1) * GTS + (l2 & 1) * (2052 * 32) + lane * 16, O_GB);
        }
    }
    STAMP(0, 7);
}

constexpr int SLOT = 18432, NSL = 6, L_EX = NSL * SLOT, L_F = L_EX, L_ST = L_EX + 36864, L_MN = L_ST + 2048, K2_LDS = L_MN + 1024, L_PE = 5 * SLOT;
constexpr unsigned F_B = O_FLAG, F_C = O_FLAG + 66 * 128, F_D = F_C + 16 * 128, F_E = F_D + 16 * 128;
#define P_RD 4
#define P_B0 0
#define P_WBS {0, 2, 5, 8, 11, 14}
#define P_KB 8
#define P_KC 3
#define P_W2A 6
#define P_W2B 6
#define P_W2C 6
#define P_PRA 3
#define P_PRB 4
#define P_X0 10
#define P_PRIO 2
constexpr int RD = P_RD, BP = RD + 1, HD = 8;
constexpr int SPIN = 1 << 17;

template <class HK> DEVI void gemm_small(const u32x4* W, unsigned fa, f32x4* acc, HK&& hook) {
    constexpr int PF = 3;
    u32x4 b[PF + 1][2];
    sfor<0, 18 + PF>([&](auto K) AI {
        constexpr int k = K, m = k - PF;
        if constexpr (m >= 0) {
            const f32x4 z = {0, 0, 0, 0};
            acc[0] = mfma16(asb(W[m]), asb(b[m % (PF + 1)][0]), m ? acc[0] : z); acc[1] = mfma16(asb(W[m]), asb(b[m % (PF + 1)][1]), m ? acc[1] : z);
            SB();
        }
        if constexpr (k < 18) { b[k % (PF + 1)][0] = ldsr<u32x4>(fa + k * 2048); b[k % (PF + 1)][1] = ldsr<u32x4>(fa + k * 2048 + 1024); SB(); }
        if constexpr (m >= 0) { hook(std::integral_constant<int, m>{}); SB(); }
    });
}
DEVI void poll(rsrc_t wr, unsigned off, unsigned need) {
#pragma unroll 1
    for (int i = 0; i < SPIN; ++i) {
        const unsigned v = bl4<16>(wr, off, 0);
        if (__builtin_amdgcn_ballot_w64(v < need) == 0) break;
        asm volatile("s_sleep 1" ::: "memory");
    }
    asm volatile("" ::: "memory");
}
constexpr int e_mem(int j) { return j < 4 ? 0 : j < 13 ? 1 + (j - 4) / 3 : j < 16 ? 0 : j < 25 ? 1 + (j - 16) / 3 : j < 27 ? 0 : j < 33 ? 1 + (j - 27) / 2 : j - 32; }
constexpr int e_til(int j) { return j < 4 ? j : j < 13 ? (j - 4) % 3 : j < 16 ? j - 9 : j < 25 ? 3 + (j - 16) % 3 : j < 27 ? j - 18 : j < 33 ? 6 + (j - 27) % 2 : 8; }
constexpr int e_first(int j) { int f = 0; for (int i = 0; i < j; ++i) f += e_mem(i) == 3 ? 12 : 8; return f; }
#define P_FB {12, 18, 24, 28}
#define P_QB {17, 23, 29, 32}
#define P_SD 2
#define P_TP 1
constexpr int FB[4] = P_FB, QB[4] = P_QB, LH[4] = {5, 11, 15, 17}, LJ[5] = {4, 16, 27, 33, 99};
constexpr int f_lvl(int u) { for (int l = 0; l < 4; ++l) if (FB[l] == u) return l + 1; return 0; }
constexpr int q_lvl(int u) { for (int l = 0; l < 4; ++l) if (QB[l] == u) return l + 1; return 0; }
constexpr signed char ACT_D[18][4] = {{0,1,8,-1},{16,9,24,-1},{32,17,40,-1},{25,2,48,10},{33,18,41,-1},{26,3,49,11},{34,19,42,-1},{27,4,50,12},{35,20,43,-1},{28,5,51,13},
                                      {36,21,44,-1},{29,6,52,14},{37,22,45,-1},{30,7,53,15},{38,23,46,-1},{31,54,56,-1},{39,57,47,-1},{55,58,59,64}};
constexpr int NE = 33;
constexpr signed char ACT_E[NE][2] = {{0,8},{16,1},{24,9},{32,17},{40,2},{25,48},{33,10},{41,18},{26,49},{56,34},{42,3},{50,11},{19,4},{27,12},{35,20},{43,5},{28,51},{57,36},{44,13},{52,21},{29,6},{37,14},{45,22},{30,53},{58,38},{46,7},{54,15},{23,-1},{31,-1},{39,-1},{47,-1},{55,59},{64,-1}};
constexpr int b_slot(int u) { return u < 18 ? 18 * u + 1 : 324 + e_first(2 * (u - 18)) + 1; }
constexpr int mx(int a, int b) { return a > b ? a : b; }
constexpr int h_pos(int j, int mt) {
    int p = j < 8 ? 290 + 4 * j + 2 * mt : 324 + e_first(j - 7) + 2 + 2 * mt;
    if (e_mem(j) == 0) { if (j == 26) p = mx(p, b_slot(FB[3]) + 1 + 2 * mt); return p; }
    for (int l = 0; l < 4; ++l) if (j >= LJ[l] && j < LJ[l + 1]) p = mx(p, b_slot(QB[l]) + 1 + 2 * (j - LJ[l]) + mt);
    return p;
}
constexpr int s_pos(int h) { return h < 14 ? 90 + 18 * h : 325 + NE * (h - 13) - 1; }
constexpr int m_wait(int h, int u) {
    int n = 0;
    for (int i = h + 1; i < 18; ++i) n += s_pos(i) < b_slot(u);
    for (int j = 0; j < 36; ++j) for (int mt = 0; mt < 2; ++mt) n += h_pos(j, mt) >= s_pos(h) && h_pos(j, mt) < b_slot(u);
    return n;
}
constexpr int x_at(int s) { return (!(s & 1) && P_X0 + s / 2 < 36) ? P_X0 + s / 2 : -1; }
struct HTab { int at[2][648], st[330], fi[37], vm[648]; bool ok;
    constexpr HTab() : at{}, st{}, fi{}, vm{}, ok(true) {
        int cnt[649] = {}, pre[649] = {}, done[649] = {};
        for (int s = 0; s < 648; ++s) { at[0][s] = at[1][s] = -1; vm[s] = 63; }
        for (int j = 0; j < 37; ++j) fi[j] = e_first(j);
        for (int h = 0; h < 18; ++h) ++cnt[s_pos(h)];
        for (int l = 0; l < 72; ++l) { const int p = h_pos(l >> 1, l & 1); ++cnt[p]; if (at[0][p] < 0) at[0][p] = l; else if (at[1][p] < 0) at[1][p] = l; else ok = false; if (p >= 323 + fi[l >> 1]) ok = false; }
        for (int s = 0; s < 648; ++s) pre[s + 1] = pre[s] + cnt[s];
        for (int e = 0, j = 0; e < 330; ++e) { while (j < 35 && fi[j + 1] <= e) ++j; st[e] = j; }
        for (int j = 0; j < 36; ++j) {
            const int p = h_pos(j, 1), s = 323 + fi[j];
            if (p < h_pos(j, 0)) ok = false;
            const int n = pre[s] - pre[p + 1] + (at[0][p] == 2 * j + 1 && at[1][p] >= 0 ? 1 : 0);
            if (n >= 63) ok = false;
            vm[s] = n; done[s] = pre[s] - n;
        }
        for (int l = 0; l < 4; ++l) { const int s = b_slot(FB[l]); done[s] = pre[s] - m_wait(LH[l], FB[l]); if (s_pos(LH[l]) >= s || FB[l] + P_SD >= QB[l]) ok = false; }
        for (int s = 1; s < 648; ++s) done[s] = mx(done[s], done[s - 1]);
        for (int j = 0; j < 36; ++j) if (e_mem(j) == 0 && j >= 4 && done[h_pos(j, 0)] <= pre[s_pos(2 * e_til(j) + 1)]) ok = false;
        for (int k = 1; k < 36; ++k) { int n = P_X0; for (int s = 0; s < 2 * k - 1; ++s) n += x_at(s) >= 0; vm[2 * k - 1] = n - 1 - k; if (n <= k) ok = false; }
        vm[289] = pre[289] - pre[s_pos(7)] - 1;
    } };
constexpr HTab HT{};
static_assert(HT.ok, "schedule");
constexpr int wcnt(int vm, int lgkm) { return (vm & 15) | ((vm >> 4) << 14) | 0x70 | (lgkm << 8); }
constexpr int g_nd(int i) { return i + 5 < 18 ? 5 : i + 5 < 36 ? 4 : 0; }
constexpr int g_nx(int i) { return (i >= 4 && i < 13) ? 1 : 0; }
constexpr int g_pre(int i) { return q_lvl(i + P_SD) ? 1 : 0; }
#define P_PF 8
constexpr int g_pf(int i) { return P_PF == 0 ? -1 : (i >= P_PF && i <= P_PF + 12 && !((i - P_PF) & 1)) ? (i - P_PF) / 2 : i == FB[2] ? 7 : i == FB[3] ? 8 : -1; }
constexpr int g_all(int i) { return g_pre(i) + g_nd(i) + g_nx(i) + (g_pf(i) >= 0); }
constexpr int g_wait(int u) {
    int n = g_nx(u - 4) + (g_pf(u - 4) >= 0) + g_all(u - 3) + g_all(u - 2) + g_all(u - 1);
    if (q_lvl(u)) { n = -1; for (int i = 1; i <= P_SD; ++i) n += g_all(u - i); }
    return n;
}

__global__ __launch_bounds__(512, 2) void k2(const char* __restrict__ w3, const char* __restrict__ w4, char* __restrict__ ws, char* __restrict__ out) {
    extern __shared__ __attribute__((aligned(256))) char smem[];
    const unsigned lb = (unsigned)(uintptr_t)(LAS char*)smem;
    const int wg = blockIdx.x, xcd = wg & 7, loc = wg >> 3, lane = threadIdx.x & 63;
    const int kap = xcd & 3, rt = 8 * (xcd >> 2) + (loc >> 2), nt = loc & 3, rb = 4 * rt + kap, fc = 4 * kap + nt;
    const int w = __builtin_amdgcn_readfirstlane(threadIdx.x >> 6), wm = w & 3;
    const int lj = lane & 15, lq = lane >> 4, hh = lane >> 5;
    const unsigned l16 = lane * 16;
    const rsrc_t wr = mkrs(ws, O_END);
    BDECL; BT(0);
    STAMP(1, 0);
    const int a9 = 9 * (fc >> 1), odd = fc & 1;
    const char* wp = w3 + (a9 + 5 * odd) * 128 + (size_t)wm * 36 * 9216;
    const char* wsg = w3 + (a9 + 4) * 128 + odd * 64 + (size_t)wm * 72 * 9216;
    const unsigned pvo = (lane >> 3) * 9216 + (((lane & 7) ^ (((lane >> 4) & 1) << 2)) * 16), svo = (lane >> 2) * 9216 + (lane & 3) * 16;
    auto dmaD = [&](int u, int i) AI {
        const unsigned dst = lb + (u % NSL) * SLOT + wm * 4608 + i * 1024;
        const char* src = u < 16 ? wp + (u >> 2) * 128 + (size_t)((u & 3) * 144 + i * 8) * 9216 : wsg + (size_t)((u & 1) * 288 + i * 16) * 9216;
        if (i < 4) dma16(dst, src, u < 16 ? pvo : svo); else dma16h(dst, src, u < 16 ? pvo : svo);
    };

    {
    constexpr unsigned OOB = 0x80000000u;
    u32x4 Y[5], X[5], W1[21], W2[24];
    int nu[5];
#pragma unroll
    for (int m = 0; m < 4; ++m) nu[m] = ((nt + m) & 3) * 8 + w;
    nu[4] = 32 + (w >> 1);
    const unsigned h16 = l16 + (w & 1) * 512, h16v = lane < 32 ? h16 : OOB, e16 = w < 2 ? l16 : OOB;
    BT(1);
    const u32x2 xs0 = bl8<0>(wr, lj * 8, O_XS + rb * 256), xs1 = bl8<0>(wr, lj * 8, O_XS + rb * 256 + 128);
#pragma unroll
    for (int m = 0; m < 5; ++m) { Y[m] = bl16<0>(wr, m < 4 ? l16 : h16v, O_YB + (rb * 36 + nu[m]) * 1024); X[m] = bl16<0>(wr, m < 4 ? l16 : h16v, O_XT + (rb * 36 + nu[m]) * 1024); SB(); }
    BT(2);
    if (P_B0) asm volatile("s_barrier" ::: "memory");
    BT(3);
    auto ldw = [&](u32x4* W, unsigned base, int k) AI {
        if (k < 0) W[k] = bl16<0>(wr, (lj < 2 && (k < -1 || w < 2)) ? lq * 32 + lj * 16 : OOB, O_W2M + (w + 8 * (k + 3)) * 128);
        else if (k < 3) W[k] = bl16<0>(wr, k < 2 ? l16 : e16, base + ((32 + nt) * 18 + w + 8 * k) * 1024);
        else W[k] = bl16<0>(wr, l16, base + ((8 * nt + w) * 18 + k - 3) * 1024);
        SB();
    };
    auto ldw2 = [&](int k) AI { ldw(W2 + 3, O_W2T, k - 3); };
    auto prime = [&](int i) AI { dmaD(2 * (i / 5) + (w >> 2), i % 5); SB(); };
    f32x2 s[2] = {{0, 0}, {0, 0}}, sq[2] = {{0, 0}, {0, 0}};
    const float rx[2] = {__uint_as_float(xs0[0]), __uint_as_float(xs1[0])}, mx[2] = {__uint_as_float(xs0[1]), __uint_as_float(xs1[1])};
    u32x4 hown, ca, cb, cea, ceb;
    constexpr int WBS[6] = P_WBS;
#pragma unroll
    for (int m = 0; m < 5; ++m) {
#pragma unroll
        for (int k = m ? WBS[m - 1] : 0; k < WBS[m]; ++k) ldw(W1, O_W1T, k);
        BT(4 + 2 * m);
        u32x4 hp;
#pragma unroll
        for (int i = 0; i < 4; ++i) {
            const int mt = i >> 1;
            const f32x2 x = {bflo(X[m][i]), bfhi(X[m][i])}, y = {bflo(Y[m][i]), bfhi(Y[m][i])};
            const f32x2 h = __builtin_elementwise_fma(__builtin_elementwise_fma(x, f32x2{rx[mt], rx[mt]}, f32x2{mx[mt], mx[mt]}), y, x);
            s[mt] += h; sq[mt] = __builtin_elementwise_fma(h, h, sq[mt]);
            hp[i] = pk2(h[0], h[1]);
        }
        OPAQ(s[0]); OPAQ(s[1]); OPAQ(sq[0]); OPAQ(sq[1]);
        const unsigned a = lb + L_F + (nu[m] >> 1) * 2048 + (nu[m] & 1) * 8 + (m < 4 ? l16 : h16);
        if (m < 4 || lane < 32) { ldsw<u32x2>(a, u32x2{hp[0], hp[1]}); ldsw<u32x2>(a + 1024, u32x2{hp[2], hp[3]}); }
        if (m == 0) hown = hp;
        SB();
        BT(5 + 2 * m);
    }
    {
        float s0 = s[0][0] + s[0][1], s1 = s[1][0] + s[1][1], q0 = sq[0][0] + sq[0][1], q1 = sq[1][0] + sq[1][1];
        swap16f(s0, s1); swap16f(q0, q1);
        float t = s0 + s1, u = q0 + q1;
        swap32f(t, u);
        ldsw<float>(lb + L_ST + w * 256 + lane * 4, t + u);
    }
    SB();
    {
        const unsigned co = lj * 64 + lq * 16, co2 = lj < 2 ? co + 1024 : OOB;
        ca = bl16<0>(wr, co, O_CSP + (8 * nt + w) * 1152); cb = bl16<0>(wr, co2, O_CSP + (8 * nt + w) * 1152);
        cea = bl16<0>(wr, w < 2 ? co : OOB, O_CSP + (32 + nt) * 1152); ceb = bl16<0>(wr, w < 2 ? co2 : OOB, O_CSP + (32 + nt) * 1152);
        SB();
    }
#pragma unroll
    for (int k = WBS[4]; k < WBS[5]; ++k) ldw(W1, O_W1T, k);
    bar();
    BT(14);
    f32x4 acc[2];
    unsigned fa = lb + L_F + l16; OPAQ(fa);
    const unsigned fx = fa + w * 2048, fx2 = fa + (16 + (w & 1)) * 2048, pw = lb + L_PE + w * 2048 + l16, pr = lb + L_PE + (w & 1) * 1024 + l16;
    u32x4 xb[3][2];
    auto slices = [&](const u32x4* We) AI {
#pragma unroll
        for (int i = 0; i < 3; ++i)
#pragma unroll
            for (int mt = 0; mt < 2; ++mt) acc[mt] = mfma16(asb(We[i]), asb(xb[i][mt]), i ? acc[mt] : f32x4{0, 0, 0, 0});
    };
    auto xread = [&]() AI {
#pragma unroll
        for (int mt = 0; mt < 2; ++mt) { xb[0][mt] = ldsr<u32x4>(fx + mt * 1024); xb[1][mt] = ldsr<u32x4>(fx + 16384 + mt * 1024); xb[2][mt] = ldsr<u32x4>(fx2 + mt * 1024); }
    };
    auto psum = [&]() AI { f32x4 t[8];
#pragma unroll
        for (int i = 0; i < 8; ++i) t[i] = ldsr<f32x4>(pr + i * 2048);
        return ((t[0] + t[1]) + (t[2] + t[3])) + ((t[4] + t[5]) + (t[6] + t[7])); };
    auto gelu4 = [&](f32x4 z) AI {
        const f32x4 u = z * __builtin_elementwise_fma(z * z, f32x4{GC1, GC1, GC1, GC1}, f32x4{GC0, GC0, GC0, GC0});
        f32x4 e;
#pragma unroll
        for (int r = 0; r < 4; ++r) e[r] = __builtin_amdgcn_exp2f(u[r]);
        e += 1.0f;
#pragma unroll
        for (int r = 0; r < 4; ++r) e[r] = __builtin_amdgcn_rcpf(e[r]);
        return z * e; };
    xread();
    float tv[8];
#pragma unroll
    for (int i = 0; i < 8; ++i) tv[i] = ldsr<float>(lb + L_ST + i * 256 + lane * 4);
    const u32x2 hext = ldsr<u32x2>(fa + (w & 1) * 1024 + ((32 + nt) >> 1) * 2048 + (nt & 1) * 8);
    SB();
    BT(15);
    slices(W1);
    ldsw<f32x4>(pw, acc[0]); ldsw<f32x4>(pw + 1024, acc[1]);
    SB();
    BT(16);
    float mm[2], rr[2], r1[2];
    f32x4 cz[2];
    gemm_small(W1 + 3, fa, acc, [&](auto K) AI {
        constexpr int k = K;
        if constexpr (k + WBS[5] < 21) ldw(W1, O_W1T, k + WBS[5]);
        if constexpr (k == 0) {
            float su = ((tv[0] + tv[1]) + (tv[2] + tv[3])) + ((tv[4] + tv[5]) + (tv[6] + tv[7])), qu = su; swap32f(su, qu);
            const float mean = su * RD576, var = fmaxf(qu * RD576 - mean * mean, 0.f);
            const float r1s = __builtin_amdgcn_rsqf(var + EPS), r12 = r1s * __builtin_amdgcn_rsqf(var * r1s * r1s + EPS);
            mm[0] = mm[1] = mean; rr[0] = rr[1] = r12; r1[0] = r1[1] = r1s;
            swap16f(mm[0], mm[1]); swap16f(rr[0], rr[1]); swap16f(r1[0], r1[1]);
            OPAQ(mm[0]); OPAQ(rr[0]); OPAQ(r1[0]);
        }
        if constexpr (k == P_KB) asm volatile("s_barrier" ::: "memory");
        if constexpr (k >= 4 && k < 8) {
            constexpr int r = k - 4;
            const float c = rowsum16(__uint_as_float(ca[r]) + __uint_as_float(cb[r]));
            cz[0][r] = -mm[0] * rr[0] * c; cz[1][r] = -mm[1] * rr[1] * c; OPAQ(cz[0][r]); OPAQ(cz[1][r]);
        }
        if constexpr (k == P_KB + 1) {
            if (w < 2) {
                const f32x4 ae = psum();
                const float me = w ? mm[1] : mm[0], re = w ? rr[1] : rr[0];
                f32x4 z;
#pragma unroll
                for (int r = 0; r < 4; ++r) z[r] = (ae[r] - me * rowsum16(__uint_as_float(cea[r]) + __uint_as_float(ceb[r]))) * re;
                const f32x4 g = gelu4(z);
                unsigned a0 = pk2(g[0], g[1]), a1 = pk2(g[2], g[3]), b0 = a0, b1 = a1;
                swap16(a0, b0); swap16(a1, b1);
                if (!(lq & 1)) bs16<XA>(u32x4{a0, a1, b0, b1}, wr, lj * 32 + hh * 16, O_GB + (32 + nt) * GTS + (4 + 32 * rb + 16 * w) * 32);
            }
        }
        if constexpr (k % 3 == 0) BT(17 + k / 3);
        if constexpr (k >= 18 - P_W2A) ldw2(k - (18 - P_W2A));
    });
    BT(23);
    {
        unsigned o[2][2];
#pragma unroll
        for (int mt = 0; mt < 2; ++mt) {
            const f32x4 g = gelu4(__builtin_elementwise_fma(acc[mt], f32x4{rr[mt], rr[mt], rr[mt], rr[mt]}, cz[mt]));
            o[mt][0] = pk2(g[0], g[1]); o[mt][1] = pk2(g[2], g[3]);
        }
        swap16(o[0][0], o[1][0]); swap16(o[0][1], o[1][1]);
        bs16<XA>(u32x4{o[0][0], o[0][1], o[1][0], o[1][1]}, wr, (lane & 31) * 32 + hh * 16, O_GB + (8 * nt + w) * GTS + (4 + 32 * rb) * 32);
    }
    SB();
    BT(24);
#pragma unroll
    for (int k = P_W2A; k < P_W2A + P_W2B; ++k) ldw2(k);
    BT(25);
    waitvm<P_W2B>();
    BT(26);
    bar();
    BT(27);
    if (w == 0 && lane == 0) bs4<16>(1u, wr, 0, F_B + (rb + 1) * 128 + nt * 4);
    STAMP(1, 3);
#pragma unroll
    for (int k = P_W2A + P_W2B; k < P_W2A + P_W2B + P_W2C; ++k) ldw2(k);
    BT(28);
    {
        const int pr = rb + (lane >> 2);
        poll(wr, F_B + (lane < 12 ? pr * 128 + (lane & 3) * 4 : 0), (lane < 12 && pr >= 1 && pr <= 64) ? 1u : 0u);
    }
    BT(29);
    u32x4 G[5];
    auto gaddr = [&](int n) AI { const int gr = n / 9, sh = gr == 0 ? 1 : gr == 1 ? -1 : gr == 2 ? 2 : -2; return O_GB + n * GTS + (4 + 32 * rb - sh) * 32; };
#pragma unroll
    for (int m = 0; m < 5; ++m) { G[m] = bl16<XA>(wr, m < 4 ? l16 : h16v, gaddr(m < 4 ? 8 * m + w : 32 + (w >> 1))); SB(); }
    const unsigned gw = lb + L_F + (((lane >> 1) & 15) + 16 * (lane & 1)) * 16;
#pragma unroll
    for (int m = 0; m < 4; ++m) ldsw<u32x4>(gw + (w >> 1) * 2048 + hh * 1024 + (w & 1) * 512 + m * 8192, G[m]);
    if (lane < 32) ldsw<u32x4>(gw + (16 + (w >> 2)) * 2048 + (w & 1) * 1024 + ((w >> 1) & 1) * 512, G[4]);
    SB();
#pragma unroll
    for (int k = P_W2A + P_W2B + P_W2C; k < 24; ++k) ldw2(k);
    BT(30);
    bar();
    BT(31);
    xread();
    SB();
    slices(W2 + 3);
    ldsw<f32x4>(pw, acc[0]); ldsw<f32x4>(pw + 1024, acc[1]);
    slices(W2);
    {
        float a = acc[0][0] + acc[0][1], b = acc[1][0] + acc[1][1];
        swap16f(a, b);
        if (lane < 32) ldsw<float>(lb + L_MN + w * 128 + lane * 4, a);
    }
    SB();
    BT(32);
    float mn[2];
    f32x4 hr[2], rese;
    auto quad = [&](float t) AI { return u32x4{__float_as_uint(t), __float_as_uint(dppf<0x55>(t)), __float_as_uint(dppf<0xAA>(t)), __float_as_uint(dppf<0xFF>(t))}; };
    gemm_small(W2 + 6, fa, acc, [&](auto K) AI {
        constexpr int k = K;
        if constexpr (k == P_KC) {
            asm volatile("s_barrier" ::: "memory");
#pragma unroll
            for (int i = 0; i < 4; ++i) tv[i] = ldsr<float>(lb + L_MN + i * 256 + lane * 4);
        }
        if constexpr (k == P_KC + 2) {
            float t = (tv[0] + tv[1]) + (tv[2] + tv[3]), u = t; swap32f(t, u);
            mn[0] = mn[1] = t + u; swap16f(mn[0], mn[1]); OPAQ(mn[0]);
        }
        if constexpr (k == P_KC + 3) {
            if (w < 2) {
                const f32x4 ae = psum();
                const float h[4] = {bflo(hext[0]), bfhi(hext[0]), bflo(hext[1]), bfhi(hext[1])};
                const float re = w ? r1[1] : r1[0], me = w ? mm[1] : mm[0], ne = w ? mn[1] : mn[0];
                float xc[4];
#pragma unroll
                for (int r = 0; r < 4; ++r) { const float x1 = __builtin_fmaf(h[r] - me, re, ae[r]); xc[r] = x1 - ne; rese[r] = h[r] + x1; }
                unsigned a0 = pk2(xc[0], xc[1]), a1 = pk2(xc[2], xc[3]), b0 = a0, b1 = a1;
                swap16(a0, b0); swap16(a1, b1);
                if (!(lq & 1)) bs16<XA>(u32x4{a0, a1, b0, b1}, wr, hh * 512 + lj * 16, O_XB + (rb * 36 + 32 + nt) * 1024 + w * 256);
                float t = (xc[0] * xc[0] + xc[1] * xc[1]) + (xc[2] * xc[2] + xc[3] * xc[3]), u = t;
                swap16f(t, u); t += u; u = t; swap32f(t, u);
                const u32x4 qv = quad(t + u);
                if (lane < 16 && !(lane & 3)) bs16<XA>(qv, wr, lane * 4, O_SQ + (rb * 36 + 32 + nt) * 128 + 64 * w);
                bs16<0>(__builtin_bit_cast(u32x4, rese), wr, l16, O_R + ((rb * 36 + 32 + nt) * 2 + w) * 1024);
            }
        }
        if constexpr (k >= P_KC + 4 && k < P_KC + 8) {
            constexpr int i = k - (P_KC + 4), mt = i >> 1, r = 2 * (i & 1);
            const float h0 = bflo(hown[i]), h1 = bfhi(hown[i]);
            cz[mt][r] = __builtin_fmaf(h0 - mm[mt], r1[mt], -mn[mt]); cz[mt][r + 1] = __builtin_fmaf(h1 - mm[mt], r1[mt], -mn[mt]);
            hr[mt][r] = h0 + mn[mt]; hr[mt][r + 1] = h1 + mn[mt];
            OPAQ(cz[mt][r]); OPAQ(cz[mt][r + 1]); OPAQ(hr[mt][r]); OPAQ(hr[mt][r + 1]);
        }
        if constexpr (k >= 18 - P_PRA) prime(k - (18 - P_PRA));
    });
    BT(33);
    {
        unsigned o[2][2]; float q2[2]; f32x4 xc[2];
#pragma unroll
        for (int mt = 0; mt < 2; ++mt) { xc[mt] = acc[mt] + cz[mt]; o[mt][0] = pk2(xc[mt][0], xc[mt][1]); o[mt][1] = pk2(xc[mt][2], xc[mt][3]); }
        swap16(o[0][0], o[1][0]); swap16(o[0][1], o[1][1]);
        bs16<XA>(u32x4{o[0][0], o[0][1], o[1][0], o[1][1]}, wr, l16, O_XB + (rb * 36 + 8 * nt + w) * 1024);
        SB();
#pragma unroll
        for (int mt = 0; mt < 2; ++mt) { const f32x4 t = xc[mt] * xc[mt]; q2[mt] = (t[0] + t[1]) + (t[2] + t[3]); }
        {
            swap16f(q2[0], q2[1]);
            float t = q2[0] + q2[1], u = t; swap32f(t, u);
            const u32x4 qv = quad(t + u);
            if (lane < 32 && !(lane & 3)) bs16<XA>(qv, wr, lane * 4, O_SQ + (rb * 36 + 8 * nt + w) * 128);
        }
        SB();
        BT(34);
#pragma unroll
        for (int i = P_PRA; i < P_PRA + P_PRB; ++i) prime(i);
        BT(35);
        waitvm<P_PRB>();
        BT(36);
        bar();
        BT(37);
        if (w == 0 && lane == 0) bs4<16>(1u, wr, 0, F_C + rt * 128 + (kap * 4 + nt) * 4);
#pragma unroll
        for (int i = P_PRA + P_PRB; i < 10; ++i) prime(i);
#pragma unroll
        for (int mt = 0; mt < 2; ++mt) bs16<0>(__builtin_bit_cast(u32x4, xc[mt] + hr[mt]), wr, l16, O_R + ((rb * 36 + 8 * nt + w) * 2 + mt) * 1024);
    }
    }
    BT(38);
    BDUMP(1);
    STAMP(1, 7);

    unsigned kb[4], kq[4];
#pragma unroll
    for (int i = 0; i < 4; ++i) { const int m = (nt + i) & 3; kb[i] = 36 * kap + 18 * (m >> 1) + 10 * (m & 1); kq[i] = 36 * kap + 18 * (m >> 1) + 8 + (m & 1); }
    const unsigned kpa = lb + L_ST + wm * 512 + (lane & 31) * 16;
    if (w < 4) {
        poll(wr, F_C + rt * 128 + (wm * 4 + (lane & 3)) * 4, 1u);
        STAMP(2, 0);
        u32x4 X[36];
#pragma unroll
        for (int i = 0; i < P_X0; ++i) X[i] = bl16<XA>(wr, l16, O_XB + ((4 * rt + wm) * 36 + i) * 1024);
        asm volatile("s_barrier" ::: "memory");
        const int r = 8 * hh + (lj >> 2);
        unsigned ta[2][2], ts;
#pragma unroll
        for (int s = 0; s < 2; ++s)
#pragma unroll
            for (int ab = 0; ab < 2; ++ab) { ta[s][ab] = lb + s * 3 * SLOT + r * 128 + ((ab ^ ((r >> 1) & 1)) * 64) + (lq & 1) * 32 + (lane & 3) * 8; OPAQ(ta[s][ab]); }
        ts = lb + 3 * SLOT + r * 64 + (lq & 1) * 32 + (lane & 3) * 8; OPAQ(ts);
        const int re = 16 * (lq >> 1) + 4 * (lq & 1) + (lj >> 2);
        unsigned am[2][8], ax[4];
        const unsigned amb = lb + re * 256 + ((re & 7) << 5) + (lane & 3) * 8, axb = lb + L_EX + re * 128 + (((re >> 1) & 3) << 5) + (lane & 3) * 8;
        u32x4 H[HD][2];
        const unsigned hvo = l16 + (8 * rt + 2 * wm) * 144 * 1024;
        unsigned hso = (8 * rt + 2 * wm + (lq & 1)) * 144 * 1024 + (lj + 16 * hh) * 16 + O_HB + kb[0] * 1024;
        OPAQ(hso);
        auto ldH = [&](int j, int mt) AI {
            const int t = e_til(j);
            H[j % HD][mt] = bl16<XA>(wr, hvo + mt * 144 * 1024 + (t & 3) * 1024, O_HB + (t < 8 ? kb[e_mem(j)] + (t & 4) : kq[e_mem(j)]) * 1024);
        };
        __builtin_amdgcn_s_setprio(P_PRIO);
        f32x16 acc[4]; f32x4 ec[12][2]; bf16x8 A[RD + 1];
        f32x4 kp; float t[8]; u32x4 hp;
        sfor<0, 19>([&](auto C) AI { sfor<0, 36>([&](auto I) AI {
            constexpr int fr = 36 * C + I, fm = fr - RD;
            if constexpr (fr < 648 + RD) {
            constexpr int em = fm - 324, jm = em >= 0 ? HT.st[em] : 0, cm = em >= 0 ? em - HT.fi[jm] : 0;
            constexpr bool z0 = cm < 8 ? jm == 0 : jm == 10;
            constexpr int gm = fm < 73 ? 0 : fm < 325 ? 1 : fm < 325 + 4 * NE ? 2 : 0;
            constexpr int gh = gm == 1 ? (fm - 73) / 18 : gm == 2 ? 14 + (fm - 325) / NE : 0, go = gm == 1 ? (fm - 73) % 18 : gm == 2 ? (fm - 325) % NE : 0, gt = gh >> 1, gi = gt < 8 ? (gt & 3) : 0;
            constexpr bool formA = gm == 2 && ((ACT_E[go][0] >> 3) == 3 || (ACT_E[go][0] >> 3) == 5);
            auto act = [&](auto K) AI {
                constexpr int op = gm == 1 ? ACT_D[go][K] : (gm == 2 && K < 2) ? ACT_E[go][K & 1] : -1;
                if constexpr (op >= 0) {
                    constexpr int kd = op >> 3, e = op & 7;
                    const float x = acc[gi][8 * (gh & 1) + e];
                    if constexpr (kd == 0) t[e] = x * x;
                    if constexpr (kd == 1) t[e] = __builtin_fmaf(t[e], kp[1], kp[0]);
                    if constexpr (kd == 2 || kd == 6) t[e] = t[e] * x;
                    if constexpr (kd == 3) t[e] = __builtin_amdgcn_exp2f(t[e]);
                    if constexpr (kd == 4) t[e] = __builtin_fmaf(t[e], kp[2], kp[2]);
                    if constexpr (kd == 5) t[e] = __builtin_amdgcn_rcpf(t[e]);
                    if constexpr (kd < 7) OPAQ(t[e]);
                    if constexpr (kd == 7) { hp[e] = pk2(t[2 * e], t[2 * e + 1]); OPAQ(hp[e]); }
                    if constexpr (kd == 8) { static_assert(s_pos(gh) == fm, "store slot"); bs16<XA>(hp, wr, hso, (gt < 8 ? gt * 1024 : (kq[0] - kb[0]) * 1024) + (gh & 1) * 512); }
                    SB();
                }
            };
            using I0 = std::integral_constant<int, 0>; using I1 = std::integral_constant<int, 1>; using I2 = std::integral_constant<int, 2>; using I3 = std::integral_constant<int, 3>;
            auto mf1 = [&]() AI { ec[cm][1] = mfma16(A[(fm + RD + 1) % (RD + 1)], asb(H[jm % HD][1]), z0 ? f32x4{0, 0, 0, 0} : ec[cm][1]); SB(); };
            if constexpr (em >= 0) ec[cm][0] = mfma16(A[fm % (RD + 1)], asb(H[jm % HD][0]), z0 ? f32x4{0, 0, 0, 0} : ec[cm][0]);
            if constexpr (fm >= 0 && fm < 324) {
                constexpr int u = fm / 18, o = fm % 18, ks = u < 16 ? 9 * (u & 3) + (o >> 1) : 18 * (u & 1) + o, ai = u < 16 ? ((u >> 2) & 1) * 2 + (o & 1) : 0;
                f32x16 z = {};
                acc[ai] = __builtin_amdgcn_mfma_f32_32x32x16_bf16(A[fm % (RD + 1)], asb(X[ks]), ks == 0 ? z : acc[ai], 0, 0, 0);
            }
            SB();
            if constexpr (formA) { act(I0{}); act(I1{}); mf1(); }
            s16x4 t0, t1;
            unsigned a = 0, a2 = 0;
            constexpr int er = fr - 324, jr = er >= 0 ? HT.st[er] : 0, cr = er >= 0 ? er - HT.fi[jr] : 0;
            constexpr int bu = fr < 324 ? (fr % 18 == BP ? fr / 18 : -1) : (fr < 648 && !(jr & 1) && cr == BP) ? 18 + jr / 2 : -1;
            constexpr int nl = fr <= 648 ? 2 * (RD - 2) : 2 * (646 - fm) > 0 ? 2 * (646 - fm) : 0;
            if constexpr (bu >= 0) {
                static_assert(b_slot(bu) == fm, "barrier slot");
                constexpr int vw = f_lvl(bu) ? m_wait(LH[(f_lvl(bu) + 3) & 3], bu) : 63;
                if constexpr (vw < 63) waitvm<vw>();
                asm volatile("s_barrier" ::: "memory");
                SB();
            }
            if constexpr (fm >= 0 && fm < 647) { __builtin_amdgcn_s_waitcnt(wcnt(HT.vm[fm], nl)); SB(); }
            if constexpr (fr < 324) {
                constexpr int u = fr / 18, o = fr % 18, sl = u % NSL;
                if constexpr (u < 16) { a = ta[sl / 3][o & 1] + (sl % 3) * SLOT + (o >> 1) * 2048; a2 = a + 512; }
                else { a = ts + (sl - 3) * SLOT + o * 1024; a2 = a + 256; }
            } else if constexpr (fr < 648) {
                constexpr int sl = (18 + (jr >> 1)) % NSL;
                if constexpr (cr < 8) { a = am[sl / 3][cr & 7] + (sl % 3) * SLOT + (jr & 1) * 8192; a2 = a + 2048; }
                else { a = ax[cr & 3] + e_til(jr) * 4096; a2 = a + 1024; }
            }
            if constexpr (fr < 648) { t0 = __builtin_amdgcn_ds_read_tr16_b64_v4i16((LAS s16x4*)(uintptr_t)a); SB(); }
            if constexpr (em >= 0 && !formA) { act(I0{}); if constexpr (gm == 1) act(I1{}); mf1(); }
            if constexpr (fr < 648) {
                t1 = __builtin_amdgcn_ds_read_tr16_b64_v4i16((LAS s16x4*)(uintptr_t)a2);
                A[fr % (RD + 1)] = __builtin_bit_cast(bf16x8, __builtin_shufflevector(t0, t1, 0, 1, 2, 3, 4, 5, 6, 7));
                SB();
            }
            if constexpr (bu == 3) { kp = ldsr<f32x4>(kpa); SB(); }
            if constexpr (em < 0) { act(I0{}); act(I1{}); }
            if constexpr (em >= 0 && gm == 2 && !formA) act(I1{});
            act(I2{}); act(I3{});
            if constexpr (fm >= 299 && fm < 315 && (fm & 1)) { constexpr int c = (fm - 299) >> 1; am[0][c] = amb ^ (c << 5); OPAQ(am[0][c]); }
            if constexpr (fm >= 330 && fm < 346 && !(fm & 1)) { constexpr int c = (fm - 330) >> 1; am[1][c] = am[0][c] + 3 * SLOT; OPAQ(am[1][c]); }
            if constexpr (fm >= 390 && fm < 398 && !(fm & 1)) { constexpr int c = (fm - 390) >> 1; ax[c] = axb ^ (c << 5); OPAQ(ax[c]); }
            if constexpr (fm >= 0 && x_at(fm) >= 0) X[x_at(fm)] = bl16<XA>(wr, l16, O_XB + ((4 * rt + wm) * 36 + x_at(fm)) * 1024);
            constexpr int l0 = fm >= 0 ? HT.at[0][fm] : -1, l1 = fm >= 0 ? HT.at[1][fm] : -1;
            if constexpr (l0 >= 0) ldH(l0 >> 1, l0 & 1);
            if constexpr (l1 >= 0) ldH(l1 >> 1, l1 & 1);
            SB();
            }
        }); });
        __builtin_amdgcn_s_setprio(0);
        STAMP(2, 2);
#pragma unroll
        for (int c = 0; c < 12; ++c) {
            const u32x4 o = {pk2(ec[c][0][0], ec[c][0][1]), pk2(ec[c][0][2], ec[c][0][3]), pk2(ec[c][1][0], ec[c][1][1]), pk2(ec[c][1][2], ec[c][1][3])};
            if (c < 8) bs16<XA>(o, wr, l16, O_PB + (((4 * rt + wm) * 32 + 8 * nt + c) * 4 + kap) * 1024);
            else bs16<XA>(o, wr, l16, O_PX + (((4 * rt + wm) * 4 + c - 8) * 16 + fc) * 1024);
        }
        waitvm<0>();
    } else {
#pragma unroll
        for (int i = 0; i < 5; ++i) dmaD(4, i);
        poll(wr, F_C + rt * 128 + (wm * 4 + (lane & 3)) * 4, 1u);
        STAMP(2, 0);
        asm volatile("s_barrier" ::: "memory");
        f32x4 sqv[5];
        {
            const char* sb = ws + O_SQ + (4 * rt + wm) * 4608;
#pragma unroll
            for (int i = 0; i < 5; ++i) asm volatile("global_load_dwordx4 %0, %1, %2 offset:%3 sc1" : "=v"(sqv[i]) : "v"(i < 4 ? l16 : (l16 & 511) + 1024), "s"(sb), "i"(i < 4 ? i * 1024 : 3072) : "memory");
        }
        const int drow = 4 * wm + lq;
        const unsigned dvo = drow * 1152 + ((lj ^ (2 * (drow & 7))) << 4);
        const char* w4m = w4 + nt * 256;
        auto ksE = [&](int j) AI { return e_til(j) < 8 ? kb[e_mem(j)] + e_til(j) : kq[e_mem(j)]; };
        auto dmaE = [&](int u, int i) AI { const int j = 2 * (u - 18) + (i >> 1);
            dma16(lb + (u % NSL) * SLOT + (j & 1) * 8192 + (wm + 4 * (i & 1)) * 1024, w4m + (size_t)(ksE(j) * 32 + 16 * (i & 1)) * 1152, dvo); };
        const int xrow = lane >> 3;
        const unsigned xvo = xrow * 1152 + (((lane & 7) ^ (2 * ((xrow >> 1) & 3))) << 4);
        auto dmaX = [&](int t) AI { dma16(lb + L_EX + t * 4096 + wm * 1024, w4 + 1024 + (size_t)((t < 8 ? kb[3] + t : kq[3]) * 32 + wm * 8) * 1152, xvo); };
        const unsigned fdo = F_D + rt * 128 + kap * 16;
        unsigned pv = 0, pfd = 0;
        const unsigned pfo = (8 * rt + 2 * wm + ((lane >> 3) & 1)) * 144 * 1024 + (lane & 7) * 128;
        TDECL; TS(0);
        sfor<0, 36>([&](auto U) AI {
            constexpr int u = U;
            if constexpr (u >= 3) waitvm<g_wait(u)>();
            if constexpr (u == 3) {
#pragma unroll
                for (int i = 0; i < 5; ++i) OPAQ(sqv[i]);
                f32x4 sq = (sqv[0] + sqv[1]) + (sqv[2] + sqv[3]) + 0.5f * sqv[4];
                float t = 0;
#pragma unroll
                for (int r = 0; r < 4; ++r) {
                    float a = sq[r] + dppf<0x128>(sq[r]), b = a;
                    swap16f(a, b); a += b; b = a; swap32f(a, b); a += b;
                    if (((lane >> 3) & 3) == r) t = a;
                }
                const float sg = sqrtf(t * RD576 + EPS), rstd = 1.0f / sg;
                if (lane < 32) ldsw<f32x4>(lb + L_ST + wm * 512 + ((lane & 7) * 4 + (lane >> 3)) * 16, f32x4{GC0 * rstd, GC1 * rstd * rstd * rstd, sg, 0.f});
            }
            if constexpr (q_lvl(u) != 0) {
                OPAQ(pv);
                TS(72 + q_lvl(u));
                if (__builtin_amdgcn_ballot_w64(pv < (unsigned)q_lvl(u)) != 0) poll(wr, fdo + (lane & 3) * 4, q_lvl(u));
            }
            TS(2 * u + 1);
            bar();
            TS(2 * u + 2);
            if constexpr (f_lvl(u) != 0) { if (w == 4 && lane == 0) bs4<16>(f_lvl(u), wr, 0, fdo + nt * 4); }
            if constexpr (g_pre(u)) asm volatile("global_load_dword %0, %1, %2 sc1" : "=v"(pv) : "v"((lane & 3) * 4), "s"(ws + fdo) : "memory");
#pragma unroll
            for (int i = 0; i < g_nd(u); ++i) { if (u + 5 < 18) dmaD(u + 5, i); else dmaE(u + 5, i); SB(); }
            if constexpr (g_nx(u)) dmaX(u - 4);
            if constexpr (g_pf(u) >= 0) asm volatile("global_load_dword %0, %1, %2 sc1" : "+v"(pfd) : "v"(pfo), "s"(ws + O_HB + (size_t)(g_pf(u) < 8 ? kb[0] + g_pf(u) : kq[0]) * 1024) : "memory");
            SB();
        });
        waitvm<0>();
        OPAQ(pfd);
        TDUMP(2, 32);
    }
    bar();
    if (w == 0 && lane == 0) bs4<16>(1u, wr, 0, F_E + rt * 128 + fc * 4);
    STAMP(2, 3);
    {
        const rsrc_t orr = mkrs(out, 2048 * 1152);
        u32x4 P[4], R[2], Q[16], Re[2];
#pragma unroll
        for (int i = 0; i < 2; ++i) R[i] = bl16<16>(wr, l16, O_R + ((rb * 36 + 8 * nt + w) * 2 + i) * 1024);
        if (w == 7) {
#pragma unroll
            for (int i = 0; i < 2; ++i) Re[i] = bl16<16>(wr, l16, O_R + ((rb * 36 + 32 + nt) * 2 + i) * 1024);
        }
        poll(wr, F_E + rt * 128 + lj * 4, (w == 7 || (lj & 3) == nt) ? 1u : 0u);
        STAMP(2, 4);
#pragma unroll
        for (int i = 0; i < 4; ++i) P[i] = bl16<XA>(wr, l16, O_PB + ((rb * 32 + 8 * nt + w) * 4 + i) * 1024);
        if (w == 7) {
#pragma unroll
            for (int i = 0; i < 16; ++i) Q[i] = bl16<XA>(wr, l16, O_PX + ((rb * 4 + nt) * 16 + i) * 1024);
        }
        auto fin = [&](u32x4* p, int n, u32x4* r, int tile) AI {
#pragma unroll
            for (int mt = 0; mt < 2; ++mt) {
                float a[4] = {0, 0, 0, 0};
                for (int i = 0; i < n; ++i) { a[0] += bflo(p[i][2 * mt]); a[1] += bfhi(p[i][2 * mt]); a[2] += bflo(p[i][2 * mt + 1]); a[3] += bfhi(p[i][2 * mt + 1]); }
                bs8<0>(u32x2{pk2(a[0] + __uint_as_float(r[mt][0]), a[1] + __uint_as_float(r[mt][1])), pk2(a[2] + __uint_as_float(r[mt][2]), a[3] + __uint_as_float(r[mt][3]))},
                       orr, lj * 1152 + lq * 8, (32 * rb + 16 * mt) * 1152 + tile * 32);
            }
        };
        fin(P, 4, R, 8 * nt + w);
        if (w == 7) fin(Q, 16, Re, 32 + nt);
    }
    STAMP(2, 7);
}

__global__ __launch_bounds__(256) void k3(char* __restrict__ ws) {
    const u64 t3 = __builtin_amdgcn_s_memrealtime();
    __shared__ u64 red[8][256];
    const u64* S = (const u64*)(ws + O_DBG);
    const int b = threadIdx.x;
    auto mn = [&](int k, int i, int nw) { u64 m = ~0ull; for (int w = 0; w < nw; ++w) { u64 t = S[((k * 256 + b) * 8 + w) * 8 + i]; m = t < m ? t : m; } return m; };
    auto mxx = [&](int k, int i, int nw) { u64 m = 0; for (int w = 0; w < nw; ++w) { u64 t = S[((k * 256 + b) * 8 + w) * 8 + i]; m = t > m ? t : m; } return m; };
    red[0][b] = mn(0, 0, 8); red[1][b] = mxx(0, 7, 8); red[2][b] = mn(1, 0, 8); red[3][b] = mxx(2, 0, 4); red[4][b] = mxx(2, 2, 4); red[5][b] = mxx(2, 7, 8); red[6][b] = mxx(1, 3, 8);
    __syncthreads();
    if (b == 0) {
        u64 r[7];
        for (int q = 0; q < 7; ++q) { u64 m = red[q][0]; for (int i = 1; i < 256; ++i) { u64 t = red[q][i]; if (q == 0 || q == 2) m = t < m ? t : m; else m = t > m ? t : m; } r[q] = m; }
        long long x = SIDEQ;
        if (x < 0) x = 0; if (x > 20000) x = 20000;
        const u64 tgt = t3 + 1000 + x;
        for (int i = 0; i < (1 << 20); ++i) { if (__builtin_amdgcn_s_memrealtime() >= tgt) break; }
    }
}

extern "C" void kernel_launch(void* const* d_in, const int* in_sizes, int n_in, void* d_out, int out_size, void* d_ws, size_t ws_size, hipStream_t stream) {
    char* ws = (char*)d_ws;
    (void)hipFuncSetAttribute((const void*)k1, hipFuncAttributeMaxDynamicSharedMemorySize, K1_LDS);
    (void)hipFuncSetAttribute((const void*)k2, hipFuncAttributeMaxDynamicSharedMemorySize, K2_LDS);
    k1<<<256, 512, K1_LDS, stream>>>((const char*)d_in[0], (const char*)d_in[1], (const char*)d_in[2], (const char*)d_in[3], (const char*)d_in[4], (const u16*)d_in[5],
                                     (const char*)d_in[7], (const char*)d_in[8], (const char*)d_in[9], (const char*)d_in[10], ws);
    k2<<<256, 512, K2_LDS, stream>>>((const char*)d_in[9], (const char*)d_in[10], ws, (char*)d_out);
    k3<<<1, 256, 0, stream>>>(ws);
}
```

```cpp
#include <hip/hip_runtime.h>
#include <stdint.h>
#include <utility>
#define LAS __attribute__((address_space(3)))
typedef short s16x4 __attribute__((ext_vector_type(4)));
typedef __bf16 bf16x8 __attribute__((ext_vector_type(8)));
typedef __bf16 bf16x2 __attribute__((ext_vector_type(2)));
typedef float f32x16 __attribute__((ext_vector_type(16)));
typedef float f32x4 __attribute__((ext_vector_type(4)));
typedef float f32x2 __attribute__((ext_vector_type(2)));
typedef unsigned u32x4 __attribute__((ext_vector_type(4)));
typedef unsigned u32x2 __attribute__((ext_vector_type(2)));
typedef unsigned long long u64;
typedef unsigned short u16;
typedef __amdgpu_buffer_rsrc_t rsrc_t;
#define DEVI __device__ __forceinline__
#define AI __attribute__((always_inline))
#define SB() __builtin_amdgcn_sched_barrier(0)
#define OPAQ(x) asm volatile("" : "+v"(x))

DEVI unsigned pk2(float a, float b) { f32x2 p = {a, b}; return __builtin_bit_cast(unsigned, __builtin_convertvector(p, bf16x2)); }
DEVI float bflo(unsigned x) { return __uint_as_float(x << 16); }
DEVI float bfhi(unsigned x) { return __uint_as_float(x & 0xffff0000u); }
DEVI bf16x8 asb(u32x4 x) { return __builtin_bit_cast(bf16x8, x); }
constexpr float GC0 = -2.0f * 0.7978845608028654f * 1.4426950408889634f, GC1 = GC0 * 0.044715f;
DEVI float gelu(float x) {
    float e = __builtin_amdgcn_exp2f(x * __builtin_fmaf(x * x, GC1, GC0));
    return x * __builtin_amdgcn_rcpf(1.0f + e);
}
DEVI void dma16(unsigned ldsaddr, const void* sbase, unsigned voff) {
    asm volatile("s_mov_b32 m0, %0\n\ts_nop 0\n\tglobal_load_lds_dwordx4 %1, %2" :: "s"(ldsaddr), "v"(voff), "s"(sbase) : "memory");
}
DEVI void dma16h(unsigned ldsaddr, const void* sbase, unsigned voff) {
    asm volatile("s_mov_b32 m0, %0\n\ts_mov_b32 exec_hi, 0\n\tglobal_load_lds_dwordx4 %1, %2\n\ts_mov_b32 exec_hi, -1" :: "s"(ldsaddr), "v"(voff), "s"(sbase) : "memory");
}
template <int N> DEVI void waitvm() { asm volatile("s_waitcnt vmcnt(%0)" :: "i"(N) : "memory"); }
DEVI void bar() { asm volatile("s_waitcnt lgkmcnt(0)\n\ts_barrier" ::: "memory"); }
DEVI bf16x8 trfrag(unsigned a0, unsigned a1) {
    s16x4 a = __builtin_amdgcn_ds_read_tr16_b64_v4i16((LAS s16x4*)(uintptr_t)a0);
    s16x4 b = __builtin_amdgcn_ds_read_tr16_b64_v4i16((LAS s16x4*)(uintptr_t)a1);
    return __builtin_bit_cast(bf16x8, __builtin_shufflevector(a, b, 0, 1, 2, 3, 4, 5, 6, 7));
}
template <int B, class F, int... I> DEVI void sfor_impl(F&& f, std::integer_sequence<int, I...>) { (f(std::integral_constant<int, B + I>{}), ...); }
template <int B, int E, class F> DEVI void sfor(F&& f) { sfor_impl<B>(f, std::make_integer_sequence<int, E - B>{}); }
template <class T> DEVI T ldsr(unsigned a) { return *(LAS T*)(uintptr_t)a; }
template <class T> DEVI void ldsw(unsigned a, T v) { *(LAS T*)(uintptr_t)a = v; }
DEVI rsrc_t mkrs(const void* p, unsigned bytes) { return __builtin_amdgcn_make_buffer_rsrc((void*)p, 0, bytes, 0x00020000); }
template <int AUX> DEVI u32x4 bl16(rsrc_t r, unsigned vo, unsigned so) { return __builtin_amdgcn_raw_buffer_load_b128(r, vo, so, AUX); }
template <int AUX> DEVI u32x2 bl8(rsrc_t r, unsigned vo, unsigned so) { return __builtin_amdgcn_raw_buffer_load_b64(r, vo, so, AUX); }
template <int AUX> DEVI unsigned bl4(rsrc_t r, unsigned vo, unsigned so) { return __builtin_amdgcn_raw_buffer_load_b32(r, vo, so, AUX); }
template <int AUX> DEVI void bs16(u32x4 v, rsrc_t r, unsigned vo, unsigned so) { __builtin_amdgcn_raw_buffer_store_b128(v, r, vo + so, 0, AUX); }
template <int AUX> DEVI void bs8(u32x2 v, rsrc_t r, unsigned vo, unsigned so) { __builtin_amdgcn_raw_buffer_store_b64(v, r, vo, so, AUX); }
template <int AUX> DEVI void bs4(unsigned v, rsrc_t r, unsigned vo, unsigned so) { __builtin_amdgcn_raw_buffer_store_b32(v, r, vo, so, AUX); }
DEVI void swap16(unsigned& a, unsigned& b) { auto r = __builtin_amdgcn_permlane16_swap(a, b, false, false); a = r[0]; b = r[1]; }
DEVI void swap32(unsigned& a, unsigned& b) { auto r = __builtin_amdgcn_permlane32_swap(a, b, false, false); a = r[0]; b = r[1]; }
DEVI void swap16f(float& a, float& b) { unsigned x = __float_as_uint(a), y = __float_as_uint(b); swap16(x, y); a = __uint_as_float(x); b = __uint_as_float(y); }
DEVI void swap32f(float& a, float& b) { unsigned x = __float_as_uint(a), y = __float_as_uint(b); swap32(x, y); a = __uint_as_float(x); b = __uint_as_float(y); }
template <int CTRL> DEVI float dppf(float x) { return __uint_as_float(__builtin_amdgcn_update_dpp(0u, __float_as_uint(x), CTRL, 0xf, 0xf, false)); }
DEVI float rowsum16(float x) { x += dppf<0x128>(x); x += dppf<0x124>(x); x += dppf<0x122>(x); x += dppf<0x121>(x); return x; }
DEVI f32x4 mfma16(bf16x8 a, bf16x8 b, f32x4 c) { return __builtin_amdgcn_mfma_f32_16x16x32_bf16(a, b, c, 0, 0, 0); }

constexpr unsigned CLSZ = 64 * 36 * 1024;
constexpr unsigned GTS = 2056 * 32;
constexpr unsigned O_FLAG = 0, O_YB = 65536, O_XT = O_YB + CLSZ, O_XS = O_XT + CLSZ, O_W1T = O_XS + 16384, O_W2T = O_W1T + 663552,
                   O_CSP = O_W2T + 663552, O_W2M = O_CSP + 41472, O_GB = O_W2M + 2304, O_HT = O_GB + 36 * GTS, O_HS = O_HT + CLSZ,
                   O_XB = O_HS + 65536, O_SQ = O_XB + CLSZ, O_R = O_SQ + 36 * 2048 * 4, O_HB = O_R + 2 * CLSZ, O_PB = O_HB + 128 * 144 * 1024,
                   O_PX = O_PB + 64 * 32 * 4 * 1024, O_END = O_PX + 64 * 4 * 16 * 1024, O_DBG = O_END;
constexpr float EPS = 1e-5f, RD576 = 1.0f / 576.0f;
#define XA 16
#define TDECL
#define BDECL
#define BT(i) do {} while (0)
#define BDUMP(kid) do {} while (0)
#define TS(i) do {} while (0)
#define TDUMP(kid, n) do {} while (0)
#define STAMP(kid, i) do {} while (0)
#define CSTAMP(kid, i) do {} while (0)

#define P_CPF 1
#define P_KPF 1
constexpr int K1_KC = 0, K1_UR = 33792, K1_P = K1_UR + 18432, K1_LDS = K1_P + 32768;
__global__ __launch_bounds__(512, 2) void k1(const char* __restrict__ xq, const char* __restrict__ kk, const char* __restrict__ vv, const char* __restrict__ shk,
                                             const char* __restrict__ ssk, const u16* __restrict__ ssd, const char* __restrict__ w1, const char* __restrict__ w2,
                                             const char* __restrict__ w3, const char* __restrict__ w4, char* __restrict__ ws) {
    extern __shared__ __attribute__((aligned(256))) char smem[];
    const unsigned lb = (unsigned)(uintptr_t)(LAS char*)smem;
    const int wg = blockIdx.x, xcd = wg & 7, loc = wg >> 3, tid = threadIdx.x, lane = tid & 63;
    const int w = __builtin_amdgcn_readfirstlane(tid >> 6);
    const rsrc_t wr = mkrs(ws, O_END);
    STAMP(0, 0);
    if (loc < 18) {
        const int cw = 18 * xcd + loc;
        const int kch = tid >> 7, tau = tid & 127;
        const rsrc_t sr = mkrs(ssk, 576 * 4096);
        const unsigned so = (4 * cw + kch) * 4096 + tau * 32;
        u32x4 K0 = bl16<0>(sr, so, 0), K1 = bl16<0>(sr, so + 16, 0);
        unsigned K8 = bl4<0>(sr, so + 32, 0);
        const float dv = bflo(ssd[4 * cw + kch]);
        const rsrc_t kr = mkrs(kk, 2048 * 1152), vr = mkrs(vv, 2048 * 1152);
        u32x2 ks[4], vs[4];
        auto strip = [&](int g) AI { SB(); ks[g] = bl8<0>(kr, tid * 1152 + cw * 8, g * 512 * 1152); SB(); vs[g] = bl8<0>(vr, tid * 1152 + cw * 8, g * 512 * 1152); SB(); };
        strip(0);
        asm volatile("s_barrier" ::: "memory");
        strip(1);
        u64 halo[4][3];
#pragma unroll
        for (int g = 0; g < 4; ++g)
#pragma unroll
            for (int i = 0; i < 3; ++i) { int r = 512 * g + 64 * w - 1 - i; u64 t = *(const u64*)(kk + (size_t)(r < 0 ? 0 : r) * 1152 + cw * 8); halo[g][i] = r < 0 ? 0ull : t; }
        const u32x4 sa = *(const u32x4*)(shk + cw * 32), sb = *(const u32x4*)(shk + cw * 32 + 16);
        if (tid < 120) ldsw<u32x4>(lb + K1_UR + (tid / 30) * 4608 + 4096 + (tid % 30) * 16, u32x4{0, 0, 0, 0});
        if (tau == 0) K0[0] = (K0[0] & 0xffff0000u) | (pk2(bflo(K0[0]) + dv, 0.f) & 0xffffu);
        {
            const unsigned c0 = lb + K1_KC + kch * 8448 + 32 + tau * 32, c1 = c0 + 4224;
            ldsw<u32x4>(c0, K0); ldsw<u32x4>(c0 + 16, K1);
            ldsw<u32x4>(c1, u32x4{__builtin_amdgcn_alignbit(K0[1], K0[0], 16), __builtin_amdgcn_alignbit(K0[2], K0[1], 16), __builtin_amdgcn_alignbit(K0[3], K0[2], 16), __builtin_amdgcn_alignbit(K1[0], K0[3], 16)});
            ldsw<u32x4>(c1 + 16, u32x4{__builtin_amdgcn_alignbit(K1[1], K1[0], 16), __builtin_amdgcn_alignbit(K1[2], K1[1], 16), __builtin_amdgcn_alignbit(K1[3], K1[2], 16), __builtin_amdgcn_alignbit(K8, K1[3], 16)});
            if (tau == 0) {
                ldsw<u32x4>(c0 - 32, u32x4{0, 0, 0, 0}); ldsw<u32x4>(c0 - 16, u32x4{0, 0, 0, 0});
                ldsw<u32x4>(c1 - 32, u32x4{0, 0, 0, 0}); ldsw<u32x4>(c1 - 16, u32x4{0, 0, 0, K0[0] << 16});
            }
        }
        SB();
        bar();
        strip(2);
        STAMP(0, 1);
        const int ch = w & 3, hf = w >> 2, li = lane & 15, q = lane >> 4;
        u32x4 A[4][8];
        {
            const int r = (li + 1) & 1;
            unsigned aa = lb + K1_KC + ch * 8448 + r * 4224 + (128 * hf + li + 25 - 8 * q - r) * 2;
            OPAQ(aa);
#pragma unroll
            for (int c0 = 0; c0 < 4; ++c0)
#pragma unroll
                for (int c1 = 0; c1 < 8; ++c1) {
                    const unsigned a = aa + c1 * 512 + c0 * 64;
                    A[c0][c1] = u32x4{ldsr<unsigned>(a), ldsr<unsigned>(a + 4), ldsr<unsigned>(a + 8), ldsr<unsigned>(a + 12)};
                }
        }
        strip(3);
        f32x4 acc[8];
#pragma unroll
        for (int t = 0; t < 8; ++t) acc[t] = f32x4{0, 0, 0, 0};
        unsigned ba = lb + K1_UR + ch * 4608 + (2056 - 1792 - 16 * li + 128 * hf - 8 * q) * 2;
        OPAQ(ba);
        const unsigned ua = lb + K1_UR + (511 - tid) * 2;
        const unsigned pw = lb + K1_P + (hf * 4 + ch) * 2048 + li * 64 + q * 16;
        const unsigned pr = lb + K1_P + ((tid >> 4) * 32 + (tid & 15)) * 4;
        auto output = [&](int g) AI {
            if (tid < 256) {
                float y[2][4];
#pragma unroll
                for (int c = 0; c < 4; ++c)
#pragma unroll
                    for (int m = 0; m < 2; ++m) y[m][c] = ldsr<float>(pr + (g & 1) * 16384 + c * 2048 + m * 64) + ldsr<float>(pr + (g & 1) * 16384 + 8192 + c * 2048 + m * 64);
                bs16<0>(u32x4{pk2(y[0][0], y[0][1]), pk2(y[0][2], y[0][3]), pk2(y[1][0], y[1][1]), pk2(y[1][2], y[1][3])}, wr, (tid >> 4) * 36864 + (tid & 15) * 16, O_YB + g * 16 * 36864 + cw * 256);
            }
        };
        sfor<0, 4>([&](auto G) AI {
            constexpr int g = G;
            unsigned kx[4][2];
            kx[0][0] = ks[g][0]; kx[0][1] = ks[g][1];
#pragma unroll
            for (int i = 1; i < 4; ++i) {
                kx[i][0] = __builtin_amdgcn_update_dpp((unsigned)halo[g][i - 1], kx[i - 1][0], 0x138, 0xf, 0xf, false);
                kx[i][1] = __builtin_amdgcn_update_dpp((unsigned)(halo[g][i - 1] >> 32), kx[i - 1][1], 0x138, 0xf, 0xf, false);
            }
            float s0 = bflo(kx[0][0]) * bflo(sa[0]) + bflo(kx[1][0]) * bfhi(sa[0]) + bflo(kx[2][0]) * bflo(sa[1]) + bflo(kx[3][0]) * bfhi(sa[1]);
            float s1 = bfhi(kx[0][0]) * bflo(sa[2]) + bfhi(kx[1][0]) * bfhi(sa[2]) + bfhi(kx[2][0]) * bflo(sa[3]) + bfhi(kx[3][0]) * bfhi(sa[3]);
            float s2 = bflo(kx[0][1]) * bflo(sb[0]) + bflo(kx[1][1]) * bfhi(sb[0]) + bflo(kx[2][1]) * bflo(sb[1]) + bflo(kx[3][1]) * bfhi(sb[1]);
            float s3 = bfhi(kx[0][1]) * bflo(sb[2]) + bfhi(kx[1][1]) * bfhi(sb[2]) + bfhi(kx[2][1]) * bflo(sb[3]) + bfhi(kx[3][1]) * bfhi(sb[3]);
            const unsigned p01 = pk2(s0 * bflo(vs[g][0]), s1 * bfhi(vs[g][0])), p23 = pk2(s2 * bflo(vs[g][1]), s3 * bfhi(vs[g][1]));
            const unsigned u = ua + (3 - g) * 1024;
            ldsw<u16>(u, (u16)p01); ldsw<u16>(u + 4608, (u16)(p01 >> 16)); ldsw<u16>(u + 9216, (u16)p23); ldsw<u16>(u + 13824, (u16)(p23 >> 16));
            SB();
            bar();
            u32x4 B[8];
#pragma unroll
            for (int i = 0; i < 8; ++i) B[i] = ldsr<u32x4>(ba + (7 - 2 * g - (i >> 2)) * 512 + (i & 3) * 64);
            SB();
            if constexpr (g >= 1) output(g - 1);
            SB();
#pragma unroll
            for (int i = 0; i < 8; ++i) {
                const int b = 2 * g + (i >> 2);
#pragma unroll
                for (int c1 = 0; c1 + b < 8; ++c1) acc[b + c1] = mfma16(asb(A[i & 3][c1]), asb(B[i]), acc[b + c1]);
            }
            SB();
            ldsw<f32x4>(pw + (g & 1) * 16384, acc[2 * g]); ldsw<f32x4>(pw + (g & 1) * 16384 + 1024, acc[2 * g + 1]);
            STAMP(0, 2 + g);
        });
        bar();
        output(3);
    } else {
        const int u = (loc - 18) * 8 + w;
        if (u < 32 || (u >= 73 && u < 82)) {
            const bool isx = u < 32;
            const int rl = lane >> 4, c = lane & 15;
            const int rb = 32 * (xcd >> 2) + 4 * (u >> 2) + (xcd & 3), rho = 4 * (u & 3) + rl;
            const int r0 = isx ? 32 * rb + rho : 8 * (9 * xcd + u - 73) + rl, r1 = r0 + (isx ? 16 : 4);
            const rsrc_t xr = mkrs(isx ? xq : w2, isx ? 2048 * 1152 : 576 * 1152);
            u32x2 a[9], b[9];
#pragma unroll
            for (int m = 0; m < 9; ++m) { a[m] = bl8<0>(xr, r0 * 1152 + c * 8, m * 128); b[m] = bl8<0>(xr, r1 * 1152 + c * 8, m * 128); }
            float sa = 0, sb = 0;
#pragma unroll
            for (int m = 0; m < 9; ++m) {
                sa += (bflo(a[m][0]) + bfhi(a[m][0])) + (bflo(a[m][1]) + bfhi(a[m][1]));
                sb += (bflo(b[m][0]) + bfhi(b[m][0])) + (bflo(b[m][1]) + bfhi(b[m][1]));
                if (isx) bs16<0>(u32x4{a[m][0], a[m][1], b[m][0], b[m][1]}, wr, c * 256 + rho * 16, O_XT + rb * 36864 + m * 4096);
            }
            const float ma = rowsum16(sa) * RD576, mb = rowsum16(sb) * RD576;
            if (isx) {
                float qa = 0, qb = 0;
#pragma unroll
                for (int m = 0; m < 9; ++m)
#pragma unroll
                    for (int i = 0; i < 2; ++i) {
                        float t;
                        t = bflo(a[m][i]) - ma; qa += t * t; t = bfhi(a[m][i]) - ma; qa += t * t;
                        t = bflo(b[m][i]) - mb; qb += t * t; t = bfhi(b[m][i]) - mb; qb += t * t;
                    }
                const float ra = 1.0f / sqrtf(rowsum16(qa) * RD576 + EPS), rbb = 1.0f / sqrtf(rowsum16(qb) * RD576 + EPS);
                if (c == 0) { bs8<0>(u32x2{__float_as_uint(ra), __float_as_uint(-ma * ra)}, wr, r0 * 8, O_XS); bs8<0>(u32x2{__float_as_uint(rbb), __float_as_uint(-mb * rbb)}, wr, r1 * 8, O_XS); }
            } else if (c < 2) {
                const int k = c ? r1 : r0; const float m = c ? mb : ma;
                const int p = (k >> 3) * 32 + (k & 7) * 2;
                const unsigned hi = pk2(m, 0.f) & 0xffffu, lo = pk2(m - bflo(hi), 0.f) & 0xffffu;
                *(u16*)(ws + O_W2M + p) = (u16)hi; *(u16*)(ws + O_W2M + 16 + p) = (u16)lo;
            }
        } else if (u < 73) {
            const int g = 41 * xcd + u - 32;
            if (g < 324) {
                const int mat = g >= 162, r = g - 162 * mat, ks = r / 9, cg = r - 9 * ks;
                const rsrc_t mr = mkrs(mat ? w2 : w1, 576 * 1152);
                u32x4 t[4];
#pragma unroll
                for (int i = 0; i < 4; ++i) t[i] = bl16<0>(mr, (lane >> 3) * 1152 + (lane & 7) * 16, (32 * ks + 8 * i) * 1152 + cg * 128);
                const unsigned lw = lb + w * 4096;
#pragma unroll
                for (int i = 0; i < 4; ++i) ldsw<u32x4>(lw + i * 1024 + lane * 16, t[i]);
                const unsigned ta = lw + ((mat ? 8 : 4) * (lane >> 4) + ((lane & 15) >> 2)) * 128 + (lane & 3) * 8, tb = ta + (mat ? 512 : 2048);
                const u32x4 ones = {0x3f803f80u, 0x3f803f80u, 0x3f803f80u, 0x3f803f80u};
#pragma unroll
                for (int tt = 0; tt < 4; ++tt) {
                    const bf16x8 f = trfrag(ta + tt * 32, tb + tt * 32);
                    bs16<0>(__builtin_bit_cast(u32x4, f), wr, lane * 16, (mat ? O_W2T : O_W1T) + ((4 * cg + tt) * 18 + ks) * 1024);
                    if (!mat) {
                        const f32x4 cs = mfma16(f, asb(ones), f32x4{0, 0, 0, 0});
                        if ((lane & 15) == 0) bs16<0>(__builtin_bit_cast(u32x4, cs), wr, (lane >> 4) * 16, O_CSP + ((4 * cg + tt) * 18 + ks) * 64);
                    }
                }
            }
        } else if (u >= 84) {
            const rsrc_t r3 = mkrs(w3, 576 * 9216), r4 = mkrs(w4, 4608 * 1152);
            for (int i = 0; i < P_KPF; ++i) asm volatile("s_sleep 16");
            unsigned t[12], a = 0;
#pragma unroll
            for (int i = 0; i < 6; ++i) {
                const unsigned l = (i * 28 + u - 84) * 64 + lane, row = l / 18;
                t[i] = bl4<0>(r3, row * 9216 + (l - row * 18) * 128 + (xcd & 3) * 2304, 0);
            }
#pragma unroll
            for (int i = 0; i < 6; ++i) t[6 + i] = bl4<0>(r4, ((i * 28 + u - 84) * 64 + lane) * 128 + (xcd & 3) * (1152 * 1152), 0);
#pragma unroll
            for (int i = 0; i < 12; ++i) a ^= t[i];
            asm volatile("" :: "v"(a));
        } else if (u == 83) {
            {
                u64 pc; unsigned len;
                asm volatile("s_getpc_b64 %0\n.Lcode_lo:\n\ts_mov_b32 %1, .Lcode_hi-.Lcode_lo" : "=s"(pc), "=s"(len));
                const rsrc_t cr = mkrs((const void*)(pc & ~127ull), len);
                unsigned t[8], a = 0;
#pragma unroll
                for (int i = 0; i < 8; ++i) t[i] = bl4<0>(cr, lane * 128, i * 8192);
#pragma unroll
                for (int i = 0; i < 8; ++i) a ^= t[i];
                asm volatile("" :: "v"(a));
            }
            bs16<0>(u32x4{0, 0, 0, 0}, wr, lane * 16, O_FLAG + xcd * 2048); bs16<0>(u32x4{0, 0, 0, 0}, wr, lane * 16, O_FLAG + xcd * 2048 + 1024);
        } else if (u == 82) {
            const int l = 9 * xcd + (lane >> 3);
            bs16<0>(u32x4{0, 0, 0, 0}, wr, (l >> 1) * GTS + (l & 1) * (2052 * 32) + (lane & 7) * 16, O_GB);
            const int l2 = 9 * xcd + 8;
            if (lane < 8) bs16<0>(u32x4{0, 0, 0, 0}, wr, (l2 >> 1) * GTS + (l2 & 1) * (2052 * 32) + lane * 16, O_GB);
        }
    }
    STAMP(0, 7);
}

constexpr int SLOT = 18432, NSL = 6, L_EX = NSL * SLOT, L_F = L_EX, L_ST = L_EX + 36864, L_MN = L_ST + 2048, K2_LDS = L_MN + 1024, L_PE = 5 * SLOT;
constexpr unsigned F_B = O_FLAG, F_C = O_FLAG + 66 * 128, F_D = F_C + 16 * 128, F_E = F_D + 16 * 128;
#define P_RD 4
#define P_B0 0
#define P_WBS {0, 2, 5, 8, 11, 14}
#define P_KB 8
#define P_KC 3
#define P_W2A 6
#define P_W2B 6
#define P_W2C 6
#define P_PRA 3
#define P_PRB 4
#define P_X0 10
#define P_PRIO 2
constexpr int RD = P_RD, BP = RD + 1, HD = 8;
constexpr int SPIN = 1 << 17;

template <class HK> DEVI void gemm_small(const u32x4* W, unsigned fa, f32x4* acc, HK&& hook) {
    constexpr int PF = 3;
    u32x4 b[PF + 1][2];
    sfor<0, 18 + PF>([&](auto K) AI {
        constexpr int k = K, m = k - PF;
        if constexpr (m >= 0) {
            const f32x4 z = {0, 0, 0, 0};
            acc[0] = mfma16(asb(W[m]), asb(b[m % (PF + 1)][0]), m ? acc[0] : z); acc[1] = mfma16(asb(W[m]), asb(b[m % (PF + 1)][1]), m ? acc[1] : z);
            SB();
        }
        if constexpr (k < 18) { b[k % (PF + 1)][0] = ldsr<u32x4>(fa + k * 2048); b[k % (PF + 1)][1] = ldsr<u32x4>(fa + k * 2048 + 1024); SB(); }
        if constexpr (m >= 0) { hook(std::integral_constant<int, m>{}); SB(); }
    });
}
DEVI void poll(rsrc_t wr, unsigned off, unsigned need) {
#pragma unroll 1
    for (int i = 0; i < SPIN; ++i) {
        const unsigned v = bl4<16>(wr, off, 0);
        if (__builtin_amdgcn_ballot_w64(v < need) == 0) break;
        asm volatile("s_sleep 1" ::: "memory");
    }
    asm volatile("" ::: "memory");
}
constexpr int e_mem(int j) { return j < 4 ? 0 : j < 13 ? 1 + (j - 4) / 3 : j < 16 ? 0 : j < 25 ? 1 + (j - 16) / 3 : j < 27 ? 0 : j < 33 ? 1 + (j - 27) / 2 : j - 32; }
constexpr int e_til(int j) { return j < 4 ? j : j < 13 ? (j - 4) % 3 : j < 16 ? j - 9 : j < 25 ? 3 + (j - 16) % 3 : j < 27 ? j - 18 : j < 33 ? 6 + (j - 27) % 2 : 8; }
constexpr int e_first(int j) { int f = 0; for (int i = 0; i < j; ++i) f += e_mem(i) == 3 ? 12 : 8; return f; }
#define P_FB {12, 18, 24, 28}
#define P_QB {17, 23, 29, 32}
#define P_SD 2
#define P_TP 1
constexpr int FB[4] = P_FB, QB[4] = P_QB, LH[4] = {5, 11, 15, 17}, LJ[5] = {4, 16, 27, 33, 99};
constexpr int f_lvl(int u) { for (int l = 0; l < 4; ++l) if (FB[l] == u) return l + 1; return 0; }
constexpr int q_lvl(int u) { for (int l = 0; l < 4; ++l) if (QB[l] == u) return l + 1; return 0; }
constexpr signed char ACT_D[18][4] = {{0,1,8,-1},{16,9,24,-1},{32,17,40,-1},{25,2,48,10},{33,18,41,-1},{26,3,49,11},{34,19,42,-1},{27,4,50,12},{35,20,43,-1},{28,5,51,13},
                                      {36,21,44,-1},{29,6,52,14},{37,22,45,-1},{30,7,53,15},{38,23,46,-1},{31,54,56,-1},{39,57,47,-1},{55,58,59,64}};
constexpr int NE = 33;
constexpr signed char ACT_E[NE][2] = {{0,8},{16,1},{24,9},{32,17},{40,2},{25,48},{33,10},{41,18},{26,49},{56,34},{42,3},{50,11},{19,4},{27,12},{35,20},{43,5},{28,51},{57,36},{44,13},{52,21},{29,6},{37,14},{45,22},{30,53},{58,38},{46,7},{54,15},{23,-1},{31,-1},{39,-1},{47,-1},{55,59},{64,-1}};
constexpr int b_slot(int u) { return u < 18 ? 18 * u + 1 : 324 + e_first(2 * (u - 18)) + 1; }
constexpr int mx(int a, int b) { return a > b ? a : b; }
constexpr int h_pos(int j, int mt) {
    int p = j < 8 ? 290 + 4 * j + 2 * mt : 324 + e_first(j - 7) + 2 + 2 * mt;
    if (e_mem(j) == 0) { if (j == 26) p = mx(p, b_slot(FB[3]) + 1 + 2 * mt); return p; }
    for (int l = 0; l < 4; ++l) if (j >= LJ[l] && j < LJ[l + 1]) p = mx(p, b_slot(QB[l]) + 1 + 2 * (j - LJ[l]) + mt);
    return p;
}
constexpr int s_pos(int h) { return h < 14 ? 90 + 18 * h : 325 + NE * (h - 13) - 1; }
constexpr int m_wait(int h, int u) {
    int n = 0;
    for (int i = h + 1; i < 18; ++i) n += s_pos(i) < b_slot(u);
    for (int j = 0; j < 36; ++j) for (int mt = 0; mt < 2; ++mt) n += h_pos(j, mt) >= s_pos(h) && h_pos(j, mt) < b_slot(u);
    return n;
}
constexpr int x_at(int s) { return (!(s & 1) && P_X0 + s / 2 < 36) ? P_X0 + s / 2 : -1; }
struct HTab { int at[2][648], st[330], fi[37], vm[648]; bool ok;
    constexpr HTab() : at{}, st{}, fi{}, vm{}, ok(true) {
        int cnt[649] = {}, pre[649] = {}, done[649] = {};
        for (int s = 0; s < 648; ++s) { at[0][s] = at[1][s] = -1; vm[s] = 63; }
        for (int j = 0; j < 37; ++j) fi[j] = e_first(j);
        for (int h = 0; h < 18; ++h) ++cnt[s_pos(h)];
        for (int l = 0; l < 72; ++l) { const int p = h_pos(l >> 1, l & 1); ++cnt[p]; if (at[0][p] < 0) at[0][p] = l; else if (at[1][p] < 0) at[1][p] = l; else ok = false; if (p >= 323 + fi[l >> 1]) ok = false; }
        for (int s = 0; s < 648; ++s) pre[s + 1] = pre[s] + cnt[s];
        for (int e = 0, j = 0; e < 330; ++e) { while (j < 35 && fi[j + 1] <= e) ++j; st[e] = j; }
        for (int j = 0; j < 36; ++j) {
            const int p = h_pos(j, 1), s = 323 + fi[j];
            if (p < h_pos(j, 0)) ok = false;
            const int n = pre[s] - pre[p + 1] + (at[0][p] == 2 * j + 1 && at[1][p] >= 0 ? 1 : 0);
            if (n >= 63) ok = false;
            vm[s] = n; done[s] = pre[s] - n;
        }
        for (int l = 0; l < 4; ++l) { const int s = b_slot(FB[l]); done[s] = pre[s] - m_wait(LH[l], FB[l]); if (s_pos(LH[l]) >= s || FB[l] + P_SD >= QB[l]) ok = false; }
        for (int s = 1; s < 648; ++s) done[s] = mx(done[s], done[s - 1]);
        for (int j = 0; j < 36; ++j) if (e_mem(j) == 0 && j >= 4 && done[h_pos(j, 0)] <= pre[s_pos(2 * e_til(j) + 1)]) ok = false;
        for (int k = 1; k < 36; ++k) { int n = P_X0; for (int s = 0; s < 2 * k - 1; ++s) n += x_at(s) >= 0; vm[2 * k - 1] = n - 1 - k; if (n <= k) ok = false; }
        vm[289] = pre[289] - pre[s_pos(7)] - 1;
    } };
constexpr HTab HT{};
static_assert(HT.ok, "schedule");
constexpr int wcnt(int vm, int lgkm) { return (vm & 15) | ((vm >> 4) << 14) | 0x70 | (lgkm << 8); }
constexpr int g_nd(int i) { return i + 5 < 18 ? 5 : i + 5 < 36 ? 4 : 0; }
constexpr int g_nx(int i) { return (i >= 4 && i < 13) ? 1 : 0; }
constexpr int g_pre(int i) { return q_lvl(i + P_SD) ? 1 : 0; }
#define P_PF 8
constexpr int g_pf(int i) { return P_PF == 0 ? -1 : (i >= P_PF && i <= P_PF + 12 && !((i - P_PF) & 1)) ? (i - P_PF) / 2 : i == FB[2] ? 7 : i == FB[3] ? 8 : -1; }
constexpr int g_all(int i) { return g_pre(i) + g_nd(i) + g_nx(i) + (g_pf(i) >= 0); }
constexpr int g_wait(int u) {
    int n = g_nx(u - 4) + (g_pf(u - 4) >= 0) + g_all(u - 3) + g_all(u - 2) + g_all(u - 1);
    if (q_lvl(u)) { n = -1; for (int i = 1; i <= P_SD; ++i) n += g_all(u - i); }
    return n;
}

__global__ __launch_bounds__(512, 2) void k2(const char* __restrict__ w3, const char* __restrict__ w4, char* __restrict__ ws, char* __restrict__ out) {
    extern __shared__ __attribute__((aligned(256))) char smem[];
    const unsigned lb = (unsigned)(uintptr_t)(LAS char*)smem;
    const int wg = blockIdx.x, xcd = wg & 7, loc = wg >> 3, lane = threadIdx.x & 63;
    const int kap = xcd & 3, rt = 8 * (xcd >> 2) + (loc >> 2), nt = loc & 3, rb = 4 * rt + kap, fc = 4 * kap + nt;
    const int w = __builtin_amdgcn_readfirstlane(threadIdx.x >> 6), wm = w & 3;
    const int lj = lane & 15, lq = lane >> 4, hh = lane >> 5;
    const unsigned l16 = lane * 16;
    const rsrc_t wr = mkrs(ws, O_END);
    BDECL; BT(0);
    STAMP(1, 0);
    const int a9 = 9 * (fc >> 1), odd = fc & 1;
    const char* wp = w3 + (a9 + 5 * odd) * 128 + (size_t)wm * 36 * 9216;
    const char* wsg = w3 + (a9 + 4) * 128 + odd * 64 + (size_t)wm * 72 * 9216;
    const unsigned pvo = (lane >> 3) * 9216 + (((lane & 7) ^ (((lane >> 4) & 1) << 2)) * 16), svo = (lane >> 2) * 9216 + (lane & 3) * 16;
    auto dmaD = [&](int u, int i) AI {
        const unsigned dst = lb + (u % NSL) * SLOT + wm * 4608 + i * 1024;
        const char* src = u < 16 ? wp + (u >> 2) * 128 + (size_t)((u & 3) * 144 + i * 8) * 9216 : wsg + (size_t)((u & 1) * 288 + i * 16) * 9216;
        if (i < 4) dma16(dst, src, u < 16 ? pvo : svo); else dma16h(dst, src, u < 16 ? pvo : svo);
    };

    {
    constexpr unsigned OOB = 0x80000000u;
    u32x4 Y[5], X[5], W1[21], W2[24];
    int nu[5];
#pragma unroll
    for (int m = 0; m < 4; ++m) nu[m] = ((nt + m) & 3) * 8 + w;
    nu[4] = 32 + (w >> 1);
    const unsigned h16 = l16 + (w & 1) * 512, h16v = lane < 32 ? h16 : OOB, e16 = w < 2 ? l16 : OOB;
    BT(1);
    const u32x2 xs0 = bl8<0>(wr, lj * 8, O_XS + rb * 256), xs1 = bl8<0>(wr, lj * 8, O_XS + rb * 256 + 128);
#pragma unroll
    for (int m = 0; m < 5; ++m) { Y[m] = bl16<0>(wr, m < 4 ? l16 : h16v, O_YB + (rb * 36 + nu[m]) * 1024); X[m] = bl16<0>(wr, m < 4 ? l16 : h16v, O_XT + (rb * 36 + nu[m]) * 1024); SB(); }
    BT(2);
    if (P_B0) asm volatile("s_barrier" ::: "memory");
    BT(3);
    auto ldw = [&](u32x4* W, unsigned base, int k) AI {
        if (k < 0) W[k] = bl16<0>(wr, (lj < 2 && (k < -1 || w < 2)) ? lq * 32 + lj * 16 : OOB, O_W2M + (w + 8 * (k + 3)) * 128);
        else if (k < 3) W[k] = bl16<0>(wr, k < 2 ? l16 : e16, base + ((32 + nt) * 18 + w + 8 * k) * 1024);
        else W[k] = bl16<0>(wr, l16, base + ((8 * nt + w) * 18 + k - 3) * 1024);
        SB();
    };
    auto ldw2 = [&](int k) AI { ldw(W2 + 3, O_W2T, k - 3); };
    auto prime = [&](int i) AI { dmaD(2 * (i / 5) + (w >> 2), i % 5); SB(); };
    f32x2 s[2] = {{0, 0}, {0, 0}}, sq[2] = {{0, 0}, {0, 0}};
    const float rx[2] = {__uint_as_float(xs0[0]), __uint_as_float(xs1[0])}, mx[2] = {__uint_as_float(xs0[1]), __uint_as_float(xs1[1])};
    u32x4 hown, ca, cb, cea, ceb;
    constexpr int WBS[6] = P_WBS;
#pragma unroll
    for (int m = 0; m < 5; ++m) {
#pragma unroll
        for (int k = m ? WBS[m - 1] : 0; k < WBS[m]; ++k) ldw(W1, O_W1T, k);
        BT(4 + 2 * m);
        u32x4 hp;
#pragma unroll
        for (int i = 0; i < 4; ++i) {
            const int mt = i >> 1;
            const f32x2 x = {bflo(X[m][i]), bfhi(X[m][i])}, y = {bflo(Y[m][i]), bfhi(Y[m][i])};
            const f32x2 h = __builtin_elementwise_fma(__builtin_elementwise_fma(x, f32x2{rx[mt], rx[mt]}, f32x2{mx[mt], mx[mt]}), y, x);
            s[mt] += h; sq[mt] = __builtin_elementwise_fma(h, h, sq[mt]);
            hp[i] = pk2(h[0], h[1]);
        }
        OPAQ(s[0]); OPAQ(s[1]); OPAQ(sq[0]); OPAQ(sq[1]);
        const unsigned a = lb + L_F + (nu[m] >> 1) * 2048 + (nu[m] & 1) * 8 + (m < 4 ? l16 : h16);
        if (m < 4 || lane < 32) { ldsw<u32x2>(a, u32x2{hp[0], hp[1]}); ldsw<u32x2>(a + 1024, u32x2{hp[2], hp[3]}); }
        if (m == 0) hown = hp;
        SB();
        BT(5 + 2 * m);
    }
    {
        float s0 = s[0][0] + s[0][1], s1 = s[1][0] + s[1][1], q0 = sq[0][0] + sq[0][1], q1 = sq[1][0] + sq[1][1];
        swap16f(s0, s1); swap16f(q0, q1);
        float t = s0 + s1, u = q0 + q1;
        swap32f(t, u);
        ldsw<float>(lb + L_ST + w * 256 + lane * 4, t + u);
    }
    SB();
    {
        const unsigned co = lj * 64 + lq * 16, co2 = lj < 2 ? co + 1024 : OOB;
        ca = bl16<0>(wr, co, O_CSP + (8 * nt + w) * 1152); cb = bl16<0>(wr, co2, O_CSP + (8 * nt + w) * 1152);
        cea = bl16<0>(wr, w < 2 ? co : OOB, O_CSP + (32 + nt) * 1152); ceb = bl16<0>(wr, w < 2 ? co2 : OOB, O_CSP + (32 + nt) * 1152);
        SB();
    }
#pragma unroll
    for (int k = WBS[4]; k < WBS[5]; ++k) ldw(W1, O_W1T, k);
    bar();
    BT(14);
    f32x4 acc[2];
    unsigned fa = lb + L_F + l16; OPAQ(fa);
    const unsigned fx = fa + w * 2048, fx2 = fa + (16 + (w & 1)) * 2048, pw = lb + L_PE + w * 2048 + l16, pr = lb + L_PE + (w & 1) * 1024 + l16;
    u32x4 xb[3][2];
    auto slices = [&](const u32x4* We) AI {
#pragma unroll
        for (int i = 0; i < 3; ++i)
#pragma unroll
            for (int mt = 0; mt < 2; ++mt) acc[mt] = mfma16(asb(We[i]), asb(xb[i][mt]), i ? acc[mt] : f32x4{0, 0, 0, 0});
    };
    auto xread = [&]() AI {
#pragma unroll
        for (int mt = 0; mt < 2; ++mt) { xb[0][mt] = ldsr<u32x4>(fx + mt * 1024); xb[1][mt] = ldsr<u32x4>(fx + 16384 + mt * 1024); xb[2][mt] = ldsr<u32x4>(fx2 + mt * 1024); }
    };
    auto psum = [&]() AI { f32x4 t[8];
#pragma unroll
        for (int i = 0; i < 8; ++i) t[i] = ldsr<f32x4>(pr + i * 2048);
        return ((t[0] + t[1]) + (t[2] + t[3])) + ((t[4] + t[5]) + (t[6] + t[7])); };
    auto gelu4 = [&](f32x4 z) AI {
        const f32x4 u = z * __builtin_elementwise_fma(z * z, f32x4{GC1, GC1, GC1, GC1}, f32x4{GC0, GC0, GC0, GC0});
        f32x4 e;
#pragma unroll
        for (int r = 0; r < 4; ++r) e[r] = __builtin_amdgcn_exp2f(u[r]);
        e += 1.0f;
#pragma unroll
        for (int r = 0; r < 4; ++r) e[r] = __builtin_amdgcn_rcpf(e[r]);
        return z * e; };
    xread();
    float tv[8];
#pragma unroll
    for (int i = 0; i < 8; ++i) tv[i] = ldsr<float>(lb + L_ST + i * 256 + lane * 4);
    const u32x2 hext = ldsr<u32x2>(fa + (w & 1) * 1024 + ((32 + nt) >> 1) * 2048 + (nt & 1) * 8);
    SB();
    BT(15);
    slices(W1);
    ldsw<f32x4>(pw, acc[0]); ldsw<f32x4>(pw + 1024, acc[1]);
    SB();
    BT(16);
    float mm[2], rr[2], r1[2];
    f32x4 cz[2];
    gemm_small(W1 + 3, fa, acc, [&](auto K) AI {
        constexpr int k = K;
        if constexpr (k + WBS[5] < 21) ldw(W1, O_W1T, k + WBS[5]);
        if constexpr (k == 0) {
            float su = ((tv[0] + tv[1]) + (tv[2] + tv[3])) + ((tv[4] + tv[5]) + (tv[6] + tv[7])), qu = su; swap32f(su, qu);
            const float mean = su * RD576, var = fmaxf(qu * RD576 - mean * mean, 0.f);
            const float r1s = __builtin_amdgcn_rsqf(var + EPS), r12 = r1s * __builtin_amdgcn_rsqf(var * r1s * r1s + EPS);
            mm[0] = mm[1] = mean; rr[0] = rr[1] = r12; r1[0] = r1[1] = r1s;
            swap16f(mm[0], mm[1]); swap16f(rr[0], rr[1]); swap16f(r1[0], r1[1]);
            OPAQ(mm[0]); OPAQ(rr[0]); OPAQ(r1[0]);
        }
        if constexpr (k == P_KB) asm volatile("s_barrier" ::: "memory");
        if constexpr (k >= 4 && k < 8) {
            constexpr int r = k - 4;
            const float c = rowsum16(__uint_as_float(ca[r]) + __uint_as_float(cb[r]));
            cz[0][r] = -mm[0] * rr[0] * c; cz[1][r] = -mm[1] * rr[1] * c; OPAQ(cz[0][r]); OPAQ(cz[1][r]);
        }
        if constexpr (k == P_KB + 1) {
            if (w < 2) {
                const f32x4 ae = psum();
                const float me = w ? mm[1] : mm[0], re = w ? rr[1] : rr[0];
                f32x4 z;
#pragma unroll
                for (int r = 0; r < 4; ++r) z[r] = (ae[r] - me * rowsum16(__uint_as_float(cea[r]) + __uint_as_float(ceb[r]))) * re;
                const f32x4 g = gelu4(z);
                unsigned a0 = pk2(g[0], g[1]), a1 = pk2(g[2], g[3]), b0 = a0, b1 = a1;
                swap16(a0, b0); swap16(a1, b1);
                if (!(lq & 1)) bs16<XA>(u32x4{a0, a1, b0, b1}, wr, lj * 32 + hh * 16, O_GB + (32 + nt) * GTS + (4 + 32 * rb + 16 * w) * 32);
            }
        }
        if constexpr (k % 3 == 0) BT(17 + k / 3);
        if constexpr (k >= 18 - P_W2A) ldw2(k - (18 - P_W2A));
    });
    BT(23);
    {
        unsigned o[2][2];
#pragma unroll
        for (int mt = 0; mt < 2; ++mt) {
            const f32x4 g = gelu4(__builtin_elementwise_fma(acc[mt], f32x4{rr[mt], rr[mt], rr[mt], rr[mt]}, cz[mt]));
            o[mt][0] = pk2(g[0], g[1]); o[mt][1] = pk2(g[2], g[3]);
        }
        swap16(o[0][0], o[1][0]); swap16(o[0][1], o[1][1]);
        bs16<XA>(u32x4{o[0][0], o[0][1], o[1][0], o[1][1]}, wr, (lane & 31) * 32 + hh * 16, O_GB + (8 * nt + w) * GTS + (4 + 32 * rb) * 32);
    }
    SB();
    BT(24);
#pragma unroll
    for (int k = P_W2A; k < P_W2A + P_W2B; ++k) ldw2(k);
    BT(25);
    waitvm<P_W2B>();
    BT(26);
    bar();
    BT(27);
    if (w == 0 && lane == 0) bs4<16>(1u, wr, 0, F_B + (rb + 1) * 128 + nt * 4);
    STAMP(1, 3);
#pragma unroll
    for (int k = P_W2A + P_W2B; k < P_W2A + P_W2B + P_W2C; ++k) ldw2(k);
    BT(28);
    {
        const int pr = rb + (lane >> 2);
        poll(wr, F_B + (lane < 12 ? pr * 128 + (lane & 3) * 4 : 0), (lane < 12 && pr >= 1 && pr <= 64) ? 1u : 0u);
    }
    BT(29);
    u32x4 G[5];
    auto gaddr = [&](int n) AI { const int gr = n / 9, sh = gr == 0 ? 1 : gr == 1 ? -1 : gr == 2 ? 2 : -2; return O_GB + n * GTS + (4 + 32 * rb - sh) * 32; };
#pragma unroll
    for (int m = 0; m < 5; ++m) { G[m] = bl16<XA>(wr, m < 4 ? l16 : h16v, gaddr(m < 4 ? 8 * m + w : 32 + (w >> 1))); SB(); }
    const unsigned gw = lb + L_F + (((lane >> 1) & 15) + 16 * (lane & 1)) * 16;
#pragma unroll
    for (int m = 0; m < 4; ++m) ldsw<u32x4>(gw + (w >> 1) * 2048 + hh * 1024 + (w & 1) * 512 + m * 8192, G[m]);
    if (lane < 32) ldsw<u32x4>(gw + (16 + (w >> 2)) * 2048 + (w & 1) * 1024 + ((w >> 1) & 1) * 512, G[4]);
    SB();
#pragma unroll
    for (int k = P_W2A + P_W2B + P_W2C; k < 24; ++k) ldw2(k);
    BT(30);
    bar();
    BT(31);
    xread();
    SB();
    slices(W2 + 3);
    ldsw<f32x4>(pw, acc[0]); ldsw<f32x4>(pw + 1024, acc[1]);
    slices(W2);
    {
        float a = acc[0][0] + acc[0][1], b = acc[1][0] + acc[1][1];
        swap16f(a, b);
        if (lane < 32) ldsw<float>(lb + L_MN + w * 128 + lane * 4, a);
    }
    SB();
    BT(32);
    float mn[2];
    f32x4 hr[2], rese;
    auto quad = [&](float t) AI { return u32x4{__float_as_uint(t), __float_as_uint(dppf<0x55>(t)), __float_as_uint(dppf<0xAA>(t)), __float_as_uint(dppf<0xFF>(t))}; };
    gemm_small(W2 + 6, fa, acc, [&](auto K) AI {
        constexpr int k = K;
        if constexpr (k == P_KC) {
            asm volatile("s_barrier" ::: "memory");
#pragma unroll
            for (int i = 0; i < 4; ++i) tv[i] = ldsr<float>(lb + L_MN + i * 256 + lane * 4);
        }
        if constexpr (k == P_KC + 2) {
            float t = (tv[0] + tv[1]) + (tv[2] + tv[3]), u = t; swap32f(t, u);
            mn[0] = mn[1] = t + u; swap16f(mn[0], mn[1]); OPAQ(mn[0]);
        }
        if constexpr (k == P_KC + 3) {
            if (w < 2) {
                const f32x4 ae = psum();
                const float h[4] = {bflo(hext[0]), bfhi(hext[0]), bflo(hext[1]), bfhi(hext[1])};
                const float re = w ? r1[1] : r1[0], me = w ? mm[1] : mm[0], ne = w ? mn[1] : mn[0];
                float xc[4];
#pragma unroll
                for (int r = 0; r < 4; ++r) { const float x1 = __builtin_fmaf(h[r] - me, re, ae[r]); xc[r] = x1 - ne; rese[r] = h[r] + x1; }
                unsigned a0 = pk2(xc[0], xc[1]), a1 = pk2(xc[2], xc[3]), b0 = a0, b1 = a1;
                swap16(a0, b0); swap16(a1, b1);
                if (!(lq & 1)) bs16<XA>(u32x4{a0, a1, b0, b1}, wr, hh * 512 + lj * 16, O_XB + (rb * 36 + 32 + nt) * 1024 + w * 256);
                float t = (xc[0] * xc[0] + xc[1] * xc[1]) + (xc[2] * xc[2] + xc[3] * xc[3]), u = t;
                swap16f(t, u); t += u; u = t; swap32f(t, u);
                const u32x4 qv = quad(t + u);
                if (lane < 16 && !(lane & 3)) bs16<XA>(qv, wr, lane * 4, O_SQ + (rb * 36 + 32 + nt) * 128 + 64 * w);
                bs16<0>(__builtin_bit_cast(u32x4, rese), wr, l16, O_R + ((rb * 36 + 32 + nt) * 2 + w) * 1024);
            }
        }
        if constexpr (k >= P_KC + 4 && k < P_KC + 8) {
            constexpr int i = k - (P_KC + 4), mt = i >> 1, r = 2 * (i & 1);
            const float h0 = bflo(hown[i]), h1 = bfhi(hown[i]);
            cz[mt][r] = __builtin_fmaf(h0 - mm[mt], r1[mt], -mn[mt]); cz[mt][r + 1] = __builtin_fmaf(h1 - mm[mt], r1[mt], -mn[mt]);
            hr[mt][r] = h0 + mn[mt]; hr[mt][r + 1] = h1 + mn[mt];
            OPAQ(cz[mt][r]); OPAQ(cz[mt][r + 1]); OPAQ(hr[mt][r]); OPAQ(hr[mt][r + 1]);
        }
        if constexpr (k >= 18 - P_PRA) prime(k - (18 - P_PRA));
    });
    BT(33);
    {
        unsigned o[2][2]; float q2[2]; f32x4 xc[2];
#pragma unroll
        for (int mt = 0; mt < 2; ++mt) { xc[mt] = acc[mt] + cz[mt]; o[mt][0] = pk2(xc[mt][0], xc[mt][1]); o[mt][1] = pk2(xc[mt][2], xc[mt][3]); }
        swap16(o[0][0], o[1][0]); swap16(o[0][1], o[1][1]);
        bs16<XA>(u32x4{o[0][0], o[0][1], o[1][0], o[1][1]}, wr, l16, O_XB + (rb * 36 + 8 * nt + w) * 1024);
        SB();
#pragma unroll
        for (int mt = 0; mt < 2; ++mt) { const f32x4 t = xc[mt] * xc[mt]; q2[mt] = (t[0] + t[1]) + (t[2] + t[3]); }
        {
            swap16f(q2[0], q2[1]);
            float t = q2[0] + q2[1], u = t; swap32f(t, u);
            const u32x4 qv = quad(t + u);
            if (lane < 32 && !(lane & 3)) bs16<XA>(qv, wr, lane * 4, O_SQ + (rb * 36 + 8 * nt + w) * 128);
        }
        SB();
        BT(34);
#pragma unroll
        for (int i = P_PRA; i < P_PRA + P_PRB; ++i) prime(i);
        BT(35);
        waitvm<P_PRB>();
        BT(36);
        bar();
        BT(37);
        if (w == 0 && lane == 0) bs4<16>(1u, wr, 0, F_C + rt * 128 + (kap * 4 + nt) * 4);
#pragma unroll
        for (int i = P_PRA + P_PRB; i < 10; ++i) prime(i);
#pragma unroll
        for (int mt = 0; mt < 2; ++mt) bs16<0>(__builtin_bit_cast(u32x4, xc[mt] + hr[mt]), wr, l16, O_R + ((rb * 36 + 8 * nt + w) * 2 + mt) * 1024);
    }
    }
    BT(38);
    BDUMP(1);
    STAMP(1, 7);

    unsigned kb[4], kq[4];
#pragma unroll
    for (int i = 0; i < 4; ++i) { const int m = (nt + i) & 3; kb[i] = 36 * kap + 18 * (m >> 1) + 10 * (m & 1); kq[i] = 36 * kap + 18 * (m >> 1) + 8 + (m & 1); }
    const unsigned kpa = lb + L_ST + wm * 512 + (lane & 31) * 16;
    if (w < 4) {
        poll(wr, F_C + rt * 128 + (wm * 4 + (lane & 3)) * 4, 1u);
        STAMP(2, 0);
        u32x4 X[36];
#pragma unroll
        for (int i = 0; i < P_X0; ++i) X[i] = bl16<XA>(wr, l16, O_XB + ((4 * rt + wm) * 36 + i) * 1024);
        asm volatile("s_barrier" ::: "memory");
        const int r = 8 * hh + (lj >> 2);
        unsigned ta[2][2], ts;
#pragma unroll
        for (int s = 0; s < 2; ++s)
#pragma unroll
            for (int ab = 0; ab < 2; ++ab) { ta[s][ab] = lb + s * 3 * SLOT + r * 128 + ((ab ^ ((r >> 1) & 1)) * 64) + (lq & 1) * 32 + (lane & 3) * 8; OPAQ(ta[s][ab]); }
        ts = lb + 3 * SLOT + r * 64 + (lq & 1) * 32 + (lane & 3) * 8; OPAQ(ts);
        const int re = 16 * (lq >> 1) + 4 * (lq & 1) + (lj >> 2);
        unsigned am[2][8], ax[4];
        const unsigned amb = lb + re * 256 + ((re & 7) << 5) + (lane & 3) * 8, axb = lb + L_EX + re * 128 + (((re >> 1) & 3) << 5) + (lane & 3) * 8;
        u32x4 H[HD][2];
        const unsigned hvo = l16 + (8 * rt + 2 * wm) * 144 * 1024;
        unsigned hso = (8 * rt + 2 * wm + (lq & 1)) * 144 * 1024 + (lj + 16 * hh) * 16 + O_HB + kb[0] * 1024;
        OPAQ(hso);
        auto ldH = [&](int j, int mt) AI {
            const int t = e_til(j);
            H[j % HD][mt] = bl16<XA>(wr, hvo + mt * 144 * 1024 + (t & 3) * 1024, O_HB + (t < 8 ? kb[e_mem(j)] + (t & 4) : kq[e_mem(j)]) * 1024);
        };
        __builtin_amdgcn_s_setprio(P_PRIO);
        f32x16 acc[4]; f32x4 ec[12][2]; bf16x8 A[RD + 1];
        f32x4 kp; float t[8]; u32x4 hp;
        sfor<0, 19>([&](auto C) AI { sfor<0, 36>([&](auto I) AI {
            constexpr int fr = 36 * C + I, fm = fr - RD;
            if constexpr (fr < 648 + RD) {
            constexpr int em = fm - 324, jm = em >= 0 ? HT.st[em] : 0, cm = em >= 0 ? em - HT.fi[jm] : 0;
            constexpr bool z0 = cm < 8 ? jm == 0 : jm == 10;
            constexpr int gm = fm < 73 ? 0 : fm < 325 ? 1 : fm < 325 + 4 * NE ? 2 : 0;
            constexpr int gh = gm == 1 ? (fm - 73) / 18 : gm == 2 ? 14 + (fm - 325) / NE : 0, go = gm == 1 ? (fm - 73) % 18 : gm == 2 ? (fm - 325) % NE : 0, gt = gh >> 1, gi = gt < 8 ? (gt & 3) : 0;
            constexpr bool formA = gm == 2 && ((ACT_E[go][0] >> 3) == 3 || (ACT_E[go][0] >> 3) == 5);
            auto act = [&](auto K) AI {
                constexpr int op = gm == 1 ? ACT_D[go][K] : (gm == 2 && K < 2) ? ACT_E[go][K & 1] : -1;
                if constexpr (op >= 0) {
                    constexpr int kd = op >> 3, e = op & 7;
                    const float x = acc[gi][8 * (gh & 1) + e];
                    if constexpr (kd == 0) t[e] = x * x;
                    if constexpr (kd == 1) t[e] = __builtin_fmaf(t[e], kp[1], kp[0]);
                    if constexpr (kd == 2 || kd == 6) t[e] = t[e] * x;
                    if constexpr (kd == 3) t[e] = __builtin_amdgcn_exp2f(t[e]);
                    if constexpr (kd == 4) t[e] = __builtin_fmaf(t[e], kp[2], kp[2]);
                    if constexpr (kd == 5) t[e] = __builtin_amdgcn_rcpf(t[e]);
                    if constexpr (kd < 7) OPAQ(t[e]);
                    if constexpr (kd == 7) { hp[e] = pk2(t[2 * e], t[2 * e + 1]); OPAQ(hp[e]); }
                    if constexpr (kd == 8) { static_assert(s_pos(gh) == fm, "store slot"); bs16<XA>(hp, wr, hso, (gt < 8 ? gt * 1024 : (kq[0] - kb[0]) * 1024) + (gh & 1) * 512); }
                    SB();
                }
            };
            using I0 = std::integral_constant<int, 0>; using I1 = std::integral_constant<int, 1>; using I2 = std::integral_constant<int, 2>; using I3 = std::integral_constant<int, 3>;
            auto mf1 = [&]() AI { ec[cm][1] = mfma16(A[(fm + RD + 1) % (RD + 1)], asb(H[jm % HD][1]), z0 ? f32x4{0, 0, 0, 0} : ec[cm][1]); SB(); };
            if constexpr (em >= 0) ec[cm][0] = mfma16(A[fm % (RD + 1)], asb(H[jm % HD][0]), z0 ? f32x4{0, 0, 0, 0} : ec[cm][0]);
            if constexpr (fm >= 0 && fm < 324) {
                constexpr int u = fm / 18, o = fm % 18, ks = u < 16 ? 9 * (u & 3) + (o >> 1) : 18 * (u & 1) + o, ai = u < 16 ? ((u >> 2) & 1) * 2 + (o & 1) : 0;
                f32x16 z = {};
                acc[ai] = __builtin_amdgcn_mfma_f32_32x32x16_bf16(A[fm % (RD + 1)], asb(X[ks]), ks == 0 ? z : acc[ai], 0, 0, 0);
            }
            SB();
            if constexpr (formA) { act(I0{}); act(I1{}); mf1(); }
            s16x4 t0, t1;
            unsigned a = 0, a2 = 0;
            constexpr int er = fr - 324, jr = er >= 0 ? HT.st[er] : 0, cr = er >= 0 ? er - HT.fi[jr] : 0;
            constexpr int bu = fr < 324 ? (fr % 18 == BP ? fr / 18 : -1) : (fr < 648 && !(jr & 1) && cr == BP) ? 18 + jr / 2 : -1;
            constexpr int nl = fr <= 648 ? 2 * (RD - 2) : 2 * (646 - fm) > 0 ? 2 * (646 - fm) : 0;
            if constexpr (bu >= 0) {
                static_assert(b_slot(bu) == fm, "barrier slot");
                constexpr int vw = f_lvl(bu) ? m_wait(LH[(f_lvl(bu) + 3) & 3], bu) : 63;
                if constexpr (vw < 63) waitvm<vw>();
                asm volatile("s_barrier" ::: "memory");
                SB();
            }
            if constexpr (fm >= 0 && fm < 647) { __builtin_amdgcn_s_waitcnt(wcnt(HT.vm[fm], nl)); SB(); }
            if constexpr (fr < 324) {
                constexpr int u = fr / 18, o = fr % 18, sl = u % NSL;
                if constexpr (u < 16) { a = ta[sl / 3][o & 1] + (sl % 3) * SLOT + (o >> 1) * 2048; a2 = a + 512; }
                else { a = ts + (sl - 3) * SLOT + o * 1024; a2 = a + 256; }
            } else if constexpr (fr < 648) {
                constexpr int sl = (18 + (jr >> 1)) % NSL;
                if constexpr (cr < 8) { a = am[sl / 3][cr & 7] + (sl % 3) * SLOT + (jr & 1) * 8192; a2 = a + 2048; }
                else { a = ax[cr & 3] + e_til(jr) * 4096; a2 = a + 1024; }
            }
            if constexpr (fr < 648) { t0 = __builtin_amdgcn_ds_read_tr16_b64_v4i16((LAS s16x4*)(uintptr_t)a); SB(); }
            if constexpr (em >= 0 && !formA) { act(I0{}); if constexpr (gm == 1) act(I1{}); mf1(); }
            if constexpr (fr < 648) {
                t1 = __builtin_amdgcn_ds_read_tr16_b64_v4i16((LAS s16x4*)(uintptr_t)a2);
                A[fr % (RD + 1)] = __builtin_bit_cast(bf16x8, __builtin_shufflevector(t0, t1, 0, 1, 2, 3, 4, 5, 6, 7));
                SB();
            }
            if constexpr (bu == 3) { kp = ldsr<f32x4>(kpa); SB(); }
            if constexpr (em < 0) { act(I0{}); act(I1{}); }
            if constexpr (em >= 0 && gm == 2 && !formA) act(I1{});
            act(I2{}); act(I3{});
            if constexpr (fm >= 299 && fm < 315 && (fm & 1)) { constexpr int c = (fm - 299) >> 1; am[0][c] = amb ^ (c << 5); OPAQ(am[0][c]); }
            if constexpr (fm >= 330 && fm < 346 && !(fm & 1)) { constexpr int c = (fm - 330) >> 1; am[1][c] = am[0][c] + 3 * SLOT; OPAQ(am[1][c]); }
            if constexpr (fm >= 390 && fm < 398 && !(fm & 1)) { constexpr int c = (fm - 390) >> 1; ax[c] = axb ^ (c << 5); OPAQ(ax[c]); }
            if constexpr (fm >= 0 && x_at(fm) >= 0) X[x_at(fm)] = bl16<XA>(wr, l16, O_XB + ((4 * rt + wm) * 36 + x_at(fm)) * 1024);
            constexpr int l0 = fm >= 0 ? HT.at[0][fm] : -1, l1 = fm >= 0 ? HT.at[1][fm] : -1;
            if constexpr (l0 >= 0) ldH(l0 >> 1, l0 & 1);
            if constexpr (l1 >= 0) ldH(l1 >> 1, l1 & 1);
            SB();
            }
        }); });
        __builtin_amdgcn_s_setprio(0);
        STAMP(2, 2);
#pragma unroll
        for (int c = 0; c < 12; ++c) {
            const u32x4 o = {pk2(ec[c][0][0], ec[c][0][1]), pk2(ec[c][0][2], ec[c][0][3]), pk2(ec[c][1][0], ec[c][1][1]), pk2(ec[c][1][2], ec[c][1][3])};
            if (c < 8) bs16<XA>(o, wr, l16, O_PB + (((4 * rt + wm) * 32 + 8 * nt + c) * 4 + kap) * 1024);
            else bs16<XA>(o, wr, l16, O_PX + (((4 * rt + wm) * 4 + c - 8) * 16 + fc) * 1024);
        }
        waitvm<0>();
    } else {
#pragma unroll
        for (int i = 0; i < 5; ++i) dmaD(4, i);
        poll(wr, F_C + rt * 128 + (wm * 4 + (lane & 3)) * 4, 1u);
        STAMP(2, 0);
        asm volatile("s_barrier" ::: "memory");
        f32x4 sqv[5];
        {
            const char* sb = ws + O_SQ + (4 * rt + wm) * 4608;
#pragma unroll
            for (int i = 0; i < 5; ++i) asm volatile("global_load_dwordx4 %0, %1, %2 offset:%3 sc1" : "=v"(sqv[i]) : "v"(i < 4 ? l16 : (l16 & 511) + 1024), "s"(sb), "i"(i < 4 ? i * 1024 : 3072) : "memory");
        }
        const int drow = 4 * wm + lq;
        const unsigned dvo = drow * 1152 + ((lj ^ (2 * (drow & 7))) << 4);
        const char* w4m = w4 + nt * 256;
        auto ksE = [&](int j) AI { return e_til(j) < 8 ? kb[e_mem(j)] + e_til(j) : kq[e_mem(j)]; };
        auto dmaE = [&](int u, int i) AI { const int j = 2 * (u - 18) + (i >> 1);
            dma16(lb + (u % NSL) * SLOT + (j & 1) * 8192 + (wm + 4 * (i & 1)) * 1024, w4m + (size_t)(ksE(j) * 32 + 16 * (i & 1)) * 1152, dvo); };
        const int xrow = lane >> 3;
        const unsigned xvo = xrow * 1152 + (((lane & 7) ^ (2 * ((xrow >> 1) & 3))) << 4);
        auto dmaX = [&](int t) AI { dma16(lb + L_EX + t * 4096 + wm * 1024, w4 + 1024 + (size_t)((t < 8 ? kb[3] + t : kq[3]) * 32 + wm * 8) * 1152, xvo); };
        const unsigned fdo = F_D + rt * 128 + kap * 16;
        unsigned pv = 0, pfd = 0;
        const unsigned pfo = (8 * rt + 2 * wm + ((lane >> 3) & 1)) * 144 * 1024 + (lane & 7) * 128;
        TDECL; TS(0);
        sfor<0, 36>([&](auto U) AI {
            constexpr int u = U;
            if constexpr (u >= 3) waitvm<g_wait(u)>();
            if constexpr (u == 3) {
#pragma unroll
                for (int i = 0; i < 5; ++i) OPAQ(sqv[i]);
                f32x4 sq = (sqv[0] + sqv[1]) + (sqv[2] + sqv[3]) + 0.5f * sqv[4];
                float t = 0;
#pragma unroll
                for (int r = 0; r < 4; ++r) {
                    float a = sq[r] + dppf<0x128>(sq[r]), b = a;
                    swap16f(a, b); a += b; b = a; swap32f(a, b); a += b;
                    if (((lane >> 3) & 3) == r) t = a;
                }
                const float sg = sqrtf(t * RD576 + EPS), rstd = 1.0f / sg;
                if (lane < 32) ldsw<f32x4>(lb + L_ST + wm * 512 + ((lane & 7) * 4 + (lane >> 3)) * 16, f32x4{GC0 * rstd, GC1 * rstd * rstd * rstd, sg, 0.f});
            }
            if constexpr (q_lvl(u) != 0) {
                OPAQ(pv);
                TS(72 + q_lvl(u));
                if (__builtin_amdgcn_ballot_w64(pv < (unsigned)q_lvl(u)) != 0) poll(wr, fdo + (lane & 3) * 4, q_lvl(u));
            }
            TS(2 * u + 1);
            bar();
            TS(2 * u + 2);
            if constexpr (f_lvl(u) != 0) { if (w == 4 && lane == 0) bs4<16>(f_lvl(u), wr, 0, fdo + nt * 4); }
            if constexpr (g_pre(u)) asm volatile("global_load_dword %0, %1, %2 sc1" : "=v"(pv) : "v"((lane & 3) * 4), "s"(ws + fdo) : "memory");
#pragma unroll
            for (int i = 0; i < g_nd(u); ++i) { if (u + 5 < 18) dmaD(u + 5, i); else dmaE(u + 5, i); SB(); }
            if constexpr (g_nx(u)) dmaX(u - 4);
            if constexpr (g_pf(u) >= 0) asm volatile("global_load_dword %0, %1, %2 sc1" : "+v"(pfd) : "v"(pfo), "s"(ws + O_HB + (size_t)(g_pf(u) < 8 ? kb[0] + g_pf(u) : kq[0]) * 1024) : "memory");
            SB();
        });
        waitvm<0>();
        OPAQ(pfd);
        TDUMP(2, 32);
    }
    bar();
    if (w == 0 && lane == 0) bs4<16>(1u, wr, 0, F_E + rt * 128 + fc * 4);
    STAMP(2, 3);
    {
        const rsrc_t orr = mkrs(out, 2048 * 1152);
        u32x4 P[4], R[2], Q[16], Re[2];
#pragma unroll
        for (int i = 0; i < 2; ++i) R[i] = bl16<16>(wr, l16, O_R + ((rb * 36 + 8 * nt + w) * 2 + i) * 1024);
        if (w == 7) {
#pragma unroll
            for (int i = 0; i < 2; ++i) Re[i] = bl16<16>(wr, l16, O_R + ((rb * 36 + 32 + nt) * 2 + i) * 1024);
        }
        poll(wr, F_E + rt * 128 + lj * 4, (w == 7 || (lj & 3) == nt) ? 1u : 0u);
        STAMP(2, 4);
#pragma unroll
        for (int i = 0; i < 4; ++i) P[i] = bl16<XA>(wr, l16, O_PB + ((rb * 32 + 8 * nt + w) * 4 + i) * 1024);
        if (w == 7) {
#pragma unroll
            for (int i = 0; i < 16; ++i) Q[i] = bl16<XA>(wr, l16, O_PX + ((rb * 4 + nt) * 16 + i) * 1024);
        }
        auto fin = [&](u32x4* p, int n, u32x4* r, int tile) AI {
#pragma unroll
            for (int mt = 0; mt < 2; ++mt) {
                float a[4] = {0, 0, 0, 0};
                for (int i = 0; i < n; ++i) { a[0] += bflo(p[i][2 * mt]); a[1] += bfhi(p[i][2 * mt]); a[2] += bflo(p[i][2 * mt + 1]); a[3] += bfhi(p[i][2 * mt + 1]); }
                bs8<0>(u32x2{pk2(a[0] + __uint_as_float(r[mt][0]), a[1] + __uint_as_float(r[mt][1])), pk2(a[2] + __uint_as_float(r[mt][2]), a[3] + __uint_as_float(r[mt][3]))},
                       orr, lj * 1152 + lq * 8, (32 * rb + 16 * mt) * 1152 + tile * 32);
            }
        };
        fin(P, 4, R, 8 * nt + w);
        if (w == 7) fin(Q, 16, Re, 32 + nt);
    }
    STAMP(2, 6);
    STAMP(2, 7);
    asm volatile(".Lcode_hi:");
}

extern "C" void kernel_launch(void* const* d_in, const int* in_sizes, int n_in, void* d_out, int out_size, void* d_ws, size_t ws_size, hipStream_t stream) {
    char* ws = (char*)d_ws;
    (void)hipFuncSetAttribute((const void*)k1, hipFuncAttributeMaxDynamicSharedMemorySize, K1_LDS);
    (void)hipFuncSetAttribute((const void*)k2, hipFuncAttributeMaxDynamicSharedMemorySize, K2_LDS);
    k1<<<256, 512, K1_LDS, stream>>>((const char*)d_in[0], (const char*)d_in[1], (const char*)d_in[2], (const char*)d_in[3], (const char*)d_in[4], (const u16*)d_in[5],
                                     (const char*)d_in[7], (const char*)d_in[8], (const char*)d_in[9], (const char*)d_in[10], ws);
    k2<<<256, 512, K2_LDS, stream>>>((const char*)d_in[9], (const char*)d_in[10], ws, (char*)d_out);
}
```

```cpp
#define SIDEQ ((r[1]==12345))
#include <hip/hip_runtime.h>
#include <stdint.h>
#include <utility>
#define LAS __attribute__((address_space(3)))
typedef short s16x4 __attribute__((ext_vector_type(4)));
typedef __bf16 bf16x8 __attribute__((ext_vector_type(8)));
typedef __bf16 bf16x2 __attribute__((ext_vector_type(2)));
typedef float f32x16 __attribute__((ext_vector_type(16)));
typedef float f32x4 __attribute__((ext_vector_type(4)));
typedef float f32x2 __attribute__((ext_vector_type(2)));
typedef unsigned u32x4 __attribute__((ext_vector_type(4)));
typedef unsigned u32x2 __attribute__((ext_vector_type(2)));
typedef unsigned long long u64;
typedef unsigned short u16;
typedef __amdgpu_buffer_rsrc_t rsrc_t;
#define DEVI __device__ __forceinline__
#define AI __attribute__((always_inline))
#define SB() __builtin_amdgcn_sched_barrier(0)
#define OPAQ(x) asm volatile("" : "+v"(x))

DEVI unsigned pk2(float a, float b) { f32x2 p = {a, b}; return __builtin_bit_cast(unsigned, __builtin_convertvector(p, bf16x2)); }
DEVI float bflo(unsigned x) { return __uint_as_float(x << 16); }
DEVI float bfhi(unsigned x) { return __uint_as_float(x & 0xffff0000u); }
DEVI bf16x8 asb(u32x4 x) { return __builtin_bit_cast(bf16x8, x); }
constexpr float GC0 = -2.0f * 0.7978845608028654f * 1.4426950408889634f, GC1 = GC0 * 0.044715f;
DEVI float gelu(float x) {
    float e = __builtin_amdgcn_exp2f(x * __builtin_fmaf(x * x, GC1, GC0));
    return x * __builtin_amdgcn_rcpf(1.0f + e);
}
DEVI void dma16(unsigned ldsaddr, const void* sbase, unsigned voff) {
    asm volatile("s_mov_b32 m0, %0\n\ts_nop 0\n\tglobal_load_lds_dwordx4 %1, %2" :: "s"(ldsaddr), "v"(voff), "s"(sbase) : "memory");
}
DEVI void dma16h(unsigned ldsaddr, const void* sbase, unsigned voff) {
    asm volatile("s_mov_b32 m0, %0\n\ts_mov_b32 exec_hi, 0\n\tglobal_load_lds_dwordx4 %1, %2\n\ts_mov_b32 exec_hi, -1" :: "s"(ldsaddr), "v"(voff), "s"(sbase) : "memory");
}
template <int N> DEVI void waitvm() { asm volatile("s_waitcnt vmcnt(%0)" :: "i"(N) : "memory"); }
DEVI void bar() { asm volatile("s_waitcnt lgkmcnt(0)\n\ts_barrier" ::: "memory"); }
DEVI bf16x8 trfrag(unsigned a0, unsigned a1) {
    s16x4 a = __builtin_amdgcn_ds_read_tr16_b64_v4i16((LAS s16x4*)(uintptr_t)a0);
    s16x4 b = __builtin_amdgcn_ds_read_tr16_b64_v4i16((LAS s16x4*)(uintptr_t)a1);
    return __builtin_bit_cast(bf16x8, __builtin_shufflevector(a, b, 0, 1, 2, 3, 4, 5, 6, 7));
}
template <int B, class F, int... I> DEVI void sfor_impl(F&& f, std::integer_sequence<int, I...>) { (f(std::integral_constant<int, B + I>{}), ...); }
template <int B, int E, class F> DEVI void sfor(F&& f) { sfor_impl<B>(f, std::make_integer_sequence<int, E - B>{}); }
template <class T> DEVI T ldsr(unsigned a) { return *(LAS T*)(uintptr_t)a; }
template <class T> DEVI void ldsw(unsigned a, T v) { *(LAS T*)(uintptr_t)a = v; }
DEVI rsrc_t mkrs(const void* p, unsigned bytes) { return __builtin_amdgcn_make_buffer_rsrc((void*)p, 0, bytes, 0x00020000); }
template <int AUX> DEVI u32x4 bl16(rsrc_t r, unsigned vo, unsigned so) { return __builtin_amdgcn_raw_buffer_load_b128(r, vo, so, AUX); }
template <int AUX> DEVI u32x2 bl8(rsrc_t r, unsigned vo, unsigned so) { return __builtin_amdgcn_raw_buffer_load_b64(r, vo, so, AUX); }
template <int AUX> DEVI unsigned bl4(rsrc_t r, unsigned vo, unsigned so) { return __builtin_amdgcn_raw_buffer_load_b32(r, vo, so, AUX); }
template <int AUX> DEVI void bs16(u32x4 v, rsrc_t r, unsigned vo, unsigned so) { __builtin_amdgcn_raw_buffer_store_b128(v, r, vo + so, 0, AUX); }
template <int AUX> DEVI void bs8(u32x2 v, rsrc_t r, unsigned vo, unsigned so) { __builtin_amdgcn_raw_buffer_store_b64(v, r, vo, so, AUX); }
template <int AUX> DEVI void bs4(unsigned v, rsrc_t r, unsigned vo, unsigned so) { __builtin_amdgcn_raw_buffer_store_b32(v, r, vo, so, AUX); }
DEVI void swap16(unsigned& a, unsigned& b) { auto r = __builtin_amdgcn_permlane16_swap(a, b, false, false); a = r[0]; b = r[1]; }
DEVI void swap32(unsigned& a, unsigned& b) { auto r = __builtin_amdgcn_permlane32_swap(a, b, false, false); a = r[0]; b = r[1]; }
DEVI void swap16f(float& a, float& b) { unsigned x = __float_as_uint(a), y = __float_as_uint(b); swap16(x, y); a = __uint_as_float(x); b = __uint_as_float(y); }
DEVI void swap32f(float& a, float& b) { unsigned x = __float_as_uint(a), y = __float_as_uint(b); swap32(x, y); a = __uint_as_float(x); b = __uint_as_float(y); }
template <int CTRL> DEVI float dppf(float x) { return __uint_as_float(__builtin_amdgcn_update_dpp(0u, __float_as_uint(x), CTRL, 0xf, 0xf, false)); }
DEVI float rowsum16(float x) { x += dppf<0x128>(x); x += dppf<0x124>(x); x += dppf<0x122>(x); x += dppf<0x121>(x); return x; }
DEVI f32x4 mfma16(bf16x8 a, bf16x8 b, f32x4 c) { return __builtin_amdgcn_mfma_f32_16x16x32_bf16(a, b, c, 0, 0, 0); }

constexpr unsigned CLSZ = 64 * 36 * 1024;
constexpr unsigned GTS = 2056 * 32;
constexpr unsigned O_FLAG = 0, O_YB = 65536, O_XT = O_YB + CLSZ, O_XS = O_XT + CLSZ, O_W1T = O_XS + 16384, O_W2T = O_W1T + 663552,
                   O_CSP = O_W2T + 663552, O_W2M = O_CSP + 41472, O_GB = O_W2M + 2304, O_HT = O_GB + 36 * GTS, O_HS = O_HT + CLSZ,
                   O_XB = O_HS + 65536, O_SQ = O_XB + CLSZ, O_R = O_SQ + 36 * 2048 * 4, O_HB = O_R + 2 * CLSZ, O_PB = O_HB + 128 * 144 * 1024,
                   O_PX = O_PB + 64 * 32 * 4 * 1024, O_END = O_PX + 64 * 4 * 16 * 1024, O_DBG = O_END;
constexpr float EPS = 1e-5f, RD576 = 1.0f / 576.0f;
#define XA 16
#define TDECL
#define BDECL
#define BT(i) do {} while (0)
#define BDUMP(kid) do {} while (0)
#define TS(i) do {} while (0)
#define TDUMP(kid, n) do {} while (0)
#define STAMP(kid, i) do { if ((threadIdx.x & 63) == 0) { ((u64*)(ws + O_DBG))[(((kid) * 256 + blockIdx.x) * 8 + (threadIdx.x >> 6)) * 8 + (i)] = __builtin_amdgcn_s_memrealtime(); } __builtin_amdgcn_s_waitcnt(0xC07F); } while (0)
#define CSTAMP(kid, i) do {} while (0)

#define P_CPF 1
#define P_KPF 1
constexpr int K1_KC = 0, K1_UR = 33792, K1_P = K1_UR + 18432, K1_LDS = K1_P + 32768;
__global__ __launch_bounds__(512, 2) void k1(const char* __restrict__ xq, const char* __restrict__ kk, const char* __restrict__ vv, const char* __restrict__ shk,
                                             const char* __restrict__ ssk, const u16* __restrict__ ssd, const char* __restrict__ w1, const char* __restrict__ w2,
                                             const char* __restrict__ w3, const char* __restrict__ w4, char* __restrict__ ws) {
    extern __shared__ __attribute__((aligned(256))) char smem[];
    const unsigned lb = (unsigned)(uintptr_t)(LAS char*)smem;
    const int wg = blockIdx.x, xcd = wg & 7, loc = wg >> 3, tid = threadIdx.x, lane = tid & 63;
    const int w = __builtin_amdgcn_readfirstlane(tid >> 6);
    const rsrc_t wr = mkrs(ws, O_END);
    STAMP(0, 0);
    if (loc < 18) {
        const int cw = 18 * xcd + loc;
        const int kch = tid >> 7, tau = tid & 127;
        const rsrc_t sr = mkrs(ssk, 576 * 4096);
        const unsigned so = (4 * cw + kch) * 4096 + tau * 32;
        u32x4 K0 = bl16<0>(sr, so, 0), K1 = bl16<0>(sr, so + 16, 0);
        unsigned K8 = bl4<0>(sr, so + 32, 0);
        const float dv = bflo(ssd[4 * cw + kch]);
        const rsrc_t kr = mkrs(kk, 2048 * 1152), vr = mkrs(vv, 2048 * 1152);
        u32x2 ks[4], vs[4];
        auto strip = [&](int g) AI { SB(); ks[g] = bl8<0>(kr, tid * 1152 + cw * 8, g * 512 * 1152); SB(); vs[g] = bl8<0>(vr, tid * 1152 + cw * 8, g * 512 * 1152); SB(); };
        strip(0);
        asm volatile("s_barrier" ::: "memory");
        strip(1);
        u64 halo[4][3];
#pragma unroll
        for (int g = 0; g < 4; ++g)
#pragma unroll
            for (int i = 0; i < 3; ++i) { int r = 512 * g + 64 * w - 1 - i; u64 t = *(const u64*)(kk + (size_t)(r < 0 ? 0 : r) * 1152 + cw * 8); halo[g][i] = r < 0 ? 0ull : t; }
        const u32x4 sa = *(const u32x4*)(shk + cw * 32), sb = *(const u32x4*)(shk + cw * 32 + 16);
        if (tid < 120) ldsw<u32x4>(lb + K1_UR + (tid / 30) * 4608 + 4096 + (tid % 30) * 16, u32x4{0, 0, 0, 0});
        if (tau == 0) K0[0] = (K0[0] & 0xffff0000u) | (pk2(bflo(K0[0]) + dv, 0.f) & 0xffffu);
        {
            const unsigned c0 = lb + K1_KC + kch * 8448 + 32 + tau * 32, c1 = c0 + 4224;
            ldsw<u32x4>(c0, K0); ldsw<u32x4>(c0 + 16, K1);
            ldsw<u32x4>(c1, u32x4{__builtin_amdgcn_alignbit(K0[1], K0[0], 16), __builtin_amdgcn_alignbit(K0[2], K0[1], 16), __builtin_amdgcn_alignbit(K0[3], K0[2], 16), __builtin_amdgcn_alignbit(K1[0], K0[3], 16)});
            ldsw<u32x4>(c1 + 16, u32x4{__builtin_amdgcn_alignbit(K1[1], K1[0], 16), __builtin_amdgcn_alignbit(K1[2], K1[1], 16), __builtin_amdgcn_alignbit(K1[3], K1[2], 16), __builtin_amdgcn_alignbit(K8, K1[3], 16)});
            if (tau == 0) {
                ldsw<u32x4>(c0 - 32, u32x4{0, 0, 0, 0}); ldsw<u32x4>(c0 - 16, u32x4{0, 0, 0, 0});
                ldsw<u32x4>(c1 - 32, u32x4{0, 0, 0, 0}); ldsw<u32x4>(c1 - 16, u32x4{0, 0, 0, K0[0] << 16});
            }
        }
        SB();
        bar();
        strip(2);
        STAMP(0, 1);
        const int ch = w & 3, hf = w >> 2, li = lane & 15, q = lane >> 4;
        u32x4 A[4][8];
        {
            const int r = (li + 1) & 1;
            unsigned aa = lb + K1_KC + ch * 8448 + r * 4224 + (128 * hf + li + 25 - 8 * q - r) * 2;
            OPAQ(aa);
#pragma unroll
            for (int c0 = 0; c0 < 4; ++c0)
#pragma unroll
                for (int c1 = 0; c1 < 8; ++c1) {
                    const unsigned a = aa + c1 * 512 + c0 * 64;
                    A[c0][c1] = u32x4{ldsr<unsigned>(a), ldsr<unsigned>(a + 4), ldsr<unsigned>(a + 8), ldsr<unsigned>(a + 12)};
                }
        }
        strip(3);
        f32x4 acc[8];
#pragma unroll
        for (int t = 0; t < 8; ++t) acc[t] = f32x4{0, 0, 0, 0};
        unsigned ba = lb + K1_UR + ch * 4608 + (2056 - 1792 - 16 * li + 128 * hf - 8 * q) * 2;
        OPAQ(ba);
        const unsigned ua = lb + K1_UR + (511 - tid) * 2;
        const unsigned pw = lb + K1_P + (hf * 4 + ch) * 2048 + li * 64 + q * 16;
        const unsigned pr = lb + K1_P + ((tid >> 4) * 32 + (tid & 15)) * 4;
        auto output = [&](int g) AI {
            if (tid < 256) {
                float y[2][4];
#pragma unroll
                for (int c = 0; c < 4; ++c)
#pragma unroll
                    for (int m = 0; m < 2; ++m) y[m][c] = ldsr<float>(pr + (g & 1) * 16384 + c * 2048 + m * 64) + ldsr<float>(pr + (g & 1) * 16384 + 8192 + c * 2048 + m * 64);
                bs16<0>(u32x4{pk2(y[0][0], y[0][1]), pk2(y[0][2], y[0][3]), pk2(y[1][0], y[1][1]), pk2(y[1][2], y[1][3])}, wr, (tid >> 4) * 36864 + (tid & 15) * 16, O_YB + g * 16 * 36864 + cw * 256);
            }
        };
        sfor<0, 4>([&](auto G) AI {
            constexpr int g = G;
            unsigned kx[4][2];
            kx[0][0] = ks[g][0]; kx[0][1] = ks[g][1];
#pragma unroll
            for (int i = 1; i < 4; ++i) {
                kx[i][0] = __builtin_amdgcn_update_dpp((unsigned)halo[g][i - 1], kx[i - 1][0], 0x138, 0xf, 0xf, false);
                kx[i][1] = __builtin_amdgcn_update_dpp((unsigned)(halo[g][i - 1] >> 32), kx[i - 1][1], 0x138, 0xf, 0xf, false);
            }
            float s0 = bflo(kx[0][0]) * bflo(sa[0]) + bflo(kx[1][0]) * bfhi(sa[0]) + bflo(kx[2][0]) * bflo(sa[1]) + bflo(kx[3][0]) * bfhi(sa[1]);
            float s1 = bfhi(kx[0][0]) * bflo(sa[2]) + bfhi(kx[1][0]) * bfhi(sa[2]) + bfhi(kx[2][0]) * bflo(sa[3]) + bfhi(kx[3][0]) * bfhi(sa[3]);
            float s2 = bflo(kx[0][1]) * bflo(sb[0]) + bflo(kx[1][1]) * bfhi(sb[0]) + bflo(kx[2][1]) * bflo(sb[1]) + bflo(kx[3][1]) * bfhi(sb[1]);
            float s3 = bfhi(kx[0][1]) * bflo(sb[2]) + bfhi(kx[1][1]) * bfhi(sb[2]) + bfhi(kx[2][1]) * bflo(sb[3]) + bfhi(kx[3][1]) * bfhi(sb[3]);
            const unsigned p01 = pk2(s0 * bflo(vs[g][0]), s1 * bfhi(vs[g][0])), p23 = pk2(s2 * bflo(vs[g][1]), s3 * bfhi(vs[g][1]));
            const unsigned u = ua + (3 - g) * 1024;
            ldsw<u16>(u, (u16)p01); ldsw<u16>(u + 4608, (u16)(p01 >> 16)); ldsw<u16>(u + 9216, (u16)p23); ldsw<u16>(u + 13824, (u16)(p23 >> 16));
            SB();
            bar();
            u32x4 B[8];
#pragma unroll
            for (int i = 0; i < 8; ++i) B[i] = ldsr<u32x4>(ba + (7 - 2 * g - (i >> 2)) * 512 + (i & 3) * 64);
            SB();
            if constexpr (g >= 1) output(g - 1);
            SB();
#pragma unroll
            for (int i = 0; i < 8; ++i) {
                const int b = 2 * g + (i >> 2);
#pragma unroll
                for (int c1 = 0; c1 + b < 8; ++c1) acc[b + c1] = mfma16(asb(A[i & 3][c1]), asb(B[i]), acc[b + c1]);
            }
            SB();
            ldsw<f32x4>(pw + (g & 1) * 16384, acc[2 * g]); ldsw<f32x4>(pw + (g & 1) * 16384 + 1024, acc[2 * g + 1]);
            STAMP(0, 2 + g);
        });
        bar();
        output(3);
    } else {
        const int u = (loc - 18) * 8 + w;
        if (u < 32 || (u >= 73 && u < 82)) {
            const bool isx = u < 32;
            const int rl = lane >> 4, c = lane & 15;
            const int rb = 32 * (xcd >> 2) + 4 * (u >> 2) + (xcd & 3), rho = 4 * (u & 3) + rl;
            const int r0 = isx ? 32 * rb + rho : 8 * (9 * xcd + u - 73) + rl, r1 = r0 + (isx ? 16 : 4);
            const rsrc_t xr = mkrs(isx ? xq : w2, isx ? 2048 * 1152 : 576 * 1152);
            u32x2 a[9], b[9];
#pragma unroll
            for (int m = 0; m < 9; ++m) { a[m] = bl8<0>(xr, r0 * 1152 + c * 8, m * 128); b[m] = bl8<0>(xr, r1 * 1152 + c * 8, m * 128); }
            float sa = 0, sb = 0;
#pragma unroll
            for (int m = 0; m < 9; ++m) {
                sa += (bflo(a[m][0]) + bfhi(a[m][0])) + (bflo(a[m][1]) + bfhi(a[m][1]));
                sb += (bflo(b[m][0]) + bfhi(b[m][0])) + (bflo(b[m][1]) + bfhi(b[m][1]));
                if (isx) bs16<0>(u32x4{a[m][0], a[m][1], b[m][0], b[m][1]}, wr, c * 256 + rho * 16, O_XT + rb * 36864 + m * 4096);
            }
            const float ma = rowsum16(sa) * RD576, mb = rowsum16(sb) * RD576;
            if (isx) {
                float qa = 0, qb = 0;
#pragma unroll
                for (int m = 0; m < 9; ++m)
#pragma unroll
                    for (int i = 0; i < 2; ++i) {
                        float t;
                        t = bflo(a[m][i]) - ma; qa += t * t; t = bfhi(a[m][i]) - ma; qa += t * t;
                        t = bflo(b[m][i]) - mb; qb += t * t; t = bfhi(b[m][i]) - mb; qb += t * t;
                    }
                const float ra = 1.0f / sqrtf(rowsum16(qa) * RD576 + EPS), rbb = 1.0f / sqrtf(rowsum16(qb) * RD576 + EPS);
                if (c == 0) { bs8<0>(u32x2{__float_as_uint(ra), __float_as_uint(-ma * ra)}, wr, r0 * 8, O_XS); bs8<0>(u32x2{__float_as_uint(rbb), __float_as_uint(-mb * rbb)}, wr, r1 * 8, O_XS); }
            } else if (c < 2) {
                const int k = c ? r1 : r0; const float m = c ? mb : ma;
                const int p = (k >> 3) * 32 + (k & 7) * 2;
                const unsigned hi = pk2(m, 0.f) & 0xffffu, lo = pk2(m - bflo(hi), 0.f) & 0xffffu;
                *(u16*)(ws + O_W2M + p) = (u16)hi; *(u16*)(ws + O_W2M + 16 + p) = (u16)lo;
            }
        } else if (u < 73) {
            const int g = 41 * xcd + u - 32;
            if (g < 324) {
                const int mat = g >= 162, r = g - 162 * mat, ks = r / 9, cg = r - 9 * ks;
                const rsrc_t mr = mkrs(mat ? w2 : w1, 576 * 1152);
                u32x4 t[4];
#pragma unroll
                for (int i = 0; i < 4; ++i) t[i] = bl16<0>(mr, (lane >> 3) * 1152 + (lane & 7) * 16, (32 * ks + 8 * i) * 1152 + cg * 128);
                const unsigned lw = lb + w * 4096;
#pragma unroll
                for (int i = 0; i < 4; ++i) ldsw<u32x4>(lw + i * 1024 + lane * 16, t[i]);
                const unsigned ta = lw + ((mat ? 8 : 4) * (lane >> 4) + ((lane & 15) >> 2)) * 128 + (lane & 3) * 8, tb = ta + (mat ? 512 : 2048);
                const u32x4 ones = {0x3f803f80u, 0x3f803f80u, 0x3f803f80u, 0x3f803f80u};
#pragma unroll
                for (int tt = 0; tt < 4; ++tt) {
                    const bf16x8 f = trfrag(ta + tt * 32, tb + tt * 32);
                    bs16<0>(__builtin_bit_cast(u32x4, f), wr, lane * 16, (mat ? O_W2T : O_W1T) + ((4 * cg + tt) * 18 + ks) * 1024);
                    if (!mat) {
                        const f32x4 cs = mfma16(f, asb(ones), f32x4{0, 0, 0, 0});
                        if ((lane & 15) == 0) bs16<0>(__builtin_bit_cast(u32x4, cs), wr, (lane >> 4) * 16, O_CSP + ((4 * cg + tt) * 18 + ks) * 64);
                    }
                }
            }
        } else if (u >= 84) {
            const rsrc_t r3 = mkrs(w3, 576 * 9216), r4 = mkrs(w4, 4608 * 1152);
            for (int i = 0; i < P_KPF; ++i) asm volatile("s_sleep 16");
            unsigned t[12], a = 0;
#pragma unroll
            for (int i = 0; i < 6; ++i) {
                const unsigned l = (i * 28 + u - 84) * 64 + lane, row = l / 18;
                t[i] = bl4<0>(r3, row * 9216 + (l - row * 18) * 128 + (xcd & 3) * 2304, 0);
            }
#pragma unroll
            for (int i = 0; i < 6; ++i) t[6 + i] = bl4<0>(r4, ((i * 28 + u - 84) * 64 + lane) * 128 + (xcd & 3) * (1152 * 1152), 0);
#pragma unroll
            for (int i = 0; i < 12; ++i) a ^= t[i];
            asm volatile("" :: "v"(a));
        } else if (u == 83) {
            {
                u64 pc; unsigned len;
                asm volatile("s_getpc_b64 %0\n.Lcode_lo:\n\ts_mov_b32 %1, .Lcode_hi-.Lcode_lo" : "=s"(pc), "=s"(len));
                const rsrc_t cr = mkrs((const void*)(pc & ~127ull), len);
                unsigned t[8], a = 0;
#pragma unroll
                for (int i = 0; i < 8; ++i) t[i] = bl4<0>(cr, lane * 128, i * 8192);
#pragma unroll
                for (int i = 0; i < 8; ++i) a ^= t[i];
                asm volatile("" :: "v"(a));
            }
            bs16<0>(u32x4{0, 0, 0, 0}, wr, lane * 16, O_FLAG + xcd * 2048); bs16<0>(u32x4{0, 0, 0, 0}, wr, lane * 16, O_FLAG + xcd * 2048 + 1024);
        } else if (u == 82) {
            const int l = 9 * xcd + (lane >> 3);
            bs16<0>(u32x4{0, 0, 0, 0}, wr, (l >> 1) * GTS + (l & 1) * (2052 * 32) + (lane & 7) * 16, O_GB);
            const int l2 = 9 * xcd + 8;
            if (lane < 8) bs16<0>(u32x4{0, 0, 0, 0}, wr, (l2 >> 1) * GTS + (l2 & 1) * (2052 * 32) + lane * 16, O_GB);
        }
    }
    STAMP(0, 7);
}

constexpr int SLOT = 18432, NSL = 6, L_EX = NSL * SLOT, L_F = L_EX, L_ST = L_EX + 36864, L_MN = L_ST + 2048, K2_LDS = L_MN + 1024, L_PE = 5 * SLOT;
constexpr unsigned F_B = O_FLAG, F_C = O_FLAG + 66 * 128, F_D = F_C + 16 * 128, F_E = F_D + 16 * 128;
#define P_RD 4
#define P_B0 0
#define P_WBS {0, 2, 5, 8, 11, 14}
#define P_KB 8
#define P_KC 3
#define P_W2A 6
#define P_W2B 6
#define P_W2C 6
#define P_PRA 3
#define P_PRB 4
#define P_X0 10
#define P_PRIO 2
constexpr int RD = P_RD, BP = RD + 1, HD = 8;
constexpr int SPIN = 1 << 17;

template <class HK> DEVI void gemm_small(const u32x4* W, unsigned fa, f32x4* acc, HK&& hook) {
    constexpr int PF = 3;
    u32x4 b[PF + 1][2];
    sfor<0, 18 + PF>([&](auto K) AI {
        constexpr int k = K, m = k - PF;
        if constexpr (m >= 0) {
            const f32x4 z = {0, 0, 0, 0};
            acc[0] = mfma16(asb(W[m]), asb(b[m % (PF + 1)][0]), m ? acc[0] : z); acc[1] = mfma16(asb(W[m]), asb(b[m % (PF + 1)][1]), m ? acc[1] : z);
            SB();
        }
        if constexpr (k < 18) { b[k % (PF + 1)][0] = ldsr<u32x4>(fa + k * 2048); b[k % (PF + 1)][1] = ldsr<u32x4>(fa + k * 2048 + 1024); SB(); }
        if constexpr (m >= 0) { hook(std::integral_constant<int, m>{}); SB(); }
    });
}
DEVI void poll(rsrc_t wr, unsigned off, unsigned need) {
#pragma unroll 1
    for (int i = 0; i < SPIN; ++i) {
        const unsigned v = bl4<16>(wr, off, 0);
        if (__builtin_amdgcn_ballot_w64(v < need) == 0) break;
        asm volatile("s_sleep 1" ::: "memory");
    }
    asm volatile("" ::: "memory");
}
constexpr int e_mem(int j) { return j < 4 ? 0 : j < 13 ? 1 + (j - 4) / 3 : j < 16 ? 0 : j < 25 ? 1 + (j - 16) / 3 : j < 27 ? 0 : j < 33 ? 1 + (j - 27) / 2 : j - 32; }
constexpr int e_til(int j) { return j < 4 ? j : j < 13 ? (j - 4) % 3 : j < 16 ? j - 9 : j < 25 ? 3 + (j - 16) % 3 : j < 27 ? j - 18 : j < 33 ? 6 + (j - 27) % 2 : 8; }
constexpr int e_first(int j) { int f = 0; for (int i = 0; i < j; ++i) f += e_mem(i) == 3 ? 12 : 8; return f; }
#define P_FB {12, 18, 24, 28}
#define P_QB {17, 23, 29, 32}
#define P_SD 2
#define P_TP 1
constexpr int FB[4] = P_FB, QB[4] = P_QB, LH[4] = {5, 11, 15, 17}, LJ[5] = {4, 16, 27, 33, 99};
constexpr int f_lvl(int u) { for (int l = 0; l < 4; ++l) if (FB[l] == u) return l + 1; return 0; }
constexpr int q_lvl(int u) { for (int l = 0; l < 4; ++l) if (QB[l] == u) return l + 1; return 0; }
constexpr signed char ACT_D[18][4] = {{0,1,8,-1},{16,9,24,-1},{32,17,40,-1},{25,2,48,10},{33,18,41,-1},{26,3,49,11},{34,19,42,-1},{27,4,50,12},{35,20,43,-1},{28,5,51,13},
                                      {36,21,44,-1},{29,6,52,14},{37,22,45,-1},{30,7,53,15},{38,23,46,-1},{31,54,56,-1},{39,57,47,-1},{55,58,59,64}};
constexpr int NE = 33;
constexpr signed char ACT_E[NE][2] = {{0,8},{16,1},{24,9},{32,17},{40,2},{25,48},{33,10},{41,18},{26,49},{56,34},{42,3},{50,11},{19,4},{27,12},{35,20},{43,5},{28,51},{57,36},{44,13},{52,21},{29,6},{37,14},{45,22},{30,53},{58,38},{46,7},{54,15},{23,-1},{31,-1},{39,-1},{47,-1},{55,59},{64,-1}};
constexpr int b_slot(int u) { return u < 18 ? 18 * u + 1 : 324 + e_first(2 * (u - 18)) + 1; }
constexpr int mx(int a, int b) { return a > b ? a : b; }
constexpr int h_pos(int j, int mt) {
    int p = j < 8 ? 290 + 4 * j + 2 * mt : 324 + e_first(j - 7) + 2 + 2 * mt;
    if (e_mem(j) == 0) { if (j == 26) p = mx(p, b_slot(FB[3]) + 1 + 2 * mt); return p; }
    for (int l = 0; l < 4; ++l) if (j >= LJ[l] && j < LJ[l + 1]) p = mx(p, b_slot(QB[l]) + 1 + 2 * (j - LJ[l]) + mt);
    return p;
}
constexpr int s_pos(int h) { return h < 14 ? 90 + 18 * h : 325 + NE * (h - 13) - 1; }
constexpr int m_wait(int h, int u) {
    int n = 0;
    for (int i = h + 1; i < 18; ++i) n += s_pos(i) < b_slot(u);
    for (int j = 0; j < 36; ++j) for (int mt = 0; mt < 2; ++mt) n += h_pos(j, mt) >= s_pos(h) && h_pos(j, mt) < b_slot(u);
    return n;
}
constexpr int x_at(int s) { return (!(s & 1) && P_X0 + s / 2 < 36) ? P_X0 + s / 2 : -1; }
struct HTab { int at[2][648], st[330], fi[37], vm[648]; bool ok;
    constexpr HTab() : at{}, st{}, fi{}, vm{}, ok(true) {
        int cnt[649] = {}, pre[649] = {}, done[649] = {};
        for (int s = 0; s < 648; ++s) { at[0][s] = at[1][s] = -1; vm[s] = 63; }
        for (int j = 0; j < 37; ++j) fi[j] = e_first(j);
        for (int h = 0; h < 18; ++h) ++cnt[s_pos(h)];
        for (int l = 0; l < 72; ++l) { const int p = h_pos(l >> 1, l & 1); ++cnt[p]; if (at[0][p] < 0) at[0][p] = l; else if (at[1][p] < 0) at[1][p] = l; else ok = false; if (p >= 323 + fi[l >> 1]) ok = false; }
        for (int s = 0; s < 648; ++s) pre[s + 1] = pre[s] + cnt[s];
        for (int e = 0, j = 0; e < 330; ++e) { while (j < 35 && fi[j + 1] <= e) ++j; st[e] = j; }
        for (int j = 0; j < 36; ++j) {
            const int p = h_pos(j, 1), s = 323 + fi[j];
            if (p < h_pos(j, 0)) ok = false;
            const int n = pre[s] - pre[p + 1] + (at[0][p] == 2 * j + 1 && at[1][p] >= 0 ? 1 : 0);
            if (n >= 63) ok = false;
            vm[s] = n; done[s] = pre[s] - n;
        }
        for (int l = 0; l < 4; ++l) { const int s = b_slot(FB[l]); done[s] = pre[s] - m_wait(LH[l], FB[l]); if (s_pos(LH[l]) >= s || FB[l] + P_SD >= QB[l]) ok = false; }
        for (int s = 1; s < 648; ++s) done[s] = mx(done[s], done[s - 1]);
        for (int j = 0; j < 36; ++j) if (e_mem(j) == 0 && j >= 4 && done[h_pos(j, 0)] <= pre[s_pos(2 * e_til(j) + 1)]) ok = false;
        for (int k = 1; k < 36; ++k) { int n = P_X0; for (int s = 0; s < 2 * k - 1; ++s) n += x_at(s) >= 0; vm[2 * k - 1] = n - 1 - k; if (n <= k) ok = false; }
        vm[289] = pre[289] - pre[s_pos(7)] - 1;
    } };
constexpr HTab HT{};
static_assert(HT.ok, "schedule");
constexpr int wcnt(int vm, int lgkm) { return (vm & 15) | ((vm >> 4) << 14) | 0x70 | (lgkm << 8); }
constexpr int g_nd(int i) { return i + 5 < 18 ? 5 : i + 5 < 36 ? 4 : 0; }
constexpr int g_nx(int i) { return (i >= 4 && i < 13) ? 1 : 0; }
constexpr int g_pre(int i) { return q_lvl(i + P_SD) ? 1 : 0; }
#define P_PF 8
constexpr int g_pf(int i) { return P_PF == 0 ? -1 : (i >= P_PF && i <= P_PF + 12 && !((i - P_PF) & 1)) ? (i - P_PF) / 2 : i == FB[2] ? 7 : i == FB[3] ? 8 : -1; }
constexpr int g_all(int i) { return g_pre(i) + g_nd(i) + g_nx(i) + (g_pf(i) >= 0); }
constexpr int g_wait(int u) {
    int n = g_nx(u - 4) + (g_pf(u - 4) >= 0) + g_all(u - 3) + g_all(u - 2) + g_all(u - 1);
    if (q_lvl(u)) { n = -1; for (int i = 1; i <= P_SD; ++i) n += g_all(u - i); }
    return n;
}

__global__ __launch_bounds__(512, 2) void k2(const char* __restrict__ w3, const char* __restrict__ w4, char* __restrict__ ws, char* __restrict__ out) {
    extern __shared__ __attribute__((aligned(256))) char smem[];
    const unsigned lb = (unsigned)(uintptr_t)(LAS char*)smem;
    const int wg = blockIdx.x, xcd = wg & 7, loc = wg >> 3, lane = threadIdx.x & 63;
    const int kap = xcd & 3, rt = 8 * (xcd >> 2) + (loc >> 2), nt = loc & 3, rb = 4 * rt + kap, fc = 4 * kap + nt;
    const int w = __builtin_amdgcn_readfirstlane(threadIdx.x >> 6), wm = w & 3;
    const int lj = lane & 15, lq = lane >> 4, hh = lane >> 5;
    const unsigned l16 = lane * 16;
    const rsrc_t wr = mkrs(ws, O_END);
    BDECL; BT(0);
    STAMP(1, 0);
    const int a9 = 9 * (fc >> 1), odd = fc & 1;
    const char* wp = w3 + (a9 + 5 * odd) * 128 + (size_t)wm * 36 * 9216;
    const char* wsg = w3 + (a9 + 4) * 128 + odd * 64 + (size_t)wm * 72 * 9216;
    const unsigned pvo = (lane >> 3) * 9216 + (((lane & 7) ^ (((lane >> 4) & 1) << 2)) * 16), svo = (lane >> 2) * 9216 + (lane & 3) * 16;
    auto dmaD = [&](int u, int i) AI {
        const unsigned dst = lb + (u % NSL) * SLOT + wm * 4608 + i * 1024;
        const char* src = u < 16 ? wp + (u >> 2) * 128 + (size_t)((u & 3) * 144 + i * 8) * 9216 : wsg + (size_t)((u & 1) * 288 + i * 16) * 9216;
        if (i < 4) dma16(dst, src, u < 16 ? pvo : svo); else dma16h(dst, src, u < 16 ? pvo : svo);
    };

    {
    constexpr unsigned OOB = 0x80000000u;
    u32x4 Y[5], X[5], W1[21], W2[24];
    int nu[5];
#pragma unroll
    for (int m = 0; m < 4; ++m) nu[m] = ((nt + m) & 3) * 8 + w;
    nu[4] = 32 + (w >> 1);
    const unsigned h16 = l16 + (w & 1) * 512, h16v = lane < 32 ? h16 : OOB, e16 = w < 2 ? l16 : OOB;
    BT(1);
    const u32x2 xs0 = bl8<0>(wr, lj * 8, O_XS + rb * 256), xs1 = bl8<0>(wr, lj * 8, O_XS + rb * 256 + 128);
#pragma unroll
    for (int m = 0; m < 5; ++m) { Y[m] = bl16<0>(wr, m < 4 ? l16 : h16v, O_YB + (rb * 36 + nu[m]) * 1024); X[m] = bl16<0>(wr, m < 4 ? l16 : h16v, O_XT + (rb * 36 + nu[m]) * 1024); SB(); }
    BT(2);
    if (P_B0) asm volatile("s_barrier" ::: "memory");
    BT(3);
    auto ldw = [&](u32x4* W, unsigned base, int k) AI {
        if (k < 0) W[k] = bl16<0>(wr, (lj < 2 && (k < -1 || w < 2)) ? lq * 32 + lj * 16 : OOB, O_W2M + (w + 8 * (k + 3)) * 128);
        else if (k < 3) W[k] = bl16<0>(wr, k < 2 ? l16 : e16, base + ((32 + nt) * 18 + w + 8 * k) * 1024);
        else W[k] = bl16<0>(wr, l16, base + ((8 * nt + w) * 18 + k - 3) * 1024);
        SB();
    };
    auto ldw2 = [&](int k) AI { ldw(W2 + 3, O_W2T, k - 3); };
    auto prime = [&](int i) AI { dmaD(2 * (i / 5) + (w >> 2), i % 5); SB(); };
    f32x2 s[2] = {{0, 0}, {0, 0}}, sq[2] = {{0, 0}, {0, 0}};
    const float rx[2] = {__uint_as_float(xs0[0]), __uint_as_float(xs1[0])}, mx[2] = {__uint_as_float(xs0[1]), __uint_as_float(xs1[1])};
    u32x4 hown, ca, cb, cea, ceb;
    constexpr int WBS[6] = P_WBS;
#pragma unroll
    for (int m = 0; m < 5; ++m) {
#pragma unroll
        for (int k = m ? WBS[m - 1] : 0; k < WBS[m]; ++k) ldw(W1, O_W1T, k);
        BT(4 + 2 * m);
        u32x4 hp;
#pragma unroll
        for (int i = 0; i < 4; ++i) {
            const int mt = i >> 1;
            const f32x2 x = {bflo(X[m][i]), bfhi(X[m][i])}, y = {bflo(Y[m][i]), bfhi(Y[m][i])};
            const f32x2 h = __builtin_elementwise_fma(__builtin_elementwise_fma(x, f32x2{rx[mt], rx[mt]}, f32x2{mx[mt], mx[mt]}), y, x);
            s[mt] += h; sq[mt] = __builtin_elementwise_fma(h, h, sq[mt]);
            hp[i] = pk2(h[0], h[1]);
        }
        OPAQ(s[0]); OPAQ(s[1]); OPAQ(sq[0]); OPAQ(sq[1]);
        const unsigned a = lb + L_F + (nu[m] >> 1) * 2048 + (nu[m] & 1) * 8 + (m < 4 ? l16 : h16);
        if (m < 4 || lane < 32) { ldsw<u32x2>(a, u32x2{hp[0], hp[1]}); ldsw<u32x2>(a + 1024, u32x2{hp[2], hp[3]}); }
        if (m == 0) hown = hp;
        SB();
        BT(5 + 2 * m);
    }
    {
        float s0 = s[0][0] + s[0][1], s1 = s[1][0] + s[1][1], q0 = sq[0][0] + sq[0][1], q1 = sq[1][0] + sq[1][1];
        swap16f(s0, s1); swap16f(q0, q1);
        float t = s0 + s1, u = q0 + q1;
        swap32f(t, u);
        ldsw<float>(lb + L_ST + w * 256 + lane * 4, t + u);
    }
    SB();
    {
        const unsigned co = lj * 64 + lq * 16, co2 = lj < 2 ? co + 1024 : OOB;
        ca = bl16<0>(wr, co, O_CSP + (8 * nt + w) * 1152); cb = bl16<0>(wr, co2, O_CSP + (8 * nt + w) * 1152);
        cea = bl16<0>(wr, w < 2 ? co : OOB, O_CSP + (32 + nt) * 1152); ceb = bl16<0>(wr, w < 2 ? co2 : OOB, O_CSP + (32 + nt) * 1152);
        SB();
    }
#pragma unroll
    for (int k = WBS[4]; k < WBS[5]; ++k) ldw(W1, O_W1T, k);
    bar();
    BT(14);
    f32x4 acc[2];
    unsigned fa = lb + L_F + l16; OPAQ(fa);
    const unsigned fx = fa + w * 2048, fx2 = fa + (16 + (w & 1)) * 2048, pw = lb + L_PE + w * 2048 + l16, pr = lb + L_PE + (w & 1) * 1024 + l16;
    u32x4 xb[3][2];
    auto slices = [&](const u32x4* We) AI {
#pragma unroll
        for (int i = 0; i < 3; ++i)
#pragma unroll
            for (int mt = 0; mt < 2; ++mt) acc[mt] = mfma16(asb(We[i]), asb(xb[i][mt]), i ? acc[mt] : f32x4{0, 0, 0, 0});
    };
    auto xread = [&]() AI {
#pragma unroll
        for (int mt = 0; mt < 2; ++mt) { xb[0][mt] = ldsr<u32x4>(fx + mt * 1024); xb[1][mt] = ldsr<u32x4>(fx + 16384 + mt * 1024); xb[2][mt] = ldsr<u32x4>(fx2 + mt * 1024); }
    };
    auto psum = [&]() AI { f32x4 t[8];
#pragma unroll
        for (int i = 0; i < 8; ++i) t[i] = ldsr<f32x4>(pr + i * 2048);
        return ((t[0] + t[1]) + (t[2] + t[3])) + ((t[4] + t[5]) + (t[6] + t[7])); };
    auto gelu4 = [&](f32x4 z) AI {
        const f32x4 u = z * __builtin_elementwise_fma(z * z, f32x4{GC1, GC1, GC1, GC1}, f32x4{GC0, GC0, GC0, GC0});
        f32x4 e;
#pragma unroll
        for (int r = 0; r < 4; ++r) e[r] = __builtin_amdgcn_exp2f(u[r]);
        e += 1.0f;
#pragma unroll
        for (int r = 0; r < 4; ++r) e[r] = __builtin_amdgcn_rcpf(e[r]);
        return z * e; };
    xread();
    float tv[8];
#pragma unroll
    for (int i = 0; i < 8; ++i) tv[i] = ldsr<float>(lb + L_ST + i * 256 + lane * 4);
    const u32x2 hext = ldsr<u32x2>(fa + (w & 1) * 1024 + ((32 + nt) >> 1) * 2048 + (nt & 1) * 8);
    SB();
    BT(15);
    slices(W1);
    ldsw<f32x4>(pw, acc[0]); ldsw<f32x4>(pw + 1024, acc[1]);
    SB();
    BT(16);
    float mm[2], rr[2], r1[2];
    f32x4 cz[2];
    gemm_small(W1 + 3, fa, acc, [&](auto K) AI {
        constexpr int k = K;
        if constexpr (k + WBS[5] < 21) ldw(W1, O_W1T, k + WBS[5]);
        if constexpr (k == 0) {
            float su = ((tv[0] + tv[1]) + (tv[2] + tv[3])) + ((tv[4] + tv[5]) + (tv[6] + tv[7])), qu = su; swap32f(su, qu);
            const float mean = su * RD576, var = fmaxf(qu * RD576 - mean * mean, 0.f);
            const float r1s = __builtin_amdgcn_rsqf(var + EPS), r12 = r1s * __builtin_amdgcn_rsqf(var * r1s * r1s + EPS);
            mm[0] = mm[1] = mean; rr[0] = rr[1] = r12; r1[0] = r1[1] = r1s;
            swap16f(mm[0], mm[1]); swap16f(rr[0], rr[1]); swap16f(r1[0], r1[1]);
            OPAQ(mm[0]); OPAQ(rr[0]); OPAQ(r1[0]);
        }
        if constexpr (k == P_KB) asm volatile("s_barrier" ::: "memory");
        if constexpr (k >= 4 && k < 8) {
            constexpr int r = k - 4;
            const float c = rowsum16(__uint_as_float(ca[r]) + __uint_as_float(cb[r]));
            cz[0][r] = -mm[0] * rr[0] * c; cz[1][r] = -mm[1] * rr[1] * c; OPAQ(cz[0][r]); OPAQ(cz[1][r]);
        }
        if constexpr (k == P_KB + 1) {
            if (w < 2) {
                const f32x4 ae = psum();
                const float me = w ? mm[1] : mm[0], re = w ? rr[1] : rr[0];
                f32x4 z;
#pragma unroll
                for (int r = 0; r < 4; ++r) z[r] = (ae[r] - me * rowsum16(__uint_as_float(cea[r]) + __uint_as_float(ceb[r]))) * re;
                const f32x4 g = gelu4(z);
                unsigned a0 = pk2(g[0], g[1]), a1 = pk2(g[2], g[3]), b0 = a0, b1 = a1;
                swap16(a0, b0); swap16(a1, b1);
                if (!(lq & 1)) bs16<XA>(u32x4{a0, a1, b0, b1}, wr, lj * 32 + hh * 16, O_GB + (32 + nt) * GTS + (4 + 32 * rb + 16 * w) * 32);
            }
        }
        if constexpr (k % 3 == 0) BT(17 + k / 3);
        if constexpr (k >= 18 - P_W2A) ldw2(k - (18 - P_W2A));
    });
    BT(23);
    {
        unsigned o[2][2];
#pragma unroll
        for (int mt = 0; mt < 2; ++mt) {
            const f32x4 g = gelu4(__builtin_elementwise_fma(acc[mt], f32x4{rr[mt], rr[mt], rr[mt], rr[mt]}, cz[mt]));
            o[mt][0] = pk2(g[0], g[1]); o[mt][1] = pk2(g[2], g[3]);
        }
        swap16(o[0][0], o[1][0]); swap16(o[0][1], o[1][1]);
        bs16<XA>(u32x4{o[0][0], o[0][1], o[1][0], o[1][1]}, wr, (lane & 31) * 32 + hh * 16, O_GB + (8 * nt + w) * GTS + (4 + 32 * rb) * 32);
    }
    SB();
    BT(24);
#pragma unroll
    for (int k = P_W2A; k < P_W2A + P_W2B; ++k) ldw2(k);
    BT(25);
    waitvm<P_W2B>();
    BT(26);
    bar();
    BT(27);
    if (w == 0 && lane == 0) bs4<16>(1u, wr, 0, F_B + (rb + 1) * 128 + nt * 4);
    STAMP(1, 3);
#pragma unroll
    for (int k = P_W2A + P_W2B; k < P_W2A + P_W2B + P_W2C; ++k) ldw2(k);
    BT(28);
    {
        const int pr = rb + (lane >> 2);
        poll(wr, F_B + (lane < 12 ? pr * 128 + (lane & 3) * 4 : 0), (lane < 12 && pr >= 1 && pr <= 64) ? 1u : 0u);
    }
    BT(29);
    u32x4 G[5];
    auto gaddr = [&](int n) AI { const int gr = n / 9, sh = gr == 0 ? 1 : gr == 1 ? -1 : gr == 2 ? 2 : -2; return O_GB + n * GTS + (4 + 32 * rb - sh) * 32; };
#pragma unroll
    for (int m = 0; m < 5; ++m) { G[m] = bl16<XA>(wr, m < 4 ? l16 : h16v, gaddr(m < 4 ? 8 * m + w : 32 + (w >> 1))); SB(); }
    const unsigned gw = lb + L_F + (((lane >> 1) & 15) + 16 * (lane & 1)) * 16;
#pragma unroll
    for (int m = 0; m < 4; ++m) ldsw<u32x4>(gw + (w >> 1) * 2048 + hh * 1024 + (w & 1) * 512 + m * 8192, G[m]);
    if (lane < 32) ldsw<u32x4>(gw + (16 + (w >> 2)) * 2048 + (w & 1) * 1024 + ((w >> 1) & 1) * 512, G[4]);
    SB();
#pragma unroll
    for (int k = P_W2A + P_W2B + P_W2C; k < 24; ++k) ldw2(k);
    BT(30);
    bar();
    BT(31);
    xread();
    SB();
    slices(W2 + 3);
    ldsw<f32x4>(pw, acc[0]); ldsw<f32x4>(pw + 1024, acc[1]);
    slices(W2);
    {
        float a = acc[0][0] + acc[0][1], b = acc[1][0] + acc[1][1];
        swap16f(a, b);
        if (lane < 32) ldsw<float>(lb + L_MN + w * 128 + lane * 4, a);
    }
    SB();
    BT(32);
    float mn[2];
    f32x4 hr[2], rese;
    auto quad = [&](float t) AI { return u32x4{__float_as_uint(t), __float_as_uint(dppf<0x55>(t)), __float_as_uint(dppf<0xAA>(t)), __float_as_uint(dppf<0xFF>(t))}; };
    gemm_small(W2 + 6, fa, acc, [&](auto K) AI {
        constexpr int k = K;
        if constexpr (k == P_KC) {
            asm volatile("s_barrier" ::: "memory");
#pragma unroll
            for (int i = 0; i < 4; ++i) tv[i] = ldsr<float>(lb + L_MN + i * 256 + lane * 4);
        }
        if constexpr (k == P_KC + 2) {
            float t = (tv[0] + tv[1]) + (tv[2] + tv[3]), u = t; swap32f(t, u);
            mn[0] = mn[1] = t + u; swap16f(mn[0], mn[1]); OPAQ(mn[0]);
        }
        if constexpr (k == P_KC + 3) {
            if (w < 2) {
                const f32x4 ae = psum();
                const float h[4] = {bflo(hext[0]), bfhi(hext[0]), bflo(hext[1]), bfhi(hext[1])};
                const float re = w ? r1[1] : r1[0], me = w ? mm[1] : mm[0], ne = w ? mn[1] : mn[0];
                float xc[4];
#pragma unroll
                for (int r = 0; r < 4; ++r) { const float x1 = __builtin_fmaf(h[r] - me, re, ae[r]); xc[r] = x1 - ne; rese[r] = h[r] + x1; }
                unsigned a0 = pk2(xc[0], xc[1]), a1 = pk2(xc[2], xc[3]), b0 = a0, b1 = a1;
                swap16(a0, b0); swap16(a1, b1);
                if (!(lq & 1)) bs16<XA>(u32x4{a0, a1, b0, b1}, wr, hh * 512 + lj * 16, O_XB + (rb * 36 + 32 + nt) * 1024 + w * 256);
                float t = (xc[0] * xc[0] + xc[1] * xc[1]) + (xc[2] * xc[2] + xc[3] * xc[3]), u = t;
                swap16f(t, u); t += u; u = t; swap32f(t, u);
                const u32x4 qv = quad(t + u);
                if (lane < 16 && !(lane & 3)) bs16<XA>(qv, wr, lane * 4, O_SQ + (rb * 36 + 32 + nt) * 128 + 64 * w);
                bs16<0>(__builtin_bit_cast(u32x4, rese), wr, l16, O_R + ((rb * 36 + 32 + nt) * 2 + w) * 1024);
            }
        }
        if constexpr (k >= P_KC + 4 && k < P_KC + 8) {
            constexpr int i = k - (P_KC + 4), mt = i >> 1, r = 2 * (i & 1);
            const float h0 = bflo(hown[i]), h1 = bfhi(hown[i]);
            cz[mt][r] = __builtin_fmaf(h0 - mm[mt], r1[mt], -mn[mt]); cz[mt][r + 1] = __builtin_fmaf(h1 - mm[mt], r1[mt], -mn[mt]);
            hr[mt][r] = h0 + mn[mt]; hr[mt][r + 1] = h1 + mn[mt];
            OPAQ(cz[mt][r]); OPAQ(cz[mt][r + 1]); OPAQ(hr[mt][r]); OPAQ(hr[mt][r + 1]);
        }
        if constexpr (k >= 18 - P_PRA) prime(k - (18 - P_PRA));
    });
    BT(33);
    {
        unsigned o[2][2]; float q2[2]; f32x4 xc[2];
#pragma unroll
        for (int mt = 0; mt < 2; ++mt) { xc[mt] = acc[mt] + cz[mt]; o[mt][0] = pk2(xc[mt][0], xc[mt][1]); o[mt][1] = pk2(xc[mt][2], xc[mt][3]); }
        swap16(o[0][0], o[1][0]); swap16(o[0][1], o[1][1]);
        bs16<XA>(u32x4{o[0][0], o[0][1], o[1][0], o[1][1]}, wr, l16, O_XB + (rb * 36 + 8 * nt + w) * 1024);
        SB();
#pragma unroll
        for (int mt = 0; mt < 2; ++mt) { const f32x4 t = xc[mt] * xc[mt]; q2[mt] = (t[0] + t[1]) + (t[2] + t[3]); }
        {
            swap16f(q2[0], q2[1]);
            float t = q2[0] + q2[1], u = t; swap32f(t, u);
            const u32x4 qv = quad(t + u);
            if (lane < 32 && !(lane & 3)) bs16<XA>(qv, wr, lane * 4, O_SQ + (rb * 36 + 8 * nt + w) * 128);
        }
        SB();
        BT(34);
#pragma unroll
        for (int i = P_PRA; i < P_PRA + P_PRB; ++i) prime(i);
        BT(35);
        waitvm<P_PRB>();
        BT(36);
        bar();
        BT(37);
        if (w == 0 && lane == 0) bs4<16>(1u, wr, 0, F_C + rt * 128 + (kap * 4 + nt) * 4);
#pragma unroll
        for (int i = P_PRA + P_PRB; i < 10; ++i) prime(i);
#pragma unroll
        for (int mt = 0; mt < 2; ++mt) bs16<0>(__builtin_bit_cast(u32x4, xc[mt] + hr[mt]), wr, l16, O_R + ((rb * 36 + 8 * nt + w) * 2 + mt) * 1024);
    }
    }
    BT(38);
    BDUMP(1);
    STAMP(1, 7);

    unsigned kb[4], kq[4];
#pragma unroll
    for (int i = 0; i < 4; ++i) { const int m = (nt + i) & 3; kb[i] = 36 * kap + 18 * (m >> 1) + 10 * (m & 1); kq[i] = 36 * kap + 18 * (m >> 1) + 8 + (m & 1); }
    const unsigned kpa = lb + L_ST + wm * 512 + (lane & 31) * 16;
    if (w < 4) {
        poll(wr, F_C + rt * 128 + (wm * 4 + (lane & 3)) * 4, 1u);
        STAMP(2, 0);
        u32x4 X[36];
#pragma unroll
        for (int i = 0; i < P_X0; ++i) X[i] = bl16<XA>(wr, l16, O_XB + ((4 * rt + wm) * 36 + i) * 1024);
        asm volatile("s_barrier" ::: "memory");
        const int r = 8 * hh + (lj >> 2);
        unsigned ta[2][2], ts;
#pragma unroll
        for (int s = 0; s < 2; ++s)
#pragma unroll
            for (int ab = 0; ab < 2; ++ab) { ta[s][ab] = lb + s * 3 * SLOT + r * 128 + ((ab ^ ((r >> 1) & 1)) * 64) + (lq & 1) * 32 + (lane & 3) * 8; OPAQ(ta[s][ab]); }
        ts = lb + 3 * SLOT + r * 64 + (lq & 1) * 32 + (lane & 3) * 8; OPAQ(ts);
        const int re = 16 * (lq >> 1) + 4 * (lq & 1) + (lj >> 2);
        unsigned am[2][8], ax[4];
        const unsigned amb = lb + re * 256 + ((re & 7) << 5) + (lane & 3) * 8, axb = lb + L_EX + re * 128 + (((re >> 1) & 3) << 5) + (lane & 3) * 8;
        u32x4 H[HD][2];
        const unsigned hvo = l16 + (8 * rt + 2 * wm) * 144 * 1024;
        unsigned hso = (8 * rt + 2 * wm + (lq & 1)) * 144 * 1024 + (lj + 16 * hh) * 16 + O_HB + kb[0] * 1024;
        OPAQ(hso);
        auto ldH = [&](int j, int mt) AI {
            const int t = e_til(j);
            H[j % HD][mt] = bl16<XA>(wr, hvo + mt * 144 * 1024 + (t & 3) * 1024, O_HB + (t < 8 ? kb[e_mem(j)] + (t & 4) : kq[e_mem(j)]) * 1024);
        };
        __builtin_amdgcn_s_setprio(P_PRIO);
        f32x16 acc[4]; f32x4 ec[12][2]; bf16x8 A[RD + 1];
        f32x4 kp; float t[8]; u32x4 hp;
        sfor<0, 19>([&](auto C) AI { sfor<0, 36>([&](auto I) AI {
            constexpr int fr = 36 * C + I, fm = fr - RD;
            if constexpr (fr < 648 + RD) {
            constexpr int em = fm - 324, jm = em >= 0 ? HT.st[em] : 0, cm = em >= 0 ? em - HT.fi[jm] : 0;
            constexpr bool z0 = cm < 8 ? jm == 0 : jm == 10;
            constexpr int gm = fm < 73 ? 0 : fm < 325 ? 1 : fm < 325 + 4 * NE ? 2 : 0;
            constexpr int gh = gm == 1 ? (fm - 73) / 18 : gm == 2 ? 14 + (fm - 325) / NE : 0, go = gm == 1 ? (fm - 73) % 18 : gm == 2 ? (fm - 325) % NE : 0, gt = gh >> 1, gi = gt < 8 ? (gt & 3) : 0;
            constexpr bool formA = gm == 2 && ((ACT_E[go][0] >> 3) == 3 || (ACT_E[go][0] >> 3) == 5);
            auto act = [&](auto K) AI {
                constexpr int op = gm == 1 ? ACT_D[go][K] : (gm == 2 && K < 2) ? ACT_E[go][K & 1] : -1;
                if constexpr (op >= 0) {
                    constexpr int kd = op >> 3, e = op & 7;
                    const float x = acc[gi][8 * (gh & 1) + e];
                    if constexpr (kd == 0) t[e] = x * x;
                    if constexpr (kd == 1) t[e] = __builtin_fmaf(t[e], kp[1], kp[0]);
                    if constexpr (kd == 2 || kd == 6) t[e] = t[e] * x;
                    if constexpr (kd == 3) t[e] = __builtin_amdgcn_exp2f(t[e]);
                    if constexpr (kd == 4) t[e] = __builtin_fmaf(t[e], kp[2], kp[2]);
                    if constexpr (kd == 5) t[e] = __builtin_amdgcn_rcpf(t[e]);
                    if constexpr (kd < 7) OPAQ(t[e]);
                    if constexpr (kd == 7) { hp[e] = pk2(t[2 * e], t[2 * e + 1]); OPAQ(hp[e]); }
                    if constexpr (kd == 8) { static_assert(s_pos(gh) == fm, "store slot"); bs16<XA>(hp, wr, hso, (gt < 8 ? gt * 1024 : (kq[0] - kb[0]) * 1024) + (gh & 1) * 512); }
                    SB();
                }
            };
            using I0 = std::integral_constant<int, 0>; using I1 = std::integral_constant<int, 1>; using I2 = std::integral_constant<int, 2>; using I3 = std::integral_constant<int, 3>;
            auto mf1 = [&]() AI { ec[cm][1] = mfma16(A[(fm + RD + 1) % (RD + 1)], asb(H[jm % HD][1]), z0 ? f32x4{0, 0, 0, 0} : ec[cm][1]); SB(); };
            if constexpr (em >= 0) ec[cm][0] = mfma16(A[fm % (RD + 1)], asb(H[jm % HD][0]), z0 ? f32x4{0, 0, 0, 0} : ec[cm][0]);
            if constexpr (fm >= 0 && fm < 324) {
                constexpr int u = fm / 18, o = fm % 18, ks = u < 16 ? 9 * (u & 3) + (o >> 1) : 18 * (u & 1) + o, ai = u < 16 ? ((u >> 2) & 1) * 2 + (o & 1) : 0;
                f32x16 z = {};
                acc[ai] = __builtin_amdgcn_mfma_f32_32x32x16_bf16(A[fm % (RD + 1)], asb(X[ks]), ks == 0 ? z : acc[ai], 0, 0, 0);
            }
            SB();
            if constexpr (formA) { act(I0{}); act(I1{}); mf1(); }
            s16x4 t0, t1;
            unsigned a = 0, a2 = 0;
            constexpr int er = fr - 324, jr = er >= 0 ? HT.st[er] : 0, cr = er >= 0 ? er - HT.fi[jr] : 0;
            constexpr int bu = fr < 324 ? (fr % 18 == BP ? fr / 18 : -1) : (fr < 648 && !(jr & 1) && cr == BP) ? 18 + jr / 2 : -1;
            constexpr int nl = fr <= 648 ? 2 * (RD - 2) : 2 * (646 - fm) > 0 ? 2 * (646 - fm) : 0;
            if constexpr (bu >= 0) {
                static_assert(b_slot(bu) == fm, "barrier slot");
                constexpr int vw = f_lvl(bu) ? m_wait(LH[(f_lvl(bu) + 3) & 3], bu) : 63;
                if constexpr (vw < 63) waitvm<vw>();
                asm volatile("s_barrier" ::: "memory");
                SB();
            }
            if constexpr (fm >= 0 && fm < 647) { __builtin_amdgcn_s_waitcnt(wcnt(HT.vm[fm], nl)); SB(); }
            if constexpr (fr < 324) {
                constexpr int u = fr / 18, o = fr % 18, sl = u % NSL;
                if constexpr (u < 16) { a = ta[sl / 3][o & 1] + (sl % 3) * SLOT + (o >> 1) * 2048; a2 = a + 512; }
                else { a = ts + (sl - 3) * SLOT + o * 1024; a2 = a + 256; }
            } else if constexpr (fr < 648) {
                constexpr int sl = (18 + (jr >> 1)) % NSL;
                if constexpr (cr < 8) { a = am[sl / 3][cr & 7] + (sl % 3) * SLOT + (jr & 1) * 8192; a2 = a + 2048; }
                else { a = ax[cr & 3] + e_til(jr) * 4096; a2 = a + 1024; }
            }
            if constexpr (fr < 648) { t0 = __builtin_amdgcn_ds_read_tr16_b64_v4i16((LAS s16x4*)(uintptr_t)a); SB(); }
            if constexpr (em >= 0 && !formA) { act(I0{}); if constexpr (gm == 1) act(I1{}); mf1(); }
            if constexpr (fr < 648) {
                t1 = __builtin_amdgcn_ds_read_tr16_b64_v4i16((LAS s16x4*)(uintptr_t)a2);
                A[fr % (RD + 1)] = __builtin_bit_cast(bf16x8, __builtin_shufflevector(t0, t1, 0, 1, 2, 3, 4, 5, 6, 7));
                SB();
            }
            if constexpr (bu == 3) { kp = ldsr<f32x4>(kpa); SB(); }
            if constexpr (em < 0) { act(I0{}); act(I1{}); }
            if constexpr (em >= 0 && gm == 2 && !formA) act(I1{});
            act(I2{}); act(I3{});
            if constexpr (fm >= 299 && fm < 315 && (fm & 1)) { constexpr int c = (fm - 299) >> 1; am[0][c] = amb ^ (c << 5); OPAQ(am[0][c]); }
            if constexpr (fm >= 330 && fm < 346 && !(fm & 1)) { constexpr int c = (fm - 330) >> 1; am[1][c] = am[0][c] + 3 * SLOT; OPAQ(am[1][c]); }
            if constexpr (fm >= 390 && fm < 398 && !(fm & 1)) { constexpr int c = (fm - 390) >> 1; ax[c] = axb ^ (c << 5); OPAQ(ax[c]); }
            if constexpr (fm >= 0 && x_at(fm) >= 0) X[x_at(fm)] = bl16<XA>(wr, l16, O_XB + ((4 * rt + wm) * 36 + x_at(fm)) * 1024);
            constexpr int l0 = fm >= 0 ? HT.at[0][fm] : -1, l1 = fm >= 0 ? HT.at[1][fm] : -1;
            if constexpr (l0 >= 0) ldH(l0 >> 1, l0 & 1);
            if constexpr (l1 >= 0) ldH(l1 >> 1, l1 & 1);
            SB();
            }
        }); });
        __builtin_amdgcn_s_setprio(0);
        STAMP(2, 2);
#pragma unroll
        for (int c = 0; c < 12; ++c) {
            const u32x4 o = {pk2(ec[c][0][0], ec[c][0][1]), pk2(ec[c][0][2], ec[c][0][3]), pk2(ec[c][1][0], ec[c][1][1]), pk2(ec[c][1][2], ec[c][1][3])};
            if (c < 8) bs16<XA>(o, wr, l16, O_PB + (((4 * rt + wm) * 32 + 8 * nt + c) * 4 + kap) * 1024);
            else bs16<XA>(o, wr, l16, O_PX + (((4 * rt + wm) * 4 + c - 8) * 16 + fc) * 1024);
        }
        waitvm<0>();
    } else {
#pragma unroll
        for (int i = 0; i < 5; ++i) dmaD(4, i);
        poll(wr, F_C + rt * 128 + (wm * 4 + (lane & 3)) * 4, 1u);
        STAMP(2, 0);
        asm volatile("s_barrier" ::: "memory");
        f32x4 sqv[5];
        {
            const char* sb = ws + O_SQ + (4 * rt + wm) * 4608;
#pragma unroll
            for (int i = 0; i < 5; ++i) asm volatile("global_load_dwordx4 %0, %1, %2 offset:%3 sc1" : "=v"(sqv[i]) : "v"(i < 4 ? l16 : (l16 & 511) + 1024), "s"(sb), "i"(i < 4 ? i * 1024 : 3072) : "memory");
        }
        const int drow = 4 * wm + lq;
        const unsigned dvo = drow * 1152 + ((lj ^ (2 * (drow & 7))) << 4);
        const char* w4m = w4 + nt * 256;
        auto ksE = [&](int j) AI { return e_til(j) < 8 ? kb[e_mem(j)] + e_til(j) : kq[e_mem(j)]; };
        auto dmaE = [&](int u, int i) AI { const int j = 2 * (u - 18) + (i >> 1);
            dma16(lb + (u % NSL) * SLOT + (j & 1) * 8192 + (wm + 4 * (i & 1)) * 1024, w4m + (size_t)(ksE(j) * 32 + 16 * (i & 1)) * 1152, dvo); };
        const int xrow = lane >> 3;
        const unsigned xvo = xrow * 1152 + (((lane & 7) ^ (2 * ((xrow >> 1) & 3))) << 4);
        auto dmaX = [&](int t) AI { dma16(lb + L_EX + t * 4096 + wm * 1024, w4 + 1024 + (size_t)((t < 8 ? kb[3] + t : kq[3]) * 32 + wm * 8) * 1152, xvo); };
        const unsigned fdo = F_D + rt * 128 + kap * 16;
        unsigned pv = 0, pfd = 0;
        const unsigned pfo = (8 * rt + 2 * wm + ((lane >> 3) & 1)) * 144 * 1024 + (lane & 7) * 128;
        TDECL; TS(0);
        sfor<0, 36>([&](auto U) AI {
            constexpr int u = U;
            if constexpr (u >= 3) waitvm<g_wait(u)>();
            if constexpr (u == 3) {
#pragma unroll
                for (int i = 0; i < 5; ++i) OPAQ(sqv[i]);
                f32x4 sq = (sqv[0] + sqv[1]) + (sqv[2] + sqv[3]) + 0.5f * sqv[4];
                float t = 0;
#pragma unroll
                for (int r = 0; r < 4; ++r) {
                    float a = sq[r] + dppf<0x128>(sq[r]), b = a;
                    swap16f(a, b); a += b; b = a; swap32f(a, b); a += b;
                    if (((lane >> 3) & 3) == r) t = a;
                }
                const float sg = sqrtf(t * RD576 + EPS), rstd = 1.0f / sg;
                if (lane < 32) ldsw<f32x4>(lb + L_ST + wm * 512 + ((lane & 7) * 4 + (lane >> 3)) * 16, f32x4{GC0 * rstd, GC1 * rstd * rstd * rstd, sg, 0.f});
            }
            if constexpr (q_lvl(u) != 0) {
                OPAQ(pv);
                TS(72 + q_lvl(u));
                if (__builtin_amdgcn_ballot_w64(pv < (unsigned)q_lvl(u)) != 0) poll(wr, fdo + (lane & 3) * 4, q_lvl(u));
            }
            TS(2 * u + 1);
            bar();
            TS(2 * u + 2);
            if constexpr (f_lvl(u) != 0) { if (w == 4 && lane == 0) bs4<16>(f_lvl(u), wr, 0, fdo + nt * 4); }
            if constexpr (g_pre(u)) asm volatile("global_load_dword %0, %1, %2 sc1" : "=v"(pv) : "v"((lane & 3) * 4), "s"(ws + fdo) : "memory");
#pragma unroll
            for (int i = 0; i < g_nd(u); ++i) { if (u + 5 < 18) dmaD(u + 5, i); else dmaE(u + 5, i); SB(); }
            if constexpr (g_nx(u)) dmaX(u - 4);
            if constexpr (g_pf(u) >= 0) asm volatile("global_load_dword %0, %1, %2 sc1" : "+v"(pfd) : "v"(pfo), "s"(ws + O_HB + (size_t)(g_pf(u) < 8 ? kb[0] + g_pf(u) : kq[0]) * 1024) : "memory");
            SB();
        });
        waitvm<0>();
        OPAQ(pfd);
        TDUMP(2, 32);
    }
    bar();
    if (w == 0 && lane == 0) bs4<16>(1u, wr, 0, F_E + rt * 128 + fc * 4);
    STAMP(2, 3);
    {
        const rsrc_t orr = mkrs(out, 2048 * 1152);
        u32x4 P[4], R[2], Q[16], Re[2];
#pragma unroll
        for (int i = 0; i < 2; ++i) R[i] = bl16<16>(wr, l16, O_R + ((rb * 36 + 8 * nt + w) * 2 + i) * 1024);
        if (w == 7) {
#pragma unroll
            for (int i = 0; i < 2; ++i) Re[i] = bl16<16>(wr, l16, O_R + ((rb * 36 + 32 + nt) * 2 + i) * 1024);
        }
        poll(wr, F_E + rt * 128 + lj * 4, (w == 7 || (lj & 3) == nt) ? 1u : 0u);
        STAMP(2, 4);
#pragma unroll
        for (int i = 0; i < 4; ++i) P[i] = bl16<XA>(wr, l16, O_PB + ((rb * 32 + 8 * nt + w) * 4 + i) * 1024);
        if (w == 7) {
#pragma unroll
            for (int i = 0; i < 16; ++i) Q[i] = bl16<XA>(wr, l16, O_PX + ((rb * 4 + nt) * 16 + i) * 1024);
        }
        auto fin = [&](u32x4* p, int n, u32x4* r, int tile) AI {
#pragma unroll
            for (int mt = 0; mt < 2; ++mt) {
                float a[4] = {0, 0, 0, 0};
                for (int i = 0; i < n; ++i) { a[0] += bflo(p[i][2 * mt]); a[1] += bfhi(p[i][2 * mt]); a[2] += bflo(p[i][2 * mt + 1]); a[3] += bfhi(p[i][2 * mt + 1]); }
                bs8<0>(u32x2{pk2(a[0] + __uint_as_float(r[mt][0]), a[1] + __uint_as_float(r[mt][1])), pk2(a[2] + __uint_as_float(r[mt][2]), a[3] + __uint_as_float(r[mt][3]))},
                       orr, lj * 1152 + lq * 8, (32 * rb + 16 * mt) * 1152 + tile * 32);
            }
        };
        fin(P, 4, R, 8 * nt + w);
        if (w == 7) fin(Q, 16, Re, 32 + nt);
    }
    STAMP(2, 6);
    STAMP(2, 7);
    asm volatile(".Lcode_hi:");
}

__global__ __launch_bounds__(64) void k0(char* __restrict__ ws) { if (threadIdx.x == 0) ((u64*)(ws + O_DBG))[3 * 256 * 64] = __builtin_amdgcn_s_memrealtime(); }
__global__ __launch_bounds__(256) void k3(char* __restrict__ ws) {
    const u64 t3 = __builtin_amdgcn_s_memrealtime();
    __shared__ unsigned long long red[8];
    const u64* S = (const u64*)(ws + O_DBG);
    const int b = threadIdx.x;
    if (b < 8) red[b] = (b == 0 || b == 2) ? ~0ull : 0ull;
    __syncthreads();
    auto mn = [&](int k, int i, int nw) { u64 m = ~0ull; for (int w = 0; w < nw; ++w) { u64 t = S[((k * 256 + b) * 8 + w) * 8 + i]; m = t < m ? t : m; } return m; };
    auto mxx = [&](int k, int i, int nw) { u64 m = 0; for (int w = 0; w < nw; ++w) { u64 t = S[((k * 256 + b) * 8 + w) * 8 + i]; m = t > m ? t : m; } return m; };
    atomicMin(&red[0], mn(0, 0, 8)); atomicMax(&red[1], mxx(0, 7, 8)); atomicMin(&red[2], mn(1, 0, 8)); atomicMax(&red[3], mxx(2, 0, 4)); atomicMax(&red[4], mxx(2, 2, 4)); atomicMax(&red[5], mxx(2, 7, 8)); atomicMax(&red[6], mxx(1, 3, 8));
    __syncthreads();
    if (b == 0) {
        u64 r[8];
        for (int q = 0; q < 7; ++q) r[q] = red[q];
        r[7] = S[3 * 256 * 64];
        long long x = SIDEQ;
        if (x < 0) x = 0; if (x > 20000) x = 20000;
        const u64 tgt = t3 + 4000 + x;
        for (int i = 0; i < (1 << 20); ++i) { if (__builtin_amdgcn_s_memrealtime() >= tgt) break; }
    }
}

extern "C" void kernel_launch(void* const* d_in, const int* in_sizes, int n_in, void* d_out, int out_size, void* d_ws, size_t ws_size, hipStream_t stream) {
    char* ws = (char*)d_ws;
    (void)hipFuncSetAttribute((const void*)k1, hipFuncAttributeMaxDynamicSharedMemorySize, K1_LDS);
    (void)hipFuncSetAttribute((const void*)k2, hipFuncAttributeMaxDynamicSharedMemorySize, K2_LDS);
    k1<<<256, 512, K1_LDS, stream>>>((const char*)d_in[0], (const char*)d_in[1], (const char*)d_in[2], (const char*)d_in[3], (const char*)d_in[4], (const u16*)d_in[5],
                                     (const char*)d_in[7], (const char*)d_in[8], (const char*)d_in[9], (const char*)d_in[10], ws);
    k2<<<256, 512, K2_LDS, stream>>>((const char*)d_in[9], (const char*)d_in[10], ws, (char*)d_out);
    k3<<<1, 256, 0, stream>>>(ws);
}
```

```cpp
#include <hip/hip_runtime.h>
#include <stdint.h>
#include <utility>
#define LAS __attribute__((address_space(3)))
typedef short s16x4 __attribute__((ext_vector_type(4)));
typedef __bf16 bf16x8 __attribute__((ext_vector_type(8)));
typedef __bf16 bf16x2 __attribute__((ext_vector_type(2)));
typedef float f32x16 __attribute__((ext_vector_type(16)));
typedef float f32x4 __attribute__((ext_vector_type(4)));
typedef float f32x2 __attribute__((ext_vector_type(2)));
typedef unsigned u32x4 __attribute__((ext_vector_type(4)));
typedef unsigned u32x2 __attribute__((ext_vector_type(2)));
typedef unsigned long long u64;
typedef unsigned short u16;
typedef __amdgpu_buffer_rsrc_t rsrc_t;
#define DEVI __device__ __forceinline__
#define AI __attribute__((always_inline))
#define SB() __builtin_amdgcn_sched_barrier(0)
#define OPAQ(x) asm volatile("" : "+v"(x))
#define NOPK(x) asm("" : "+v"(x))

DEVI unsigned pk2(float a, float b) { f32x2 p = {a, b}; return __builtin_bit_cast(unsigned, __builtin_convertvector(p, bf16x2)); }
DEVI float bflo(unsigned x) { return __uint_as_float(x << 16); }
DEVI float bfhi(unsigned x) { return __uint_as_float(x & 0xffff0000u); }
DEVI bf16x8 asb(u32x4 x) { return __builtin_bit_cast(bf16x8, x); }
constexpr float GC0 = -2.0f * 0.7978845608028654f * 1.4426950408889634f, GC1 = GC0 * 0.044715f;
DEVI float gelu(float x) {
    float e = __builtin_amdgcn_exp2f(x * __builtin_fmaf(x * x, GC1, GC0));
    return x * __builtin_amdgcn_rcpf(1.0f + e);
}
#define P_DNT ""
DEVI void dma16(unsigned ldsaddr, const void* sbase, unsigned voff) {
    asm volatile("s_mov_b32 m0, %0\n\ts_nop 0\n\tglobal_load_lds_dwordx4 %1, %2" P_DNT :: "s"(ldsaddr), "v"(voff), "s"(sbase) : "memory");
}
DEVI void dma16h(unsigned ldsaddr, const void* sbase, unsigned voff) {
    asm volatile("s_mov_b32 m0, %0\n\ts_mov_b32 exec_hi, 0\n\tglobal_load_lds_dwordx4 %1, %2" P_DNT "\n\ts_mov_b32 exec_hi, -1" :: "s"(ldsaddr), "v"(voff), "s"(sbase) : "memory");
}
template <int N> DEVI void waitvm() { asm volatile("s_waitcnt vmcnt(%0)" :: "i"(N) : "memory"); }
DEVI void bar() { asm volatile("s_waitcnt lgkmcnt(0)\n\ts_barrier" ::: "memory"); }
DEVI bf16x8 trfrag(unsigned a0, unsigned a1) {
    s16x4 a = __builtin_amdgcn_ds_read_tr16_b64_v4i16((LAS s16x4*)(uintptr_t)a0);
    s16x4 b = __builtin_amdgcn_ds_read_tr16_b64_v4i16((LAS s16x4*)(uintptr_t)a1);
    return __builtin_bit_cast(bf16x8, __builtin_shufflevector(a, b, 0, 1, 2, 3, 4, 5, 6, 7));
}
template <int B, class F, int... I> DEVI void sfor_impl(F&& f, std::integer_sequence<int, I...>) { (f(std::integral_constant<int, B + I>{}), ...); }
template <int B, int E, class F> DEVI void sfor(F&& f) { sfor_impl<B>(f, std::make_integer_sequence<int, E - B>{}); }
template <class T> DEVI T ldsr(unsigned a) { return *(LAS T*)(uintptr_t)a; }
template <class T> DEVI void ldsw(unsigned a, T v) { *(LAS T*)(uintptr_t)a = v; }
DEVI rsrc_t mkrs(const void* p, unsigned bytes) { return __builtin_amdgcn_make_buffer_rsrc((void*)p, 0, bytes, 0x00020000); }
template <int AUX> DEVI u32x4 bl16(rsrc_t r, unsigned vo, unsigned so) { return __builtin_amdgcn_raw_buffer_load_b128(r, vo, so, AUX); }
template <int AUX> DEVI u32x2 bl8(rsrc_t r, unsigned vo, unsigned so) { return __builtin_amdgcn_raw_buffer_load_b64(r, vo, so, AUX); }
template <int AUX> DEVI unsigned bl4(rsrc_t r, unsigned vo, unsigned so) { return __builtin_amdgcn_raw_buffer_load_b32(r, vo, so, AUX); }
template <int AUX> DEVI void bs16(u32x4 v, rsrc_t r, unsigned vo, unsigned so) { __builtin_amdgcn_raw_buffer_store_b128(v, r, vo + so, 0, AUX); }
template <int AUX> DEVI void bs8(u32x2 v, rsrc_t r, unsigned vo, unsigned so) { __builtin_amdgcn_raw_buffer_store_b64(v, r, vo, so, AUX); }
template <int AUX> DEVI void bs4(unsigned v, rsrc_t r, unsigned vo, unsigned so) { __builtin_amdgcn_raw_buffer_store_b32(v, r, vo, so, AUX); }
DEVI void swap16(unsigned& a, unsigned& b) { auto r = __builtin_amdgcn_permlane16_swap(a, b, false, false); a = r[0]; b = r[1]; }
DEVI void swap32(unsigned& a, unsigned& b) { auto r = __builtin_amdgcn_permlane32_swap(a, b, false, false); a = r[0]; b = r[1]; }
DEVI void swap16f(float& a, float& b) { unsigned x = __float_as_uint(a), y = __float_as_uint(b); swap16(x, y); a = __uint_as_float(x); b = __uint_as_float(y); }
DEVI void swap32f(float& a, float& b) { unsigned x = __float_as_uint(a), y = __float_as_uint(b); swap32(x, y); a = __uint_as_float(x); b = __uint_as_float(y); }
template <int CTRL> DEVI float dppf(float x) { return __uint_as_float(__builtin_amdgcn_update_dpp(0u, __float_as_uint(x), CTRL, 0xf, 0xf, false)); }
DEVI float rowsum16(float x) { x += dppf<0x128>(x); x += dppf<0x124>(x); x += dppf<0x122>(x); x += dppf<0x121>(x); return x; }
DEVI f32x4 mfma16(bf16x8 a, bf16x8 b, f32x4 c) { return __builtin_amdgcn_mfma_f32_16x16x32_bf16(a, b, c, 0, 0, 0); }

constexpr unsigned CLSZ = 64 * 36 * 1024;
constexpr unsigned GTS = 2056 * 32;
constexpr unsigned O_FLAG = 0, O_YB = 196608, O_XT = O_YB + CLSZ, O_XS = O_XT + CLSZ, O_W1T = O_XS + 16384, O_W2T = O_W1T + 663552,
                   O_CSP = O_W2T + 663552, O_W2M = O_CSP + 41472, O_GB = O_W2M + 2304, O_HT = O_GB + 36 * GTS, O_HS = O_HT + CLSZ,
                   O_XB = O_HS + 65536, O_SQ = O_XB + CLSZ, O_R = O_SQ + 36 * 2048 * 4, O_HB = O_R + 2 * CLSZ, O_PB = O_HB + 128 * 144 * 1024,
                   O_END = O_PB + 64 * 36 * 4 * 1024, O_DBG = O_END;
constexpr float EPS = 1e-5f, RD576 = 1.0f / 576.0f;
#define XA 16
#define TDECL
#define BDECL
#define BT(i) do {} while (0)
#define CT(i) do {} while (0)
#define CDECL
#define CDUMP() do {} while (0)
#define BDUMP(kid) do {} while (0)
#define TS(i) do {} while (0)
#define TDUMP(kid, n) do {} while (0)
#define STAMP(kid, i) do {} while (0)
#define CSTAMP(kid, i) do {} while (0)

#define P_CPF 1
#define P_KPF 3
#define P_YA 0
#define P_SP {-4, -3, -2, -1, 2, 26, 50, 74}
#define P_KS {12, 58, 104}
#define P_VS {36, 82, 128}
#define P_PA 24
#define P_PC 8
#define P_PB 8
constexpr int CF[5] = {0, 60, 104, 132, 144}, KS[3] = P_KS, VS[3] = P_VS;
constexpr int c_st(int j) { return j < 60 ? 0 : j < 104 ? 1 : j < 132 ? 2 : 3; }
constexpr int c_bs(int g) { return VS[g] + 3; }
constexpr int c_ps(int g) { return g < 3 ? CF[g] + 17 : 143; }
struct CTab { int a[144], b[144], y[144], wt[145], bw[3]; bool ok;
    constexpr CTab() : a{}, b{}, y{}, wt{}, bw{}, ok(true) {
        int ia[64] = {}, ib[4][8] = {}, iy[3] = {}, n = 8, na = P_PA + P_PB + P_PC, done = -2;
        for (int i = 0; i < 64; ++i) ia[i] = i < na ? -1 : 9999;
        for (int g = 0; g < 4; ++g) for (int i = 0; i < 8; ++i) ib[g][i] = g ? 9999 : i;
        for (int j = 0; j < 144; ++j) {
            const int g = c_st(j), o = j - CF[g], last = CF[g + 1] - j <= 4, c1 = o >> 3, c0 = last ? (o & 3) : (o & 7) >> 1, bi = last ? 2 * c0 : (o & 7);
            int need = -1;
            if (g == 0) need = ia[(c1 * 4 + c0) * 2 + 1];
            if (ib[g][bi] > need) need = ib[g][bi];
            for (int h = 0; h < 3; ++h) if (j == c_bs(h) + 17 && h < 2 && iy[h] > need) need = iy[h];
            if (need >= n) ok = false;
            wt[j] = need <= done ? 15 : n - 1 - need > 14 ? 10 : n - 1 - need;
            if (wt[j] < 15) done = n - 1 - wt[j];
            a[j] = b[j] = y[j] = -1;
            int lw = -1;
            for (int h = 0; h < 3; ++h) {
                if (j == VS[h] + 2 || j == VS[h] + 3) { n += 2; lw = n; }
                if (h < 2 && j > c_bs(h) && j <= c_bs(h) + 8) { b[j] = j - c_bs(h) - 1; ib[h + 1][b[j]] = n++; }
                if (h < 2 && j > c_bs(h) + 8 && j <= c_bs(h) + 12) { y[j] = j - c_bs(h) - 9; n += 2; iy[h] = n - 1; }
                if (j == c_ps(h)) n += 2;
                if (c_ps(h) >= c_bs(h) || KS[h] + 22 > VS[h] || (h && (KS[h] <= VS[h - 1] + 3 || c_bs(h - 1) + 21 >= c_bs(h))) || c_bs(h) < CF[h]) ok = false;
            }
            if (na < 64 && b[j] < 0 && lw < 0) { a[j] = na; ia[na++] = n++; }
            for (int h = 0; h < 3; ++h) if (j == c_bs(h)) { bw[h] = n - lw; done = lw - 1; if (h == 2) { for (int i = 0; i < 8; ++i) ib[3][i] = n++; n += 8; } }
        }
    } };
constexpr CTab CTB{};
static_assert(CTB.ok, "conv schedule");
#define P_D1 4
#define P_B01 1
#define P_PRX {20, 26, 26, 6}
#define P_WA 16
#define P_XA 16
#define P_D2 0
#define P_KVPF 1
#define P_TS 0
#define P_EPF 1
#define P_NT 0
#define NT(b) ((P_NT & (b)) ? 2 : 0)
#define P_PFP 3
#define P_CW4 110
#define P_CL 20
#define P_SEC 0
#define P_KVW 4

constexpr int K1_CS = 4160, K1_KC = 0, K1_UR = 8 * K1_CS, K1_P = K1_UR + 18432, K1_PT = 1152, K1_LDS = K1_P + 32 * K1_PT;
__global__ __launch_bounds__(512, 2) void k1(const char* __restrict__ xq, const char* __restrict__ kk, const char* __restrict__ vv, const char* __restrict__ shk,
                                             const char* __restrict__ ssk, const u16* __restrict__ ssd, const char* __restrict__ w1, const char* __restrict__ w2,
                                             const char* __restrict__ w3, const char* __restrict__ w4, char* __restrict__ ws) {
    extern __shared__ __attribute__((aligned(256))) char smem[];
    CDECL; CT(0);
    const unsigned lb = (unsigned)(uintptr_t)(LAS char*)smem;
    const int wg = blockIdx.x, xcd = wg & 7, loc = wg >> 3, tid = threadIdx.x, lane = tid & 63;
    const int w = __builtin_amdgcn_readfirstlane(tid >> 6);
    unsigned pfc = 0;
    if (loc >= 24 && w == 7) {
        u64 pc; unsigned len;
        asm volatile("s_getpc_b64 %0\n.Lcode_lo:\n\ts_mov_b32 %1, .Lcode_hi-.Lcode_lo" : "=s"(pc), "=s"(len));
        pfc = bl4<NT(32)>(mkrs((const void*)(pc & ~127ull), len), lane * 128 + (loc - 24) * 8192, 0);
    }
    SB();
    const rsrc_t wr = mkrs(ws, O_END);
    const rsrc_t kr = mkrs(kk, 2048 * 1152), vr = mkrs(vv, 2048 * 1152);
    asm volatile("" :: "s"(ssk), "s"(shk), "s"(ssd), "s"(xq), "s"(w1), "s"(w2), "s"(w3), "s"(w4));
    STAMP(0, 0);
    CT(55);
    if (loc >= 18) {
        const int h = loc - 18, u = h * 8 + w;
        constexpr unsigned OOB = 0x80000000u;
        unsigned pf0 = 0, pf1 = 0, pw3 = 0, pw4 = 0;
        auto kvpf = [&]() AI {
            SB();
            const unsigned l = (h * 2 + (w & 1)) * 37 + lane, o = (lane < 37 && l < 1024) ? (512 * (w >> 1) + (l >> 1)) * 1152 + ((xcd * 144) & ~127) + (l & 1) * 128 : OOB;
            pf0 = bl4<NT(8)>(kr, o, 0); pf1 = bl4<NT(8)>(vr, o, 0);
            SB();
        };
        auto pfw = [&](int p) AI {
            SB();
            if (p) CT(4);
            if (P_KPF) {
                const unsigned l = u * 47 + lane, row = l / 9;
                if ((P_KPF & 1) && p == (P_PFP & 1)) { if (P_D2) asm volatile("s_sleep %0" :: "i"(P_D2) : "memory");
                    pw3 = bl4<0>(mkrs(w3, 576 * 9216), lane < 47 ? row * 9216 + (l - row * 9) * 128 : OOB, (xcd & 3) * 2304 + (xcd >> 2) * 1152); }
                constexpr int HL = (5184 - 144 * P_CL + 111) / 112;
                if (HL > 0 && (P_KPF & 2) && p == (P_PFP >> 1)) pw4 = bl4<0>(mkrs(w4 + ((xcd & 3) * 2 + (xcd >> 2)) * 663552, 663552), lane < HL ? (144 * P_CL + u * HL + lane) * 128 : OOB, 0);
            }
            SB();
        };
        auto mid = [&]() AI {
            SB();
            CT(2);
            if (w >= P_KVW) { if (P_D1) for (int i = P_KVW; i <= w; i += 2) asm volatile("s_sleep %0" :: "i"(P_D1) : "memory"); kvpf(); }
            CT(3);
            pfw(0);
        };
        CT(56);
        if (w < P_KVW) kvpf();
        CT(1);
        if (h < 8) {
            constexpr int RS = 1168;
            const int rb = 32 * (xcd >> 2) + 4 * h + (xcd & 3);
            const rsrc_t xr = mkrs(xq, 2048 * 1152);
            u32x4 t[5];
#pragma unroll
            for (int m = 0; m < 5; ++m) t[m] = bl16<NT(2)>(xr, (m < 4 || w < 4) ? tid * 16 : OOB, rb * 36864 + m * 8192);
            mid();
#pragma unroll
            for (int m = 0; m < 5; ++m) { const unsigned o = m * 8192 + tid * 16; if (m < 4 || w < 4) ldsw<u32x4>(lb + o + o / 1152 * (RS - 1152), t[m]); }
            pfw(1);
            bar();
            const unsigned ta = lb + (lane & 15) * RS + (lane >> 4) * 8 + w * 32, sa = lb + (4 * w + (lane >> 4)) * RS + (lane & 15) * 8;
            u32x2 a[9];
#pragma unroll
            for (int m = 0; m < 9; ++m) a[m] = ldsr<u32x2>(sa + m * 128);
#pragma unroll
            for (int m = 0; m < 5; ++m) if (m < 4 || w < 4) {
                const u32x2 p = ldsr<u32x2>(ta + m * 256), q = ldsr<u32x2>(ta + m * 256 + 16 * RS);
                bs16<P_XA>(u32x4{p[0], p[1], q[0], q[1]}, wr, tid * 16, O_XT + rb * 36864 + m * 8192);
            }
            float su = 0, qa = 0;
#pragma unroll
            for (int m = 0; m < 9; ++m) su += (bflo(a[m][0]) + bfhi(a[m][0])) + (bflo(a[m][1]) + bfhi(a[m][1]));
            const float ma = rowsum16(su) * RD576;
#pragma unroll
            for (int m = 0; m < 9; ++m)
#pragma unroll
                for (int i = 0; i < 2; ++i) { float d; d = bflo(a[m][i]) - ma; qa += d * d; d = bfhi(a[m][i]) - ma; qa += d * d; }
            const float ra = 1.0f / sqrtf(rowsum16(qa) * RD576 + EPS);
            if ((lane & 15) == 0) bs8<0>(u32x2{__float_as_uint(ra), __float_as_uint(-ma * ra)}, wr, (32 * rb + 4 * w + (lane >> 4)) * 8, O_XS);
        } else {
            const int v = w * 6 + h - 8, g = 41 * xcd + v, mu = 5 * xcd + v - 41;
            if (v < 41 && g < 324) {
                const int mat = g >= 162, r = g - 162 * mat, ks = r / 9, cg = r - 9 * ks;
                const rsrc_t mr = mkrs(mat ? w2 : w1, 576 * 1152);
                u32x4 t[4];
#pragma unroll
                for (int i = 0; i < 4; ++i) t[i] = bl16<NT(4)>(mr, (lane >> 3) * 1152 + (lane & 7) * 16, (32 * ks + 8 * i) * 1152 + cg * 128);
                mid();
                const unsigned lw = lb + w * 4096;
#pragma unroll
                for (int i = 0; i < 4; ++i) ldsw<u32x4>(lw + i * 1024 + lane * 16, t[i]);
                pfw(1);
                const unsigned ta = lw + ((mat ? 8 : 4) * (lane >> 4) + ((lane & 15) >> 2)) * 128 + (lane & 3) * 8, tb = ta + (mat ? 512 : 2048);
                const u32x4 ones = {0x3f803f80u, 0x3f803f80u, 0x3f803f80u, 0x3f803f80u};
#pragma unroll
                for (int tt = 0; tt < 4; ++tt) {
                    const bf16x8 f = trfrag(ta + tt * 32, tb + tt * 32);
                    bs16<P_WA>(__builtin_bit_cast(u32x4, f), wr, lane * 16, (mat ? O_W2T : O_W1T) + ((4 * cg + tt) * 18 + ks) * 1024);
                    if (!mat) {
                        const f32x4 cs = mfma16(f, asb(ones), f32x4{0, 0, 0, 0});
                        if ((lane & 15) == 0) bs16<0>(__builtin_bit_cast(u32x4, cs), wr, (lane >> 4) * 16, O_CSP + ((4 * cg + tt) * 18 + ks) * 64);
                    }
                }
            } else if (v >= 41 && v < 46 && mu < 36) {
                const rsrc_t mr = mkrs(w2, 576 * 1152);
                u32x4 t[18];
#pragma unroll
                for (int i = 0; i < 18; ++i) t[i] = bl16<NT(4)>(mr, (lane >> 3) * 1152 + (lane & 7) * 16, (16 * mu + 8 * (i & 1)) * 1152 + (i >> 1) * 128);
                mid();
                float s[2] = {0, 0};
#pragma unroll
                for (int i = 0; i < 18; ++i) {
                    if (i == 17) pfw(1);
                    s[i & 1] += ((bflo(t[i][0]) + bfhi(t[i][0])) + (bflo(t[i][1]) + bfhi(t[i][1]))) + ((bflo(t[i][2]) + bfhi(t[i][2])) + (bflo(t[i][3]) + bfhi(t[i][3])));
                }
#pragma unroll
                for (int i = 0; i < 2; ++i) { s[i] += dppf<0xB1>(s[i]); s[i] += dppf<0x4E>(s[i]); s[i] += dppf<0x141>(s[i]); }
                if ((lane & 7) < 2) {
                    const float m = ((lane & 1) ? s[1] : s[0]) * RD576;
                    const int p = (2 * mu + (lane & 1)) * 32 + (lane >> 3) * 2;
                    const unsigned hi = pk2(m, 0.f) & 0xffffu, lo = pk2(m - bflo(hi), 0.f) & 0xffffu;
                    *(u16*)(ws + O_W2M + p) = (u16)hi; *(u16*)(ws + O_W2M + 16 + p) = (u16)lo;
                }
            } else {
                if (v == 46) {
#pragma unroll
                    for (int i = 0; i < 18; ++i) bs16<0>(u32x4{0, 0, 0, 0}, wr, lane * 16, O_FLAG + (xcd * 18 + i) * 1024);
                }
                if (v == 47) {
                    const int l = 9 * xcd + (lane >> 3), l2 = 9 * xcd + 8;
                    bs16<0>(u32x4{0, 0, 0, 0}, wr, (l >> 1) * GTS + (l & 1) * (2052 * 32) + (lane & 7) * 16, O_GB);
                    if (lane < 8) bs16<0>(u32x4{0, 0, 0, 0}, wr, (l2 >> 1) * GTS + (l2 & 1) * (2052 * 32) + lane * 16, O_GB);
                }
                mid();
                asm volatile("" :: "v"(pf0), "v"(pf1));
                pfw(1);
            }
        }
        CT(5);
        asm volatile("" :: "v"(pf0), "v"(pf1));
        CT(6);
        asm volatile("" :: "v"(pw3), "v"(pw4), "v"(pfc));
        CT(7);
        CDUMP();
    } else {
        const int cw = 18 * xcd + loc;
        CT(56);
        const int kch = w >> 1;
        const rsrc_t sr = mkrs(ssk, 576 * 4096);
        u32x4 K0 = bl16<NT(1)>(sr, lane * 16, cw * 16384 + w * 2048), K1 = bl16<NT(1)>(sr, lane * 16 + 1024, cw * 16384 + w * 2048);
        SB();
        const unsigned dw = *(const unsigned*)(ssd + 4 * cw + (kch & 2)), knx = *(const unsigned*)(ssk + (4 * cw + kch) * 4096 + 2048);
        const u32x4 sa = *(const u32x4*)(shk + cw * 32), sb = *(const u32x4*)(shk + cw * 32 + 16);
        const u32x2 halo = bl8<NT(16)>(kr, lane < 16 ? (unsigned)((lane >> 2) * 512 + 64 * w - 1 - (lane & 3)) * 1152u + cw * 8 : 0x80000000u, 0);
        SB();
        u32x2 st[8];
        constexpr int SP[8] = P_SP;
        auto strip = [&](int i) AI { SB(); st[i] = bl8<NT(16)>((i & 1) ? vr : kr, tid * 1152 + cw * 8, (i >> 1) * 512 * 1152); SB(); };
        auto strips = [&](int s) AI {
#pragma unroll
            for (int i = 0; i < 8; ++i) if (SP[i] == s) strip(i); };
        CT(1);
        strips(-5);
        CT(2);
        if (lane < ((w & 1) ? 30 : 4)) ldsw<u32x4>(lb + ((w & 1) ? K1_UR + kch * 4608 + 4096 + lane * 16 : K1_KC + (w + (lane >> 1)) * K1_CS + (lane & 1) * 16), u32x4{0, 0, 0, 0});
        const int ch = w & 3, hf = w >> 2, li = lane & 15, q = lane >> 4;
        unsigned aa[4];
#pragma unroll
        for (int i = 0; i < 4; ++i) { const int r = (li + 1) & 1; aa[i] = lb + K1_KC + (ch * 2 + r) * K1_CS + (128 * hf + li + 25 - 8 * q - r) * 2 + i * 1024; OPAQ(aa[i]); }
        unsigned ua = lb + K1_UR + (511 - tid) * 2, c0a = lb + K1_KC + kch * 2 * K1_CS + 32 + (w & 1) * 2048 + lane * 16;
        unsigned ba = lb + K1_UR + ch * 4608 + (2056 - 1792 - 16 * li + 128 * hf - 8 * q) * 2;
        unsigned pw = lb + K1_P + (hf * 4 + ch) * 2 * K1_PT + q * 288 + li * 16;
        unsigned pr = lb + K1_P + (tid >> 8) * K1_PT + ((tid >> 3) & 3) * 288 + (((tid >> 5) & 7) * 2 + (tid & 1)) * 16 + ((tid >> 1) & 3) * 4;
        unsigned yo = (tid >> 5) * 36864 + (tid & 31) * 8;
        OPAQ(ua); OPAQ(c0a); OPAQ(ba); OPAQ(pw); OPAQ(pr); OPAQ(yo);
        const float dv = (kch & 1) ? bfhi(dw) : bflo(dw);
        float fw[4][4] = {{bflo(sa[0]), bfhi(sa[0]), bflo(sa[1]), bfhi(sa[1])}, {bflo(sa[2]), bfhi(sa[2]), bflo(sa[3]), bfhi(sa[3])},
                          {bflo(sb[0]), bfhi(sb[0]), bflo(sb[1]), bfhi(sb[1])}, {bflo(sb[2]), bfhi(sb[2]), bflo(sb[3]), bfhi(sb[3])}};
#pragma unroll
        for (int i = 0; i < 16; ++i) asm volatile("" : "+s"(fw[i >> 2][i & 3]));
        CT(3);
        OPAQ(K0);
        CT(4);
        strips(-4);
        if ((tid & 127) == 0) { K0[0] = (K0[0] & 0xffff0000u) | (pk2(bflo(K0[0]) + dv, 0.f) & 0xffffu); ldsw<u16>(c0a + K1_CS - 2, (u16)K0[0]); }
        ldsw<u32x4>(c0a, K0); ldsw<u32x4>(c0a + 1024, K1);
        {
            const unsigned n0 = __builtin_amdgcn_update_dpp(__builtin_amdgcn_readfirstlane(K1[0]), K0[0], 0x130, 0xf, 0xf, false), n1 = __builtin_amdgcn_update_dpp(knx, K1[0], 0x130, 0xf, 0xf, false);
            ldsw<u32x4>(c0a + K1_CS, u32x4{__builtin_amdgcn_alignbit(K0[1], K0[0], 16), __builtin_amdgcn_alignbit(K0[2], K0[1], 16), __builtin_amdgcn_alignbit(K0[3], K0[2], 16), __builtin_amdgcn_alignbit(n0, K0[3], 16)});
            ldsw<u32x4>(c0a + K1_CS + 1024, u32x4{__builtin_amdgcn_alignbit(K1[1], K1[0], 16), __builtin_amdgcn_alignbit(K1[2], K1[1], 16), __builtin_amdgcn_alignbit(K1[3], K1[2], 16), __builtin_amdgcn_alignbit(n1, K1[3], 16)});
        }
        strips(-3);
        CT(5);
        bar();
        CT(6);
        u32x2 A[64];
        auto aread = [&](int i) AI {
            const int o = ((i >> 3) & 1) * 128 + ((i >> 1) & 3) * 16 + (i & 1) * 2;
            asm volatile("ds_read2_b32 %0, %1 offset0:%2 offset1:%3" : "=v"(A[i]) : "v"(aa[i >> 4]), "i"(o), "i"(o + 1) : "memory");
        };
#pragma unroll
        for (int i = 0; i < P_PA; ++i) aread(i);
        strips(-2);
        float fs[4]; unsigned kx, pp[2];
        unsigned ho;
        auto fir = [&](auto G, auto OP) AI {
            constexpr int g = G, op = OP, d = op / 11, r = op % 11, i = (r + 1) / 3, k = (r + 1) % 3, n = 4 * g + i;
            if constexpr (r == 0) kx = st[2 * g][d];
            if constexpr (k == 0) { kx = __builtin_amdgcn_update_dpp(ho, kx, 0x138, 0xf, 0xf, false); OPAQ(kx); }
            else {
                if constexpr (k == 2 && i < 3) { if constexpr (n) ho = __builtin_amdgcn_mov_dpp(halo[d], 0x100 + n, 0xf, 0xf, true); else ho = halo[d]; OPAQ(ho); }
                float& f = fs[2 * d + k - 1]; const float x = k == 1 ? bflo(kx) : bfhi(kx); f = i ? __builtin_fmaf(x, fw[2 * d + k - 1][i], f) : x * fw[2 * d + k - 1][0]; OPAQ(f);
            }
        };
        auto kv = [&](int g, int op) AI {
            const unsigned u = ua + (3 - g) * 1024;
            if (op < 2) { const unsigned v = st[2 * g + 1][op]; float t = fs[2 * op] * bflo(v); OPAQ(t); pp[op] = pk2(t, fs[2 * op + 1] * bfhi(v)); OPAQ(pp[op]); }
            else { ldsw<u16>(u + (op - 2) * 9216, (u16)pp[op - 2]); ldsw<u16>(u + (op - 2) * 9216 + 4608, (u16)(pp[op - 2] >> 16)); }
        };
        CT(7);
        sfor<0, 22>([&](auto I) AI { fir(std::integral_constant<int, 0>{}, I); if constexpr (I < P_PC) aread(P_PA + I); });
        CT(8);
#pragma unroll
        for (int i = 0; i < 4; ++i) kv(0, i);
        SB();
#pragma unroll
        for (int i = P_PA + P_PC; i < P_PA + P_PC + P_PB; ++i) aread(i);
        CT(9);
        strips(-1);
        CT(10);
        f32x4 acc[8];
        unsigned pw4 = 0;
        float yr[8];
        u32x4 B[2][8];
        auto bread = [&](int g, int i) AI { asm volatile("ds_read_b128 %0, %1 offset:%2" : "=v"(B[g & 1][i]) : "v"(ba), "i"((7 - 2 * g - (i & 1)) * 512 + (i >> 1) * 64) : "memory"); };
        auto yread = [&](int g, int c) AI { asm volatile("ds_read_b32 %0, %1 offset:%2" : "=v"(yr[c]) : "v"(pr), "i"(((g & 1) * 8 + c) * 2 * K1_PT) : "memory"); };
        auto yout = [&](int g, int op) AI {
            if (op < 2) { OPAQ(yr[2 * op]); OPAQ(yr[2 * op + 1]); OPAQ(yr[2 * op + 4]); OPAQ(yr[2 * op + 5]); yr[2 * op] += yr[2 * op + 4]; OPAQ(yr[2 * op]); yr[2 * op + 1] += yr[2 * op + 5]; OPAQ(yr[2 * op + 1]); }
            else bs8<P_YA>(u32x2{pk2(yr[0], yr[1]), pk2(yr[2], yr[3])}, wr, yo, O_YB + g * 16 * 36864 + cw * 256);
        };
        auto lwait = [&](int n) AI { asm volatile("s_waitcnt lgkmcnt(%0)" :: "i"(n) : "memory"); };
        constexpr int PX[4] = P_PRX;
        if (PX[0] && hf) __builtin_amdgcn_s_setprio(1);
        lwait(P_PB);
        asm volatile("s_barrier" ::: "memory");
        CT(11);
#pragma unroll
        for (int i = 0; i < 8; ++i) bread(0, i);
        sfor<0, 144>([&](auto J) AI {
            constexpr int j = J, g = c_st(j), o = j - CF[g], last = CF[g + 1] - j <= 4, c1 = o >> 3, c0 = last ? (o & 3) : (o & 7) >> 1, bb = last ? 0 : (o & 1), t = 2 * g + bb + c1;
            if constexpr (j % 4 == 0) CT(12 + j / 4);
            if constexpr (CTB.wt[j] < 15) lwait(CTB.wt[j]);
            if constexpr (g == 0 && bb == 0) { OPAQ(A[(c1 * 4 + c0) * 2]); OPAQ(A[(c1 * 4 + c0) * 2 + 1]); }
            if constexpr (c1 == 0) OPAQ(B[g & 1][2 * c0 + bb]);
            const u32x2 x = A[(c1 * 4 + c0) * 2], y = A[(c1 * 4 + c0) * 2 + 1];
            acc[t] = mfma16(asb(u32x4{x[0], x[1], y[0], y[1]}), asb(B[g & 1][2 * c0 + bb]), (g == 0 && c0 == 0 && (bb || !c1)) ? f32x4{0, 0, 0, 0} : acc[t]);
            SB();
            constexpr int fh = j >= KS[2] ? 2 : j >= KS[1] ? 1 : 0;
            if constexpr (j >= KS[fh] && j < KS[fh] + 22) fir(std::integral_constant<int, fh + 1>{}, std::integral_constant<int, j - KS[fh]>{});
#pragma unroll
            for (int h = 0; h < 3; ++h) {
                if (j >= VS[h] && j < VS[h] + 4) kv(h + 1, j - VS[h]);
                if (h < 2 && j > c_bs(h) && j <= c_bs(h) + 8) bread(h + 1, CTB.b[j]);
                if (h < 2 && j > c_bs(h) + 8 && j <= c_bs(h) + 12) { yread(h, 2 * CTB.y[j]); yread(h, 2 * CTB.y[j] + 1); }
                if (h < 2 && j >= c_bs(h) + 17 && j < c_bs(h) + 20) yout(h, j - c_bs(h) - 17);
            }
            SB();
            if constexpr (j == c_ps(g)) { ldsw<f32x4>(pw + (g & 1) * 16 * K1_PT, acc[2 * g]); ldsw<f32x4>(pw + (g & 1) * 16 * K1_PT + K1_PT, acc[2 * g + 1]); SB(); }
            if constexpr (CTB.a[j] >= 0) aread(CTB.a[j]);
            strips(j);
            if constexpr (j == P_CW4) { pw4 = bl4<0>(mkrs(w4 + ((xcd & 3) * 2 + (xcd >> 2)) * 663552, 663552), lane < P_CL ? ((loc * 8 + w) * P_CL + lane) * 128 : 0x80000000u, 0); SB(); }
            if constexpr (j == PX[0] || j == c_bs(0) + PX[1] || j == c_bs(1) + PX[2] || j == c_bs(2) + PX[3]) __builtin_amdgcn_s_setprio(0);
#pragma unroll
            for (int h = 0; h < 3; ++h) if (j == c_bs(h)) {
                if (PX[h + 1] && hf) __builtin_amdgcn_s_setprio(1);
                lwait(CTB.bw[h]); asm volatile("s_barrier" ::: "memory");
                if (h == 2) {
#pragma unroll
                    for (int i = 0; i < 8; ++i) bread(3, i);
#pragma unroll
                    for (int i = 0; i < 8; ++i) yread(2, i);
                }
                SB();
            }
        });
        CT(52);
        lwait(2);
        yout(2, 0); yout(2, 1); yout(2, 2);
        SB();
        bar();
        CT(53);
#pragma unroll
        for (int i = 0; i < 8; ++i) yread(3, i);
        lwait(0);
        yout(3, 0); yout(3, 1); yout(3, 2);
        asm volatile("" :: "v"(pw4));
        CT(54);
        CDUMP();
    }
    STAMP(0, 7);
}

constexpr int SLOT = 18432, NSL = 6, L_EX = NSL * SLOT, L_F = L_EX, L_ST = L_EX + 36864, L_MN = L_ST + 2048, K2_LDS = L_MN + 1024, L_PE = 5 * SLOT;
#define P_FS {4, 4, 4, 4}
constexpr int FS[4] = P_FS;
constexpr unsigned F_B = O_FLAG, F_C = O_FLAG + 66 * 512, F_D = F_C + 16 * 2048, F_E = F_D + 16 * 2048;
#define P_RD 4
#define P_B0 0
#define P_WBS {0, 2, 5, 8, 11, 14}
#define P_KB 8
#define P_KC 3
#define P_W2A 6
#define P_W2B 6
#define P_W2C 6
#define P_PRA 3
#define P_PRB 4
#define P_X0 10
#define P_PRIO 2
constexpr int RD = P_RD, BP = RD + 1, HD = 8;
constexpr int SPIN = 1 << 17;

template <class HK> DEVI void gemm_small(const u32x4* W, unsigned fa, f32x4* acc, HK&& hook) {
    constexpr int PF = 3;
    u32x4 b[PF + 1][2];
    sfor<0, 18 + PF>([&](auto K) AI {
        constexpr int k = K, m = k - PF;
        if constexpr (m >= 0) {
            const f32x4 z = {0, 0, 0, 0};
            acc[0] = mfma16(asb(W[m]), asb(b[m % (PF + 1)][0]), m ? acc[0] : z); acc[1] = mfma16(asb(W[m]), asb(b[m % (PF + 1)][1]), m ? acc[1] : z);
            SB();
        }
        if constexpr (k < 18) { b[k % (PF + 1)][0] = ldsr<u32x4>(fa + k * 2048); b[k % (PF + 1)][1] = ldsr<u32x4>(fa + k * 2048 + 1024); SB(); }
        if constexpr (m >= 0) { hook(std::integral_constant<int, m>{}); SB(); }
    });
}
DEVI void poll(rsrc_t wr, unsigned off, unsigned need) {
#pragma unroll 1
    for (int i = 0; i < SPIN; ++i) {
        const unsigned v = bl4<16>(wr, off, 0);
        if (__builtin_amdgcn_ballot_w64(v < need) == 0) break;
        asm volatile("s_sleep 1" ::: "memory");
    }
    asm volatile("" ::: "memory");
}
constexpr int e_mem(int j) { return j < 4 ? 0 : j < 13 ? 1 + (j - 4) / 3 : j < 16 ? 0 : j < 25 ? 1 + (j - 16) / 3 : j < 27 ? 0 : j < 33 ? 1 + (j - 27) / 2 : j - 32; }
constexpr int e_til(int j) { return j < 4 ? j : j < 13 ? (j - 4) % 3 : j < 16 ? j - 9 : j < 25 ? 3 + (j - 16) % 3 : j < 27 ? j - 18 : j < 33 ? 6 + (j - 27) % 2 : 8; }
constexpr int e_first(int j) { return 9 * j; }
#define P_FB {12, 18, 24, 28}
#define P_QB {17, 23, 29, 32}
#define P_SD 2
#define P_TP 1
constexpr int FB[4] = P_FB, QB[4] = P_QB, LH[4] = {5, 11, 15, 17}, LJ[5] = {4, 16, 27, 33, 99};
constexpr int f_lvl(int u) { for (int l = 0; l < 4; ++l) if (FB[l] == u) return l + 1; return 0; }
constexpr int q_lvl(int u) { for (int l = 0; l < 4; ++l) if (QB[l] == u) return l + 1; return 0; }
constexpr signed char ACT_D[18][4] = {{0,1,8,-1},{16,9,24,-1},{32,17,40,-1},{25,2,48,10},{33,18,41,-1},{26,3,49,11},{34,19,42,-1},{27,4,50,12},{35,20,43,-1},{28,5,51,13},
                                      {36,21,44,-1},{29,6,52,14},{37,22,45,-1},{30,7,53,15},{38,23,46,-1},{31,54,56,-1},{39,57,47,-1},{55,58,59,64}};
constexpr int NE = 33;
constexpr signed char ACT_E[NE][2] = {{0,8},{16,1},{24,9},{32,17},{40,2},{25,48},{33,10},{41,18},{26,49},{56,34},{42,3},{50,11},{19,4},{27,12},{35,20},{43,5},{28,51},{57,36},{44,13},{52,21},{29,6},{37,14},{45,22},{30,53},{58,38},{46,7},{54,15},{23,-1},{31,-1},{39,-1},{47,-1},{55,59},{64,-1}};
constexpr int b_slot(int u) { return u < 18 ? 18 * u + 1 : 324 + e_first(2 * (u - 18)) + 1; }
constexpr int mx(int a, int b) { return a > b ? a : b; }
constexpr int h_pos(int j, int mt) {
    int p = j < 8 ? 290 + 4 * j + 2 * mt : 324 + e_first(j - 7) + 2 + 2 * mt;
    if (e_mem(j) == 0) { if (j == 26) p = mx(p, b_slot(FB[3]) + 1 + 2 * mt); return p; }
    for (int l = 0; l < 4; ++l) if (j >= LJ[l] && j < LJ[l + 1]) p = mx(p, b_slot(QB[l]) + 1 + 2 * (j - LJ[l]) + mt);
    return p;
}
constexpr int s_pos(int h) { return h < 14 ? 90 + 18 * h : 325 + NE * (h - 13) - 1; }
constexpr int m_wait(int h, int u) {
    int n = 0;
    for (int i = h + 1; i < 18; ++i) n += s_pos(i) < b_slot(u);
    for (int j = 0; j < 36; ++j) for (int mt = 0; mt < 2; ++mt) n += h_pos(j, mt) >= s_pos(h) && h_pos(j, mt) < b_slot(u);
    return n;
}
constexpr int x_at(int s) { return (!(s & 1) && P_X0 + s / 2 < 36) ? P_X0 + s / 2 : -1; }
#define P_NB 4
#define P_ED 2
struct HTab { int at[2][648], st[330], cc[330], fi[37], fu[36], dn[660], vm[648]; bool ok;
    constexpr HTab() : at{}, st{}, cc{}, fi{}, fu{}, dn{}, vm{}, ok(true) {
        int cnt[649] = {}, pre[649] = {}, done[649] = {};
        for (int s = 0; s < 648; ++s) { at[0][s] = at[1][s] = -1; vm[s] = 63; }
        for (int j = 0; j < 37; ++j) fi[j] = e_first(j);
        for (int e = 0, j = 0; e < 330; ++e) { while (j < 35 && fi[j + 1] <= e) ++j; st[e] = j; cc[e] = e - fi[j]; }
        {
            int e = fi[36 - P_NB];
            for (int c = 0; c < 9; ++c) for (int j = 36 - P_NB; j < 36; ++j) { st[e] = j; cc[e++] = (c + 8) % 9; }
            if (e != 324) ok = false;
        }
        for (int e = 323; e >= 0; --e) fu[st[e]] = e;
        for (int s = 0; s < 660; ++s) dn[s] = (s >= 324 && s < 648 && st[s - 324] == 35) ? cc[s - 324] : -1;
        for (int h = 0; h < 18; ++h) ++cnt[s_pos(h)];
        for (int l = 0; l < 72; ++l) { const int p = h_pos(l >> 1, l & 1); ++cnt[p]; if (at[0][p] < 0) at[0][p] = l; else if (at[1][p] < 0) at[1][p] = l; else ok = false; if (p >= 323 + fu[l >> 1]) ok = false; }
        for (int s = 0; s < 648; ++s) pre[s + 1] = pre[s] + cnt[s];
        for (int j = 0; j < 36; ++j) {
            const int p = h_pos(j, 1), s = 323 + fu[j];
            if (p < h_pos(j, 0)) ok = false;
            const int n = pre[s] - pre[p + 1] + (at[0][p] == 2 * j + 1 && at[1][p] >= 0 ? 1 : 0);
            if (n >= 63) ok = false;
            vm[s] = n; done[s] = pre[s] - n;
        }
        for (int l = 0; l < 4; ++l) { const int s = b_slot(FB[l]); done[s] = pre[s] - m_wait(LH[l], FB[l]); if (s_pos(LH[l]) >= s || FB[l] + P_SD >= QB[l]) ok = false; }
        for (int s = 1; s < 648; ++s) done[s] = mx(done[s], done[s - 1]);
        for (int j = 0; j < 36; ++j) if (e_mem(j) == 0 && j >= 4 && done[h_pos(j, 0)] <= pre[s_pos(2 * e_til(j) + 1)]) ok = false;
        for (int k = 1; k < 36; ++k) { int n = P_X0; for (int s = 0; s < 2 * k - 1; ++s) n += x_at(s) >= 0; vm[2 * k - 1] = n - 1 - k; if (n <= k) ok = false; }
        vm[289] = pre[289] - pre[s_pos(7)] - 1;
        for (int e = 0; e < 324; ++e) if (cc[e] < 8 && e < fi[(st[e] & ~1) - (st[e] >= 2 ? 2 : 0)] + (st[e] >= 2 ? BP : 0)) ok = false;
    } };
constexpr int e_bar(int er) { for (int k = 0; k < 18; ++k) if (er == e_first(2 * k) + BP) return 18 + k; return -1; }
constexpr HTab HT{};
static_assert(HT.ok, "schedule");
constexpr int wcnt(int vm, int lgkm) { return (vm & 15) | ((vm >> 4) << 14) | 0x70 | (lgkm << 8); }
constexpr int g_nd(int i) { return i + 5 < 18 ? 5 : i + 5 < 36 ? 4 : 0; }
#define P_XI {3, 5, 7, 9, 15, 19, 20, 24, 26}
constexpr int XI[9] = P_XI;
constexpr int g_xt(int i) { for (int t = 0; t < 9; ++t) if (XI[t] == i) return t; return -1; }
constexpr int g_nx(int i) { return g_xt(i) >= 0; }
constexpr bool x_ok() { for (int j = 0; j < 36; ++j) if (XI[e_til(j)] + 5 > 18 + (j < 32 ? j : 30) / 2) return false; return true; }
static_assert(x_ok(), "extra tiles");
constexpr int g_pre(int i) { return q_lvl(i + P_SD) ? 1 : 0; }
#define P_PF 8
constexpr int g_pf(int i) { return P_PF == 0 ? -1 : (i >= P_PF && i <= P_PF + 12 && !((i - P_PF) & 1)) ? (i - P_PF) / 2 : i == FB[2] ? 7 : i == FB[3] ? 8 : -1; }
#define P_PI {99, 99, 99, 99, 99, 99, 99, 99, 99, 99}
#define P_Q {99, 99, 99}
#define P_LA 8
constexpr int PI[10] = P_PI;
constexpr int g_np(int i) { int n = 0; for (int s = P_KPF; s < 10; ++s) n += PI[s] == i; return n; }
constexpr int g_all(int i) { return g_pre(i) + g_nd(i) + g_nx(i) + (g_pf(i) >= 0) + g_np(i); }
constexpr int g_wait(int u) {
    int n = g_nx(u - 4) + (g_pf(u - 4) >= 0) + g_np(u - 4) + g_all(u - 3) + g_all(u - 2) + g_all(u - 1);
    if (q_lvl(u)) { n = -1; for (int i = 1; i <= P_SD; ++i) n += g_all(u - i); }
    return n;
}

__global__ __launch_bounds__(512, 2) void k2(const char* __restrict__ w3, const char* __restrict__ w4, char* __restrict__ ws, char* __restrict__ out) {
    extern __shared__ __attribute__((aligned(256))) char smem[];
    const unsigned lb = (unsigned)(uintptr_t)(LAS char*)smem;
    const int wg = blockIdx.x, xcd = wg & 7, loc = wg >> 3, lane = threadIdx.x & 63;
    const int kap = xcd & 3, rt = 8 * (xcd >> 2) + (loc >> 2), nt = loc & 3, rb = 4 * rt + kap, fc = 4 * kap + nt;
    const int w = __builtin_amdgcn_readfirstlane(threadIdx.x >> 6), wm = w & 3;
    const int lj = lane & 15, lq = lane >> 4, hh = lane >> 5;
    const unsigned l16 = lane * 16;
    const rsrc_t wr = mkrs(ws, O_END);
    BDECL; BT(0);
    STAMP(1, 0);
    const int a9 = 9 * (fc >> 1), odd = fc & 1;
    const char* wp = w3 + (a9 + 5 * odd) * 128 + (size_t)wm * 36 * 9216;
    const char* wsg = w3 + (a9 + 4) * 128 + odd * 64 + (size_t)wm * 72 * 9216;
    const unsigned pvo = (lane >> 3) * 9216 + (((lane & 7) ^ (((lane >> 4) & 1) << 2)) * 16), svo = (lane >> 2) * 9216 + (lane & 3) * 16;
    auto dmaD = [&](int u, int i) AI {
        const unsigned dst = lb + (u % NSL) * SLOT + wm * 4608 + i * 1024;
        const char* src = u < 16 ? wp + (u >> 2) * 128 + (size_t)((u & 3) * 144 + i * 8) * 9216 : wsg + (size_t)((u & 1) * 288 + i * 16) * 9216;
        if (i < 4) dma16(dst, src, u < 16 ? pvo : svo); else dma16h(dst, src, u < 16 ? pvo : svo);
    };

    {
    constexpr unsigned OOB = 0x80000000u;
    u32x4 Y[5], X[5], W1[21], W2[24];
    int nu[5];
#pragma unroll
    for (int m = 0; m < 4; ++m) nu[m] = ((nt + m) & 3) * 8 + w;
    nu[4] = 32 + (w >> 1);
    const unsigned h16 = l16 + (w & 1) * 512, h16v = lane < 32 ? h16 : OOB, e16 = w < 2 ? l16 : OOB;
    BT(1);
    const u32x2 xs0 = bl8<0>(wr, lj * 8, O_XS + rb * 256), xs1 = bl8<0>(wr, lj * 8, O_XS + rb * 256 + 128);
#pragma unroll
    for (int m = 0; m < 5; ++m) { Y[m] = bl16<0>(wr, m < 4 ? l16 : h16v, O_YB + (rb * 36 + nu[m]) * 1024); X[m] = bl16<0>(wr, m < 4 ? l16 : h16v, O_XT + (rb * 36 + nu[m]) * 1024); SB(); }
    BT(2);
    if (P_B0) asm volatile("s_barrier" ::: "memory");
    BT(3);
    auto ldw = [&](u32x4* W, unsigned base, int k) AI {
        if (k < 0) W[k] = bl16<0>(wr, (lj < 2 && (k < -1 || w < 2)) ? lq * 32 + lj * 16 : OOB, O_W2M + (w + 8 * (k + 3)) * 128);
        else if (k < 3) W[k] = bl16<0>(wr, k < 2 ? l16 : e16, base + ((32 + nt) * 18 + w + 8 * k) * 1024);
        else W[k] = bl16<0>(wr, l16, base + ((8 * nt + w) * 18 + k - 3) * 1024);
        SB();
    };
    auto ldw2 = [&](int k) AI { ldw(W2 + 3, O_W2T, k - 3); };
    auto prime = [&](int i) AI { dmaD(2 * (i / 5) + (w >> 2), i % 5); SB(); };
    unsigned pfq[3] = {0, 0, 0};
    auto wpf = [&](int p) AI {
        constexpr int Q[3] = P_Q, N3 = 9 - 2 * P_KPF, L3 = (N3 * 576 + 255) / 256, LA = P_LA, LB = 21 - LA;
        const unsigned g = loc * 8 + w, l = g * L3 + lane, row = l / N3, c = l - row * N3 + P_KPF;
        if (Q[0] == p) pfq[0] = bl4<0>(mkrs(w3, 576 * 9216), lane < L3 ? row * 9216 + (c < 5 ? c : c + P_KPF) * 128 : OOB, kap * 2304 + (xcd >> 2) * 1152);
        if (Q[1] == p) pfq[1] = bl4<0>(mkrs(w4 + (kap * 2 + (xcd >> 2)) * 663552, 663552), lane < LA ? (g * 21 + lane) * 128 : OOB, 0);
        if (Q[2] == p && LB) pfq[2] = bl4<0>(mkrs(w4 + (kap * 2 + (xcd >> 2)) * 663552, 663552), lane < LB ? (g * 21 + LA + lane) * 128 : OOB, 0);
        SB();
    };
    auto wpd = [&](int a, int b) AI { constexpr int Q[3] = P_Q;
#pragma unroll
        for (int i = 0; i < 3; ++i) if (Q[i] >= a && Q[i] < b) asm volatile("" :: "v"(pfq[i])); };
    f32x2 s[2] = {{0, 0}, {0, 0}}, sq[2] = {{0, 0}, {0, 0}};
    const float rx[2] = {__uint_as_float(xs0[0]), __uint_as_float(xs1[0])}, mx[2] = {__uint_as_float(xs0[1]), __uint_as_float(xs1[1])};
    u32x4 hown, ca, cb, cea, ceb;
    constexpr int WBS[6] = P_WBS;
#pragma unroll
    for (int m = 0; m < 5; ++m) {
#pragma unroll
        for (int k = m ? WBS[m - 1] : 0; k < WBS[m]; ++k) ldw(W1, O_W1T, k);
        BT(4 + 2 * m);
        u32x4 hp;
#pragma unroll
        for (int i = 0; i < 4; ++i) {
            const int mt = i >> 1;
            const f32x2 x = {bflo(X[m][i]), bfhi(X[m][i])}, y = {bflo(Y[m][i]), bfhi(Y[m][i])};
            const f32x2 h = __builtin_elementwise_fma(__builtin_elementwise_fma(x, f32x2{rx[mt], rx[mt]}, f32x2{mx[mt], mx[mt]}), y, x);
            s[mt] += h; sq[mt] = __builtin_elementwise_fma(h, h, sq[mt]);
            hp[i] = pk2(h[0], h[1]);
        }
        OPAQ(s[0]); OPAQ(s[1]); OPAQ(sq[0]); OPAQ(sq[1]);
        const unsigned a = lb + L_F + (nu[m] >> 1) * 2048 + (nu[m] & 1) * 8 + (m < 4 ? l16 : h16);
        if (m < 4 || lane < 32) { ldsw<u32x2>(a, u32x2{hp[0], hp[1]}); ldsw<u32x2>(a + 1024, u32x2{hp[2], hp[3]}); }
        if (m == 0) hown = hp;
        SB();
        BT(5 + 2 * m);
    }
    {
        float s0 = s[0][0] + s[0][1], s1 = s[1][0] + s[1][1], q0 = sq[0][0] + sq[0][1], q1 = sq[1][0] + sq[1][1];
        swap16f(s0, s1); swap16f(q0, q1);
        float t = s0 + s1, u = q0 + q1;
        swap32f(t, u);
        ldsw<float>(lb + L_ST + w * 256 + lane * 4, t + u);
    }
    SB();
    {
        const unsigned co = lj * 64 + lq * 16, co2 = lj < 2 ? co + 1024 : OOB;
        ca = bl16<0>(wr, co, O_CSP + (8 * nt + w) * 1152); cb = bl16<0>(wr, co2, O_CSP + (8 * nt + w) * 1152);
        cea = bl16<0>(wr, w < 2 ? co : OOB, O_CSP + (32 + nt) * 1152); ceb = bl16<0>(wr, w < 2 ? co2 : OOB, O_CSP + (32 + nt) * 1152);
        SB();
    }
#pragma unroll
    for (int k = WBS[4]; k < WBS[5]; ++k) ldw(W1, O_W1T, k);
    bar();
    BT(14);
    f32x4 acc[2];
    unsigned fa = lb + L_F + l16; OPAQ(fa);
    const unsigned fx = fa + w * 2048, fx2 = fa + (16 + (w & 1)) * 2048, pw = lb + L_PE + w * 2048 + l16, pr = lb + L_PE + (w & 1) * 1024 + l16;
    u32x4 xb[3][2];
    auto slices = [&](const u32x4* We) AI {
#pragma unroll
        for (int i = 0; i < 3; ++i)
#pragma unroll
            for (int mt = 0; mt < 2; ++mt) acc[mt] = mfma16(asb(We[i]), asb(xb[i][mt]), i ? acc[mt] : f32x4{0, 0, 0, 0});
    };
    auto xread = [&]() AI {
#pragma unroll
        for (int mt = 0; mt < 2; ++mt) { xb[0][mt] = ldsr<u32x4>(fx + mt * 1024); xb[1][mt] = ldsr<u32x4>(fx + 16384 + mt * 1024); xb[2][mt] = ldsr<u32x4>(fx2 + mt * 1024); }
    };
    auto psum = [&]() AI { f32x4 t[8];
#pragma unroll
        for (int i = 0; i < 8; ++i) t[i] = ldsr<f32x4>(pr + i * 2048);
        return ((t[0] + t[1]) + (t[2] + t[3])) + ((t[4] + t[5]) + (t[6] + t[7])); };
    auto gelu4 = [&](f32x4 z) AI {
        const f32x4 u = z * __builtin_elementwise_fma(z * z, f32x4{GC1, GC1, GC1, GC1}, f32x4{GC0, GC0, GC0, GC0});
        f32x4 e;
#pragma unroll
        for (int r = 0; r < 4; ++r) e[r] = __builtin_amdgcn_exp2f(u[r]);
        e += 1.0f;
#pragma unroll
        for (int r = 0; r < 4; ++r) e[r] = __builtin_amdgcn_rcpf(e[r]);
        return z * e; };
    xread();
    float tv[8];
#pragma unroll
    for (int i = 0; i < 8; ++i) tv[i] = ldsr<float>(lb + L_ST + i * 256 + lane * 4);
    const u32x2 hext = ldsr<u32x2>(fa + (w & 1) * 1024 + ((32 + nt) >> 1) * 2048 + (nt & 1) * 8);
    SB();
    BT(15);
    slices(W1);
    ldsw<f32x4>(pw, acc[0]); ldsw<f32x4>(pw + 1024, acc[1]);
    SB();
    BT(16);
    float mm[2], rr[2], r1[2];
    f32x4 cz[2];
    gemm_small(W1 + 3, fa, acc, [&](auto K) AI {
        constexpr int k = K;
        if constexpr (k + WBS[5] < 21) ldw(W1, O_W1T, k + WBS[5]);
        if constexpr (k == 0) {
            float su = ((tv[0] + tv[1]) + (tv[2] + tv[3])) + ((tv[4] + tv[5]) + (tv[6] + tv[7])), qu = su; swap32f(su, qu);
            const float mean = su * RD576, var = fmaxf(qu * RD576 - mean * mean, 0.f);
            const float r1s = __builtin_amdgcn_rsqf(var + EPS), r12 = r1s * __builtin_amdgcn_rsqf(var * r1s * r1s + EPS);
            mm[0] = mm[1] = mean; rr[0] = rr[1] = r12; r1[0] = r1[1] = r1s;
            swap16f(mm[0], mm[1]); swap16f(rr[0], rr[1]); swap16f(r1[0], r1[1]);
            OPAQ(mm[0]); OPAQ(rr[0]); OPAQ(r1[0]);
        }
        if constexpr (k == P_KB) asm volatile("s_barrier" ::: "memory");
        if constexpr (k >= 4 && k < 8) {
            constexpr int r = k - 4;
            const float c = rowsum16(__uint_as_float(ca[r]) + __uint_as_float(cb[r]));
            cz[0][r] = -mm[0] * rr[0] * c; cz[1][r] = -mm[1] * rr[1] * c; OPAQ(cz[0][r]); OPAQ(cz[1][r]);
        }
        if constexpr (k == P_KB + 1) {
            if (w < 2) {
                const f32x4 ae = psum();
                const float me = w ? mm[1] : mm[0], re = w ? rr[1] : rr[0];
                f32x4 z;
#pragma unroll
                for (int r = 0; r < 4; ++r) z[r] = (ae[r] - me * rowsum16(__uint_as_float(cea[r]) + __uint_as_float(ceb[r]))) * re;
                const f32x4 g = gelu4(z);
                unsigned a0 = pk2(g[0], g[1]), a1 = pk2(g[2], g[3]), b0 = a0, b1 = a1;
                swap16(a0, b0); swap16(a1, b1);
                if (!(lq & 1)) bs16<XA>(u32x4{a0, a1, b0, b1}, wr, lj * 32 + hh * 16, O_GB + (32 + nt) * GTS + (4 + 32 * rb + 16 * w) * 32);
            }
        }
        wpf(k);
        if constexpr (k % 3 == 0) BT(17 + k / 3);
        if constexpr (k >= 18 - P_W2A) ldw2(k - (18 - P_W2A));
    });
    BT(23);
    {
        unsigned o[2][2];
#pragma unroll
        for (int mt = 0; mt < 2; ++mt) {
            const f32x4 g = gelu4(__builtin_elementwise_fma(acc[mt], f32x4{rr[mt], rr[mt], rr[mt], rr[mt]}, cz[mt]));
            o[mt][0] = pk2(g[0], g[1]); o[mt][1] = pk2(g[2], g[3]);
        }
        swap16(o[0][0], o[1][0]); swap16(o[0][1], o[1][1]);
        bs16<XA>(u32x4{o[0][0], o[0][1], o[1][0], o[1][1]}, wr, (lane & 31) * 32 + hh * 16, O_GB + (8 * nt + w) * GTS + (4 + 32 * rb) * 32);
    }
    SB();
    BT(24);
#pragma unroll
    for (int k = P_W2A; k < P_W2A + P_W2B; ++k) ldw2(k);
    BT(25);
    waitvm<P_W2B>();
    BT(26);
    bar();
    BT(27);
    if (w == 0 && lane == 0) bs4<16>(1u, wr, 0, F_B + (rb + 1) * 512 + nt * FS[0]);
    wpd(0, 18);
    STAMP(1, 3);
#pragma unroll
    for (int k = P_W2A + P_W2B; k < P_W2A + P_W2B + P_W2C; ++k) ldw2(k);
    BT(28);
    {
        const int pr = rb + (lane >> 2);
        poll(wr, F_B + (lane < 12 ? pr * 512 + (lane & 3) * FS[0] : 0), (lane < 12 && pr >= 1 && pr <= 64) ? 1u : 0u);
    }
    BT(29);
    u32x4 G[5];
    auto gaddr = [&](int n) AI { const int gr = n / 9, sh = gr == 0 ? 1 : gr == 1 ? -1 : gr == 2 ? 2 : -2; return O_GB + n * GTS + (4 + 32 * rb - sh) * 32; };
#pragma unroll
    for (int m = 0; m < 5; ++m) { G[m] = bl16<XA>(wr, m < 4 ? l16 : h16v, gaddr(m < 4 ? 8 * m + w : 32 + (w >> 1))); SB(); }
    const unsigned gw = lb + L_F + (((lane >> 1) & 15) + 16 * (lane & 1)) * 16;
#pragma unroll
    for (int m = 0; m < 4; ++m) ldsw<u32x4>(gw + (w >> 1) * 2048 + hh * 1024 + (w & 1) * 512 + m * 8192, G[m]);
    if (lane < 32) ldsw<u32x4>(gw + (16 + (w >> 2)) * 2048 + (w & 1) * 1024 + ((w >> 1) & 1) * 512, G[4]);
    SB();
#pragma unroll
    for (int k = P_W2A + P_W2B + P_W2C; k < 24; ++k) ldw2(k);
    wpf(20);
    BT(30);
    bar();
    BT(31);
    xread();
    SB();
    slices(W2 + 3);
    ldsw<f32x4>(pw, acc[0]); ldsw<f32x4>(pw + 1024, acc[1]);
    slices(W2);
    {
        float a = acc[0][0] + acc[0][1], b = acc[1][0] + acc[1][1];
        swap16f(a, b);
        if (lane < 32) ldsw<float>(lb + L_MN + w * 128 + lane * 4, a);
    }
    SB();
    BT(32);
    float mn[2];
    f32x4 hr[2], rese;
    auto quad = [&](float t) AI { return u32x4{__float_as_uint(t), __float_as_uint(dppf<0x55>(t)), __float_as_uint(dppf<0xAA>(t)), __float_as_uint(dppf<0xFF>(t))}; };
    gemm_small(W2 + 6, fa, acc, [&](auto K) AI {
        constexpr int k = K;
        if constexpr (k == P_KC) {
            asm volatile("s_barrier" ::: "memory");
#pragma unroll
            for (int i = 0; i < 4; ++i) tv[i] = ldsr<float>(lb + L_MN + i * 256 + lane * 4);
        }
        if constexpr (k == P_KC + 2) {
            float t = (tv[0] + tv[1]) + (tv[2] + tv[3]), u = t; swap32f(t, u);
            mn[0] = mn[1] = t + u; swap16f(mn[0], mn[1]); OPAQ(mn[0]);
        }
        if constexpr (k == P_KC + 3) {
            if (w < 2) {
                const f32x4 ae = psum();
                const float h[4] = {bflo(hext[0]), bfhi(hext[0]), bflo(hext[1]), bfhi(hext[1])};
                const float re = w ? r1[1] : r1[0], me = w ? mm[1] : mm[0], ne = w ? mn[1] : mn[0];
                float xc[4];
#pragma unroll
                for (int r = 0; r < 4; ++r) { const float x1 = __builtin_fmaf(h[r] - me, re, ae[r]); xc[r] = x1 - ne; rese[r] = h[r] + x1; }
                unsigned a0 = pk2(xc[0], xc[1]), a1 = pk2(xc[2], xc[3]), b0 = a0, b1 = a1;
                swap16(a0, b0); swap16(a1, b1);
                if (!(lq & 1)) bs16<XA>(u32x4{a0, a1, b0, b1}, wr, hh * 512 + lj * 16, O_XB + (rb * 36 + 32 + nt) * 1024 + w * 256);
                float t = (xc[0] * xc[0] + xc[1] * xc[1]) + (xc[2] * xc[2] + xc[3] * xc[3]), u = t;
                swap16f(t, u); t += u; u = t; swap32f(t, u);
                const u32x4 qv = quad(t + u);
                if (lane < 16 && !(lane & 3)) bs16<XA>(qv, wr, lane * 4, O_SQ + (rb * 36 + 32 + nt) * 128 + 64 * w);
                bs16<0>(__builtin_bit_cast(u32x4, rese), wr, l16, O_R + ((rb * 36 + 32 + nt) * 2 + w) * 1024);
            }
        }
        if constexpr (k >= P_KC + 4 && k < P_KC + 8) {
            constexpr int i = k - (P_KC + 4), mt = i >> 1, r = 2 * (i & 1);
            const float h0 = bflo(hown[i]), h1 = bfhi(hown[i]);
            cz[mt][r] = __builtin_fmaf(h0 - mm[mt], r1[mt], -mn[mt]); cz[mt][r + 1] = __builtin_fmaf(h1 - mm[mt], r1[mt], -mn[mt]);
            hr[mt][r] = h0 + mn[mt]; hr[mt][r + 1] = h1 + mn[mt];
            OPAQ(cz[mt][r]); OPAQ(cz[mt][r + 1]); OPAQ(hr[mt][r]); OPAQ(hr[mt][r + 1]);
        }
        wpf(21 + k);
        if constexpr (k >= 18 - P_PRA) prime(k - (18 - P_PRA));
    });
    BT(33);
    {
        unsigned o[2][2]; float q2[2]; f32x4 xc[2];
#pragma unroll
        for (int mt = 0; mt < 2; ++mt) { xc[mt] = acc[mt] + cz[mt]; o[mt][0] = pk2(xc[mt][0], xc[mt][1]); o[mt][1] = pk2(xc[mt][2], xc[mt][3]); }
        swap16(o[0][0], o[1][0]); swap16(o[0][1], o[1][1]);
        bs16<XA>(u32x4{o[0][0], o[0][1], o[1][0], o[1][1]}, wr, l16, O_XB + (rb * 36 + 8 * nt + w) * 1024);
        SB();
#pragma unroll
        for (int mt = 0; mt < 2; ++mt) { const f32x4 t = xc[mt] * xc[mt]; q2[mt] = (t[0] + t[1]) + (t[2] + t[3]); }
        {
            swap16f(q2[0], q2[1]);
            float t = q2[0] + q2[1], u = t; swap32f(t, u);
            const u32x4 qv = quad(t + u);
            if (lane < 32 && !(lane & 3)) bs16<XA>(qv, wr, lane * 4, O_SQ + (rb * 36 + 8 * nt + w) * 128);
        }
        SB();
        BT(34);
#pragma unroll
        for (int i = P_PRA; i < P_PRA + P_PRB; ++i) prime(i);
        BT(35);
        waitvm<P_PRB>();
        BT(36);
        bar();
        BT(37);
        if (w == 0 && lane == 0) bs4<16>(1u, wr, 0, F_C + rt * 2048 + (kap * 4 + nt) * FS[1]);
        wpd(18, 99);
#pragma unroll
        for (int i = P_PRA + P_PRB; i < 10; ++i) prime(i);
#pragma unroll
        for (int mt = 0; mt < 2; ++mt) bs16<0>(__builtin_bit_cast(u32x4, xc[mt] + hr[mt]), wr, l16, O_R + ((rb * 36 + 8 * nt + w) * 2 + mt) * 1024);
    }
    }
    BT(38);
    BDUMP(1);
    STAMP(1, 7);

    unsigned kb[4], kq[4];
#pragma unroll
    for (int i = 0; i < 4; ++i) { const int m = (nt + i) & 3; kb[i] = 36 * kap + 18 * (m >> 1) + 10 * (m & 1); kq[i] = 36 * kap + 18 * (m >> 1) + 8 + (m & 1); }
    const unsigned kpa = lb + L_ST + wm * 512 + (lane & 31) * 16;
    u32x4 R[2]; u32x2 Re;
    auto ldR = [&]() AI {
#pragma unroll
        for (int i = 0; i < 2; ++i) R[i] = bl16<16>(wr, l16, O_R + ((rb * 36 + 8 * nt + w) * 2 + i) * 1024);
        Re = bl8<16>(wr, hh * 1024 + (lane & 7) * 16 + (lq & 1) * 8, O_R + (rb * 36 + 32 + nt) * 2048 + w * 128);
        SB();
    };
    if (w < 4) {
        poll(wr, F_C + rt * 2048 + (wm * 4 + (lane & 3)) * FS[1], 1u);
        STAMP(2, 0);
        u32x4 X[36];
#pragma unroll
        for (int i = 0; i < P_X0; ++i) X[i] = bl16<XA>(wr, l16, O_XB + ((4 * rt + wm) * 36 + i) * 1024);
        asm volatile("s_barrier" ::: "memory");
        const int r = 8 * hh + (lj >> 2);
        unsigned ta[2][2], ts;
#pragma unroll
        for (int s = 0; s < 2; ++s)
#pragma unroll
            for (int ab = 0; ab < 2; ++ab) { ta[s][ab] = lb + s * 3 * SLOT + r * 128 + ((ab ^ ((r >> 1) & 1)) * 64) + (lq & 1) * 32 + (lane & 3) * 8; OPAQ(ta[s][ab]); }
        ts = lb + 3 * SLOT + r * 64 + (lq & 1) * 32 + (lane & 3) * 8; OPAQ(ts);
        const int re = 16 * (lq >> 1) + 4 * (lq & 1) + (lj >> 2);
        unsigned am[2][8], ax;
        const unsigned amb = lb + re * 256 + ((re & 7) << 5) + (lane & 3) * 8;
        u32x4 H[HD][2];
        const unsigned hvo = l16 + (8 * rt + 2 * wm) * 144 * 1024;
        unsigned hso = (8 * rt + 2 * wm + (lq & 1)) * 144 * 1024 + (lj + 16 * hh) * 16 + O_HB + kb[0] * 1024;
        OPAQ(hso);
        auto ldH = [&](int j, int mt) AI {
            const int t = e_til(j);
            H[j % HD][mt] = bl16<XA>(wr, hvo + mt * 144 * 1024 + (t & 3) * 1024, O_HB + (t < 8 ? kb[e_mem(j)] + (t & 4) : kq[e_mem(j)]) * 1024);
        };
        f32x4 ec[9][2]; auto& ec_ = ec; u32x4 po[9];
        const rsrc_t wo = mkrs(ws, wm == kap ? 0 : O_END);
        auto pout = [&](int c, int ph) AI {
            po[c][2 * ph] = pk2(ec_[c][ph][0], ec_[c][ph][1]); po[c][2 * ph + 1] = pk2(ec_[c][ph][2], ec_[c][ph][3]);
            if (!ph) { OPAQ(po[c][0]); OPAQ(po[c][1]); }
            else {
                __builtin_amdgcn_raw_buffer_store_b128(po[c], wo, l16, O_PB + (((4 * rt + wm) * 36 + (c < 8 ? 8 * nt + c : 32 + nt)) * 4 + kap) * 1024, XA);
                if (wm == kap) ldsw<u32x4>(lb + c * 1024 + l16, po[c]);
            }
            SB();
        };
        __builtin_amdgcn_s_setprio(P_PRIO);
        f32x16 acc[4]; bf16x8 A[RD + 1];
        f32x4 kp; float t[8]; u32x4 hp;
        sfor<0, 19>([&](auto C) AI { sfor<0, 36>([&](auto I) AI {
            constexpr int fr = 36 * C + I, fm = fr - RD;
            if constexpr (fr < 648 + RD) {
            constexpr int em = fm - 324, jm = em >= 0 ? HT.st[em] : 0, cm = em >= 0 ? HT.cc[em] : 0;
            constexpr bool z0 = jm == 0;
            constexpr int gm = fm < 73 ? 0 : fm < 325 ? 1 : fm < 325 + 4 * NE ? 2 : 0;
            constexpr int gh = gm == 1 ? (fm - 73) / 18 : gm == 2 ? 14 + (fm - 325) / NE : 0, go = gm == 1 ? (fm - 73) % 18 : gm == 2 ? (fm - 325) % NE : 0, gt = gh >> 1, gi = gt < 8 ? (gt & 3) : 0;
            constexpr bool formA = gm == 2 && ((ACT_E[go][0] >> 3) == 3 || (ACT_E[go][0] >> 3) == 5);
            auto act = [&](auto K) AI {
                constexpr int op = gm == 1 ? ACT_D[go][K] : (gm == 2 && K < 2) ? ACT_E[go][K & 1] : -1;
                if constexpr (op >= 0) {
                    constexpr int kd = op >> 3, e = op & 7;
                    const float x = acc[gi][8 * (gh & 1) + e];
                    if constexpr (kd == 0) t[e] = x * x;
                    if constexpr (kd == 1) t[e] = __builtin_fmaf(t[e], kp[1], kp[0]);
                    if constexpr (kd == 2 || kd == 6) t[e] = t[e] * x;
                    if constexpr (kd == 3) t[e] = __builtin_amdgcn_exp2f(t[e]);
                    if constexpr (kd == 4) t[e] = __builtin_fmaf(t[e], kp[2], kp[2]);
                    if constexpr (kd == 5) t[e] = __builtin_amdgcn_rcpf(t[e]);
                    if constexpr (kd < 7) OPAQ(t[e]);
                    if constexpr (kd == 7) { hp[e] = pk2(t[2 * e], t[2 * e + 1]); OPAQ(hp[e]); }
                    if constexpr (kd == 8) { static_assert(s_pos(gh) == fm, "store slot"); bs16<XA>(hp, wr, hso, (gt < 8 ? gt * 1024 : (kq[0] - kb[0]) * 1024) + (gh & 1) * 512); }
                    SB();
                }
            };
            using I0 = std::integral_constant<int, 0>; using I1 = std::integral_constant<int, 1>; using I2 = std::integral_constant<int, 2>; using I3 = std::integral_constant<int, 3>;
            auto mf1 = [&]() AI { ec[cm][1] = mfma16(A[(fm + RD + 1) % (RD + 1)], asb(H[jm % HD][1]), z0 ? f32x4{0, 0, 0, 0} : ec[cm][1]); SB(); };
            if constexpr (em >= 0) ec[cm][0] = mfma16(A[fm % (RD + 1)], asb(H[jm % HD][0]), z0 ? f32x4{0, 0, 0, 0} : ec[cm][0]);
            if constexpr (fm >= 0 && fm < 324) {
                constexpr int u = fm / 18, o = fm % 18, ks = u < 16 ? 9 * (u & 3) + (o >> 1) : 18 * (u & 1) + o, ai = u < 16 ? ((u >> 2) & 1) * 2 + (o & 1) : 0;
                f32x16 z = {};
                acc[ai] = __builtin_amdgcn_mfma_f32_32x32x16_bf16(A[fm % (RD + 1)], asb(X[ks]), ks == 0 ? z : acc[ai], 0, 0, 0);
            }
            SB();
            if constexpr (formA) { act(I0{}); act(I1{}); mf1(); }
            s16x4 t0, t1;
            unsigned a = 0, a2 = 0;
            constexpr int er = fr - 324, jr = er >= 0 ? HT.st[er] : 0, cr = er >= 0 ? HT.cc[er] : 0;
            constexpr int bu = fr < 324 ? (fr % 18 == BP ? fr / 18 : -1) : e_bar(er);
            constexpr int nl = fr <= 648 ? 2 * (RD - 2) : 2 * (646 - fm) > 0 ? 2 * (646 - fm) : 0;
            if constexpr (bu >= 0) {
                static_assert(b_slot(bu) == fm, "barrier slot");
                constexpr int vw = f_lvl(bu) ? m_wait(LH[(f_lvl(bu) + 3) & 3], bu) : 63;
                if constexpr (vw < 63) waitvm<vw>();
                asm volatile("s_barrier" ::: "memory");
                SB();
            }
            if constexpr (fm >= 0 && fm < 647) { __builtin_amdgcn_s_waitcnt(wcnt(HT.vm[fm], nl)); SB(); }
            if constexpr (fr < 324) {
                constexpr int u = fr / 18, o = fr % 18, sl = u % NSL;
                if constexpr (u < 16) { a = ta[sl / 3][o & 1] + (sl % 3) * SLOT + (o >> 1) * 2048; a2 = a + 512; }
                else { a = ts + (sl - 3) * SLOT + o * 1024; a2 = a + 256; }
            } else if constexpr (fr < 648) {
                constexpr int sl = (18 + (jr >> 1)) % NSL;
                if constexpr (cr < 8) { a = am[sl / 3][cr & 7] + (sl % 3) * SLOT + (jr & 1) * 8192; a2 = a + 2048; }
                else { a = ax + (e_mem(jr) * 9 + e_til(jr)) * 1024; a2 = a + 256; }
            }
            if constexpr (fr < 648) { t0 = __builtin_amdgcn_ds_read_tr16_b64_v4i16((LAS s16x4*)(uintptr_t)a); SB(); }
            if constexpr (em >= 0 && !formA) { act(I0{}); if constexpr (gm == 1) act(I1{}); mf1(); }
            if constexpr (fr < 648) {
                t1 = __builtin_amdgcn_ds_read_tr16_b64_v4i16((LAS s16x4*)(uintptr_t)a2);
                A[fr % (RD + 1)] = __builtin_bit_cast(bf16x8, __builtin_shufflevector(t0, t1, 0, 1, 2, 3, 4, 5, 6, 7));
                SB();
            }
            if constexpr (bu == 3) { kp = ldsr<f32x4>(kpa); SB(); }
            if constexpr (em < 0) { act(I0{}); act(I1{}); }
            if constexpr (em >= 0 && gm == 2 && !formA) act(I1{});
            act(I2{}); act(I3{});
            if constexpr (fm >= 299 && fm < 315 && (fm & 1)) { constexpr int c = (fm - 299) >> 1; am[0][c] = amb ^ (c << 5); OPAQ(am[0][c]); }
            if constexpr (fm >= 330 && fm < 346 && !(fm & 1)) { constexpr int c = (fm - 330) >> 1; am[1][c] = am[0][c] + 3 * SLOT; OPAQ(am[1][c]); }
            if constexpr (fm == 317) { ax = lb + L_EX + re * 32 + (lane & 3) * 8; OPAQ(ax); }
            if constexpr (fm >= 0 && x_at(fm) >= 0) X[x_at(fm)] = bl16<XA>(wr, l16, O_XB + ((4 * rt + wm) * 36 + x_at(fm)) * 1024);
            constexpr int l0 = fm >= 0 ? HT.at[0][fm] : -1, l1 = fm >= 0 ? HT.at[1][fm] : -1;
            if constexpr (l0 >= 0) ldH(l0 >> 1, l0 & 1);
            if constexpr (l1 >= 0) ldH(l1 >> 1, l1 & 1);
            SB();
            if constexpr (fm == 324 + HT.fu[35]) ldR();
            if constexpr (fm > 600) { if constexpr (HT.dn[fm - P_ED - 1] >= 0) pout(HT.dn[fm - P_ED - 1], 1); if constexpr (HT.dn[fm - P_ED] >= 0) pout(HT.dn[fm - P_ED], 0); }
            }
        }); });
        __builtin_amdgcn_s_setprio(0);
        STAMP(2, 2);
        sfor<648, 649 + P_ED>([&](auto S) AI { constexpr int fm = S; if constexpr (HT.dn[fm - P_ED - 1] >= 0) pout(HT.dn[fm - P_ED - 1], 1); if constexpr (HT.dn[fm - P_ED] >= 0) pout(HT.dn[fm - P_ED], 0); });
        waitvm<0>();
    } else {
#pragma unroll
        for (int i = 0; i < 5; ++i) dmaD(4, i);
        poll(wr, F_C + rt * 2048 + (wm * 4 + (lane & 3)) * FS[1], 1u);
        STAMP(2, 0);
        asm volatile("s_barrier" ::: "memory");
        f32x4 sqv[5];
        {
            const char* sb = ws + O_SQ + (4 * rt + wm) * 4608;
#pragma unroll
            for (int i = 0; i < 5; ++i) asm volatile("global_load_dwordx4 %0, %1, %2 offset:%3 sc1" : "=v"(sqv[i]) : "v"(i < 4 ? l16 : (l16 & 511) + 1024), "s"(sb), "i"(i < 4 ? i * 1024 : 3072) : "memory");
        }
        const int drow = 4 * wm + lq;
        const unsigned dvo = drow * 1152 + ((lj ^ (2 * (drow & 7))) << 4);
        const char* w4m = w4 + nt * 256;
        auto ksE = [&](int j) AI { return e_til(j) < 8 ? kb[e_mem(j)] + e_til(j) : kq[e_mem(j)]; };
        auto dmaE = [&](int u, int i) AI { const int j = 2 * (u - 18) + (i >> 1);
            dma16(lb + (u % NSL) * SLOT + (j & 1) * 8192 + (wm + 4 * (i & 1)) * 1024, w4m + (size_t)(ksE(j) * 32 + 16 * (i & 1)) * 1152, dvo); };
        const unsigned xvo = (lane >> 1) * 1152 + (lane & 1) * 16, xm = (nt + wm) & 3;
        const char* w4x = w4 + 1024 + nt * 32 + (size_t)(36 * kap + 18 * (xm >> 1)) * 32 * 1152;
        auto dmaX = [&](int t) AI { dma16(lb + L_EX + (wm * 9 + t) * 1024, w4x + (size_t)(t < 8 ? 10 * (xm & 1) + t : 8 + (xm & 1)) * 32 * 1152, xvo); };
        const unsigned fdo = F_D + rt * 2048 + kap * 512;
        unsigned pv = 0, pfd = 0;
        const unsigned pfo = (8 * rt + 2 * wm + ((lane >> 3) & 1)) * 144 * 1024 + (lane & 7) * 128;
        TDECL; TS(0);
        sfor<0, 36>([&](auto U) AI {
            constexpr int u = U;
            if constexpr (u >= 3) waitvm<g_wait(u)>();
            if constexpr (u == 3) {
#pragma unroll
                for (int i = 0; i < 5; ++i) OPAQ(sqv[i]);
                f32x4 sq = (sqv[0] + sqv[1]) + (sqv[2] + sqv[3]) + 0.5f * sqv[4];
                float t = 0;
#pragma unroll
                for (int r = 0; r < 4; ++r) {
                    float a = sq[r] + dppf<0x128>(sq[r]), b = a;
                    swap16f(a, b); a += b; b = a; swap32f(a, b); a += b;
                    if (((lane >> 3) & 3) == r) t = a;
                }
                const float sg = sqrtf(t * RD576 + EPS), rstd = 1.0f / sg;
                if (lane < 32) ldsw<f32x4>(lb + L_ST + wm * 512 + ((lane & 7) * 4 + (lane >> 3)) * 16, f32x4{GC0 * rstd, GC1 * rstd * rstd * rstd, sg, 0.f});
            }
            if constexpr (q_lvl(u) != 0) {
                OPAQ(pv);
                TS(72 + q_lvl(u));
                if (__builtin_amdgcn_ballot_w64(pv < (unsigned)q_lvl(u)) != 0) poll(wr, fdo + (lane & 3) * FS[2], q_lvl(u));
            }
            TS(2 * u + 1);
            bar();
            TS(2 * u + 2);
            if constexpr (f_lvl(u) != 0) { if (w == 4 && lane == 0) bs4<16>(f_lvl(u), wr, 0, fdo + nt * FS[2]); }
            if constexpr (g_pre(u)) asm volatile("global_load_dword %0, %1, %2 sc1" : "=v"(pv) : "v"((lane & 3) * FS[2]), "s"(ws + fdo) : "memory");
#pragma unroll
            for (int i = 0; i < g_nd(u); ++i) { if (u + 5 < 18) dmaD(u + 5, i); else dmaE(u + 5, i); SB(); }
            if constexpr (g_nx(u)) dmaX(g_xt(u));
            if constexpr (g_pf(u) >= 0) asm volatile("global_load_dword %0, %1, %2 sc1" : "+v"(pfd) : "v"(pfo), "s"(ws + O_HB + (size_t)(g_pf(u) < 8 ? kb[0] + g_pf(u) : kq[0]) * 1024) : "memory");
#pragma unroll
            for (int s = P_KPF; s < 10; ++s) if (PI[s] == u) {
                const unsigned l = (loc * 4 + wm) * 9 + lane, lo = l < 576, o = lane >= 9 || (!lo && (s == 4 || s == 9)) ? 0x80000000u : s < 5 ? (lo ? l * 9216 : (l - 576) * 9216 + 640) : (lo ? l : l + 2304) * 128;
                asm volatile("buffer_load_dword %0, %1, %2, %3 offen" : "+v"(pfd) : "v"(o), "s"(mkrs(s < 5 ? w3 : w4, 576 * 9216)),
                             "s"(s < 5 ? kap * 2304 + (xcd >> 2) * 1152 + s * 128 : (kap * 2 + (xcd >> 2)) * 5184 * 128 + (s - 5) * 576 * 128) : "memory");
            }
            SB();
        });
        ldR();
        waitvm<0>();
        OPAQ(pfd);
        TDUMP(2, 32);
    }
    bar();
    if (w == 0 && lane == 0) bs4<16>(1u, wr, 0, F_E + rt * 2048 + fc * FS[3]);
    STAMP(2, 3);
    {
        const rsrc_t orr = mkrs(out, 2048 * 1152);
        u32x4 P[4], Q = {0, 0, 0, 0};
        P[3] = ldsr<u32x4>(lb + w * 1024 + l16);
        if (lane >= 56) Q = ldsr<u32x4>(lb + 8192 + w * 128 + (lane & 7) * 16);
        poll(wr, F_E + rt * 2048 + ((lane & 3) * 4 + nt) * FS[3], 1u);
        STAMP(2, 4);
        if (lane < 24) Q = bl16<XA>(wr, ((kap + 1 + (lane >> 3)) & 3) * 1024 + (lane & 7) * 16, O_PB + (rb * 36 + 32 + nt) * 4096 + w * 128);
#pragma unroll
        for (int i = 0; i < 3; ++i) P[i] = bl16<XA>(wr, l16, O_PB + ((rb * 36 + 8 * nt + w) * 4 + ((kap + 1 + i) & 3)) * 1024);
        {
            float a[8];
#pragma unroll
            for (int i = 0; i < 4; ++i) { a[2 * i] = bflo(Q[i]); a[2 * i + 1] = bfhi(Q[i]); }
#pragma unroll
            for (int i = 0; i < 4; ++i) { swap32f(a[i], a[i + 4]); a[i] += a[i + 4]; }
#pragma unroll
            for (int i = 0; i < 2; ++i) { swap16f(a[i], a[i + 2]); a[i] += a[i + 2]; a[i] += dppf<0x128>(a[i]); }
            if (!(lane & 8)) bs4<0>(pk2(a[0] + __uint_as_float(Re[0]), a[1] + __uint_as_float(Re[1])), orr, (16 * hh + (lane & 7)) * 1152 + (lq & 1) * 4,
                                    (32 * rb + 8 * (w & 1)) * 1152 + 1024 + nt * 32 + (w >> 1) * 8);
        }
#pragma unroll
        for (int mt = 0; mt < 2; ++mt) {
            float a[4];
#pragma unroll
            for (int r = 0; r < 4; ++r) a[r] = __uint_as_float(R[mt][r]);
#pragma unroll
            for (int i = 3; i >= 0; --i) { const int k = i == 3 ? 3 : 2 - i; a[0] += bflo(P[k][2 * mt]); a[1] += bfhi(P[k][2 * mt]); a[2] += bflo(P[k][2 * mt + 1]); a[3] += bfhi(P[k][2 * mt + 1]); }
            bs8<0>(u32x2{pk2(a[0], a[1]), pk2(a[2], a[3])}, orr, lj * 1152 + lq * 8, (32 * rb + 16 * mt) * 1152 + (8 * nt + w) * 32);
        }
    }
    STAMP(2, 6);
    STAMP(2, 7);
    asm volatile(".Lcode_hi:");
}

extern "C" void kernel_launch(void* const* d_in, const int* in_sizes, int n_in, void* d_out, int out_size, void* d_ws, size_t ws_size, hipStream_t stream) {
    char* ws = (char*)d_ws;
    (void)hipFuncSetAttribute((const void*)k1, hipFuncAttributeMaxDynamicSharedMemorySize, K1_LDS);
    (void)hipFuncSetAttribute((const void*)k2, hipFuncAttributeMaxDynamicSharedMemorySize, K2_LDS);
    k1<<<256, 512, K1_LDS, stream>>>((const char*)d_in[0], (const char*)d_in[1], (const char*)d_in[2], (const char*)d_in[3], (const char*)d_in[4], (const u16*)d_in[5],
                                     (const char*)d_in[7], (const char*)d_in[8], (const char*)d_in[9], (const char*)d_in[10], ws);
    k2<<<256, 512, K2_LDS, stream>>>((const char*)d_in[9], (const char*)d_in[10], ws, (char*)d_out);
}
```
